```python
import torch

def _softmax(x, axis=-1):
    return torch.softmax(x, dim=axis)
def _gelu(x):
    return torch.nn.functional.gelu(x, approximate="tanh")
def _silu(x):
    return torch.nn.functional.silu(x)
def _sigmoid(x):
    return torch.sigmoid(x)
def _layernorm(x, axis=-1, eps=1e-5):
    m = x.mean(dim=axis, keepdim=True)
    v = x.var(dim=axis, keepdim=True, unbiased=False)
    return (x - m) / torch.sqrt(v + eps)
def _scatter_add(tgt, idx, src, axis=0):
    return tgt.clone().index_add_(axis, idx, src)
def _segment_sum(src, seg, n):
    out = torch.zeros((n,) + src.shape[1:], dtype=src.dtype)
    return out.index_add_(0, seg, src)
def _pad(x, pw):
    flat = tuple(v for pair in reversed(pw) for v in pair)
    return torch.nn.functional.pad(x, flat)

kernel_name = 'concat_heads__spectral_fnet__sliding_local_dilated__prompt'

def setup_inputs():

    _w = (lambda a: (lambda t: t.to(torch.bfloat16) if t.is_floating_point() else t)(torch.as_tensor(a)))
    def _A_setup():
        B, S, D = 1, 2048, 512
        ns = torch.arange(S)[:, None] * torch.arange(S)[None, :]
        nd = torch.arange(D)[:, None] * torch.arange(D)[None, :]
        Ws_re = torch.cos(-2.0 * torch.pi * ns / S).to(float)
        Ws_im = torch.sin(-2.0 * torch.pi * ns / S).to(float)
        Wd_re = torch.cos(-2.0 * torch.pi * nd / D).to(float)
        Wd_im = torch.sin(-2.0 * torch.pi * nd / D).to(float)
        return {
            "x": _w((lambda *s: torch.randn(*s, dtype=torch.bfloat16) * 0.05)(B, S, D)),
            "dft_seq_re": _w(Ws_re),
            "dft_seq_im": _w(Ws_im),
            "dft_hid_re": _w(Wd_re),
            "dft_hid_im": _w(Wd_im),
        }
    _A_in = _A_setup()

    def _B_setup():
        B, S, D = 1, 2048, 512
        return {
            "x": _w((lambda *s: torch.randn(*s, dtype=torch.bfloat16) * 0.2)(B, S, D)),
            "wqkv": _w((lambda *s: torch.randn(*s, dtype=torch.bfloat16) * 0.8)(D, 3 * D)),
            "wo": _w((lambda *s: torch.randn(*s, dtype=torch.bfloat16) * 0.2)(D, D)),
        }
    _B_in = _B_setup()

    return {
        "A_x": _A_in["x"],
        "A_dft_seq_re": _A_in["dft_seq_re"],
        "A_dft_seq_im": _A_in["dft_seq_im"],
        "A_dft_hid_re": _A_in["dft_hid_re"],
        "A_dft_hid_im": _A_in["dft_hid_im"],
        "B_x": _B_in["x"],
        "B_wqkv": _B_in["wqkv"],
        "B_wo": _B_in["wo"],
    }

def reference(A_x, A_dft_seq_re, A_dft_seq_im, A_dft_hid_re, A_dft_hid_im, B_x, B_wqkv, B_wo):
    def _A_ref(x, dft_seq_re, dft_seq_im, dft_hid_re, dft_hid_im):
        a = torch.einsum("bsd,ed->bse", x, dft_hid_re)
        b = torch.einsum("bsd,ed->bse", x, dft_hid_im)
        y = torch.einsum("ts,bse->bte", dft_seq_re, a) - torch.einsum("ts,bse->bte", dft_seq_im, b)
        return y

    def _B_ref(x, wqkv, wo):
        B, S, D = x.shape
        H = 32; Dh = D // H
        W, dil = 8, 2
        qkv = (lambda x, *d: x.permute(*d))((x @ wqkv).reshape(B, S, 3, H, Dh), 2, 0, 3, 1, 4)
        q, k, v = qkv[0], qkv[1], qkv[2]
        scale = Dh ** -0.5
        pos = torch.arange(S)
        off = torch.arange(W) * dil
        idx = pos[:, None] - off[None, :]
        mask = idx >= 0
        idx_c = torch.where(mask, idx, 0).reshape(-1)
        kg = k[:, :, idx_c, :].reshape(B, H, S, W, Dh)
        vg = v[:, :, idx_c, :].reshape(B, H, S, W, Dh)
        logits = torch.einsum("bhsd,bhswd->bhsw", q, kg) * scale
        logits = torch.where(mask[None, None, :, :], logits, -1e9)
        attn = _softmax(logits, axis=-1)
        o = torch.einsum("bhsw,bhswd->bhsd", attn, vg)
        return (lambda x, *d: x.permute(*d))(o, 0, 2, 1, 3).reshape(B, S, D) @ wo

    a = _A_ref(A_x, A_dft_seq_re, A_dft_seq_im, A_dft_hid_re, A_dft_hid_im)
    b = _B_ref(B_x, B_wqkv, B_wo)
    return torch.concatenate([a, b], axis=-1)
```

```cpp
#include <hip/hip_runtime.h>
#include <hip/hip_bf16.h>
#include <stdint.h>

#define LAS __attribute__((address_space(3)))
#define GAS __attribute__((address_space(1)))
typedef __attribute__((ext_vector_type(8))) __bf16 bf16x8;
typedef __attribute__((ext_vector_type(4))) __bf16 bf16x4;
typedef __attribute__((ext_vector_type(4))) short s16x4;
typedef __attribute__((ext_vector_type(4))) float f32x4;
typedef __attribute__((ext_vector_type(8))) float f32x8;
typedef __attribute__((ext_vector_type(4))) unsigned u32x4;
typedef __attribute__((ext_vector_type(2))) unsigned u32x2;
typedef __amdgpu_buffer_rsrc_t rsrc_t;

constexpr int SEQ = 2048, DM = 512;
constexpr int SLOT = 32768;

constexpr size_t WS_FLAGS = 0;
constexpr size_t WS_ABT   = 1u << 20;
constexpr size_t WS_O     = WS_ABT + (4u << 20);
constexpr size_t WS_PART  = WS_O + (2u << 20);

__device__ __forceinline__ rsrc_t mk_rsrc(const void* p, unsigned bytes) {
    return __builtin_amdgcn_make_buffer_rsrc((void*)p, 0, bytes, 0x00020000);
}
template <int AUX>
__device__ __forceinline__ void glds(rsrc_t r, LAS char* dst, int voff, int soff) {
    __builtin_amdgcn_raw_ptr_buffer_load_lds(r, (LAS void*)dst, 16, voff, soff, 0, AUX);
}
template <int N> __device__ __forceinline__ void bar_vm() {
    asm volatile("s_waitcnt vmcnt(%0)\n\ts_barrier" ::"n"(N) : "memory");
}
__device__ __forceinline__ void bar_lgkm() {
    asm volatile("s_waitcnt lgkmcnt(0)\n\ts_barrier" ::: "memory");
}
__device__ __forceinline__ f32x4 mfma32(bf16x8 a, bf16x8 b, f32x4 c) {
    return __builtin_amdgcn_mfma_f32_16x16x32_bf16(a, b, c, 0, 0, 0);
}
__device__ __forceinline__ f32x4 mfma16(bf16x4 a, bf16x4 b, f32x4 c) {
    return __builtin_amdgcn_mfma_f32_16x16x16bf16_1k(__builtin_bit_cast(s16x4, a), __builtin_bit_cast(s16x4, b), c, 0, 0, 0);
}
__device__ __forceinline__ bf16x8 lds_b128(LAS char* p) { return *(LAS bf16x8*)p; }
__device__ __forceinline__ bf16x8 lds_tr2(LAS char* p) {
    bf16x4 t0 = __builtin_amdgcn_ds_read_tr16_b64_v4bf16((LAS bf16x4*)p);
    bf16x4 t1 = __builtin_amdgcn_ds_read_tr16_b64_v4bf16((LAS bf16x4*)(p + 512));
    return __builtin_shufflevector(t0, t1, 0, 1, 2, 3, 4, 5, 6, 7);
}
__device__ __forceinline__ bf16x4 cvt4(f32x4 v) { return __builtin_convertvector(v, bf16x4); }
__device__ __forceinline__ u32x4 pack8(f32x4 lo, f32x4 hi) {
    bf16x4 a = cvt4(lo), b = cvt4(hi);
    bf16x8 c = __builtin_shufflevector(a, b, 0, 1, 2, 3, 4, 5, 6, 7);
    return __builtin_bit_cast(u32x4, c);
}
__device__ __forceinline__ float rbf(float x) { return (float)(__bf16)x; }
__device__ __forceinline__ float xmax16_32(float x) {
    unsigned u = __float_as_uint(x);
    auto r = __builtin_amdgcn_permlane16_swap(u, u, false, false);
    unsigned a = r[0], b = r[1];
    x = fmaxf(__uint_as_float(a), __uint_as_float(b));
    u = __float_as_uint(x);
    auto r2 = __builtin_amdgcn_permlane32_swap(u, u, false, false);
    a = r2[0]; b = r2[1];
    return fmaxf(__uint_as_float(a), __uint_as_float(b));
}
__device__ __forceinline__ float xsum16_32(float x) {
    unsigned u = __float_as_uint(x);
    auto r = __builtin_amdgcn_permlane16_swap(u, u, false, false);
    unsigned a = r[0], b = r[1];
    x = __uint_as_float(a) + __uint_as_float(b);
    u = __float_as_uint(x);
    auto r2 = __builtin_amdgcn_permlane32_swap(u, u, false, false);
    a = r2[0]; b = r2[1];
    return __uint_as_float(a) + __uint_as_float(b);
}

struct NTLoad {
    int va, vb;
};
__device__ __forceinline__ NTLoad nt_lane(int lane, int lw, int lda_b, int ldb_b) {
    NTLoad r;
    const int ch = ((lane & 7) ^ (4 * (lw & 1) + (lane >> 4))) * 16;
    r.va = (8 * (lane >> 5) + ((lane >> 3) & 3) + 16 * (lw & 1) + 4 * (lw >> 1)) * lda_b + ch;
    r.vb = (8 * lw + (lane >> 3)) * ldb_b + ch;
    return r;
}
template <int AUXA, int AUXB>
__device__ __forceinline__ void nt_issue(LAS char* slot, int lw, rsrc_t ra, rsrc_t rb, NTLoad nl, int sa, int sb, int lda_b, int ldb_b) {
#pragma unroll
    for (int u = 0; u < 4; ++u) {
        glds<AUXB>(rb, slot + 16384 + (lw + 4 * u) * 1024, nl.vb, sb + 32 * u * ldb_b);
        glds<AUXA>(ra, slot + (lw + 4 * u) * 1024, nl.va, sa + 32 * u * lda_b);
    }
}
struct Frag { bf16x8 a[4], b[4]; };
__device__ __forceinline__ void nt_read(Frag& f, LAS char* pa, LAS char* pb) {
#pragma unroll
    for (int m = 0; m < 4; ++m) f.a[m] = lds_b128(pa + m * 2048);
#pragma unroll
    for (int n = 0; n < 4; ++n) f.b[n] = lds_b128(pb + n * 2048);
}
__device__ __forceinline__ void nt_mma(f32x4 (&acc)[4][4], const Frag& f) {
#pragma unroll
    for (int m = 0; m < 4; ++m)
#pragma unroll
        for (int n = 0; n < 4; ++n) acc[m][n] = mfma32(f.a[m], f.b[n], acc[m][n]);
}
template <int NS, bool LASTBAR>
__device__ __forceinline__ int nt_consume(f32x4 (&acc)[4][4], LAS char* smem, int slot, int nst, int w, int lane) {
    const int r = lane & 15, g = lane >> 4;
    const int c0 = ((g ^ (r >> 1)) << 4), c1 = c0 ^ 64;
    const int oa = ((w & 1) * 64 + r) * 128, ob = 16384 + ((w >> 1) * 64 + r) * 128;
    Frag f0, f1;
    LAS char* s = smem + slot * SLOT;
    nt_read(f0, s + oa + c0, s + ob + c0);
    for (int it = 0; it < nst; ++it) {
        nt_read(f1, s + oa + c1, s + ob + c1);
        nt_mma(acc, f0);
        slot = (slot + 1 == NS) ? 0 : slot + 1;
        s = smem + slot * SLOT;
        if (it + 1 < nst) {
            bar_lgkm();
            nt_read(f0, s + oa + c0, s + ob + c0);
        } else if (LASTBAR) {
            bar_lgkm();
        }
        nt_mma(acc, f1);
    }
    return slot;
}

constexpr int T2_SLOT = 43008;

__global__ __launch_bounds__(512, 2) void k1(const __bf16* __restrict__ xa, const __bf16* __restrict__ hre, const __bf16* __restrict__ him,
                                             const __bf16* __restrict__ xb, const __bf16* __restrict__ wqkv, char* __restrict__ ws) {
    __shared__ __attribute__((aligned(1024))) char smem_[163840];
    LAS char* smem = (LAS char*)smem_;
    const int tid = threadIdx.x, lane = tid & 63;
    const int wave = __builtin_amdgcn_readfirstlane(tid >> 6);
    const int bid = blockIdx.x, xcd = bid & 7, idx = bid >> 3;
    if (tid < 64) ((unsigned*)(ws + WS_FLAGS))[bid * 64 + tid] = 0u;

    if ((idx & 1) == 0) {
        const int q = xcd & 3, half = xcd >> 2, id = idx >> 1;
        const int st = (q & 1) * 8 + half * 4 + (id >> 2), et = id & 3;
        const int s0 = st * 128, e0 = et * 128;
        const __bf16* h = (q >> 1) ? him : hre;
        constexpr int NS = 5, NST = 8;
        if (wave >= 4) {
            const int lw = wave - 4;
            rsrc_t ra = mk_rsrc(xa, SEQ * DM * 2), rb = mk_rsrc(h, DM * DM * 2);
            NTLoad nl = nt_lane(lane, lw, 1024, 1024);
            int sa = s0 * 1024, sb = e0 * 1024;
#pragma unroll
            for (int i = 0; i < NS - 1; ++i) nt_issue<0, 0>(smem + i * SLOT, lw, ra, rb, nl, sa + i * 128, sb + i * 128, 1024, 1024);
            int slot = NS - 1;
#pragma unroll 1
            for (int gi = 0; gi < NST - (NS - 1); ++gi) {
                bar_vm<8 * (NS - 2)>();
                nt_issue<0, 0>(smem + slot * SLOT, lw, ra, rb, nl, sa + (gi + NS - 1) * 128, sb + (gi + NS - 1) * 128, 1024, 1024);
                slot = (slot + 1 == NS) ? 0 : slot + 1;
            }
            bar_vm<24>(); bar_vm<16>(); bar_vm<8>(); bar_vm<0>();
        } else {
            f32x4 acc[4][4] = {};
            bar_lgkm();
            nt_consume<NS, false>(acc, smem, 0, NST, wave, lane);
            const float sg = (q >> 1) ? -1.f : 1.f;
            __bf16* abT = (__bf16*)(ws + WS_ABT);
            const int r = lane & 15, g = lane >> 4;
#pragma unroll
            for (int n = 0; n < 4; ++n) {
                const int erow = (q >> 1) * 512 + e0 + (wave >> 1) * 64 + n * 16 + r;
#pragma unroll
                for (int u = 0; u < 2; ++u) {
                    u32x4 v = pack8(acc[2 * u][n] * sg, acc[2 * u + 1][n] * sg);
                    *(u32x4*)(abT + (size_t)erow * SEQ + s0 + (wave & 1) * 64 + 32 * u + 8 * g) = v;
                }
            }
        }
    } else {
        const int id = idx >> 1;
        const int rt = (xcd >> 1) * 4 + (id >> 2), hg = (xcd & 1) * 4 + (id & 3);
        const int s0 = rt * 128;
        constexpr int NS = 3, NST = 8;
        if (wave >= 4) {
            const int lw = wave - 4;
            rsrc_t rx = mk_rsrc(xb, SEQ * DM * 2), rw = mk_rsrc(wqkv, DM * 3 * DM * 2);
            const int vx = (lane >> 3) * 1024 + (((lane & 7) ^ (4 * (lw & 1) + (lane >> 4))) * 16);
            const int vw = (lane >> 3) * 3072 + (((lane & 7) ^ (2 * (((lane >> 4) & 1) | ((lw & 1) << 1)))) * 16);
            const int sxm = (s0 + 8 * lw) * 1024;
            const int sxh = ((s0 >= 16 ? s0 - 16 : 0) + 8 * lw) * 1024;
            const int sw = (8 * lw) * 3072 + hg * 128;
            auto issue = [&](int slot, int kt) {
                LAS char* sl = smem + slot * T2_SLOT;
#pragma unroll
                for (int v = 0; v < 2; ++v)
#pragma unroll
                    for (int cg = 0; cg < 3; ++cg)
                        glds<0>(rw, sl + 18432 + ((lw + 4 * v) * 3 + cg) * 1024, vw, sw + (kt * 64 + 32 * v) * 3072 + cg * 1024);
#pragma unroll
                for (int u = 0; u < 4; ++u) glds<0>(rx, sl + 2048 + (lw + 4 * u) * 1024, vx, sxm + 32 * u * 1024 + kt * 128);
                if (lw < 2) glds<0>(rx, sl + lw * 1024, vx, sxh + kt * 128);
            };
            issue(0, 0); issue(1, 1);
            int slot = 2;
#pragma unroll 1
            for (int gi = 0; gi < NST - 2; ++gi) {
                bar_vm<10>();
                issue(slot, gi + 2);
                slot = (slot + 1 == NS) ? 0 : slot + 1;
            }
            bar_vm<10>(); bar_vm<0>();
        } else {
            const int r = lane & 15, g = lane >> 4;
            const int c0 = ((g ^ (r >> 1)) << 4);
            const int ox = r * 128;
            const int qq = (lane & 15) >> 2, p = lane & 3;
            const int ow = 18432 + g * 3072 + qq * 128 + (((2 * wave + (p >> 1)) ^ (2 * ((qq >> 1) | ((g & 1) << 1)))) << 4) + 8 * (p & 1);
            f32x4 aq[8] = {}, ak[9] = {}, av[9] = {};
            int slot = 0;
#pragma unroll 1
            for (int it = 0; it < NST; ++it) {
                bar_lgkm();
                LAS char* sl = smem + slot * T2_SLOT;
#pragma unroll
                for (int kk = 0; kk < 2; ++kk) {
                    bf16x8 wq = lds_tr2(sl + ow + kk * 12288), wk = lds_tr2(sl + ow + kk * 12288 + 1024), wv = lds_tr2(sl + ow + kk * 12288 + 2048);
#pragma unroll
                    for (int rb = 0; rb < 9; ++rb) {
                        bf16x8 xf = lds_b128(sl + ox + (c0 ^ (kk * 64)) + rb * 2048);
                        if (rb > 0) aq[rb - 1] = mfma32(wq, xf, aq[rb - 1]);
                        ak[rb] = mfma32(wk, xf, ak[rb]);
                        av[rb] = mfma32(xf, wv, av[rb]);
                    }
                }
                slot = (slot + 1 == NS) ? 0 : slot + 1;
            }
            const int j = r, par = j & 1;
            const bool uc0 = (4 * g + par) <= j, uc1 = (4 * g + par + 2) <= j;
            __bf16* o = (__bf16*)(ws + WS_O);
            const int head = hg * 4 + wave;
            bf16x4 kp = cvt4(ak[0]), vp = cvt4(av[0]);
            const f32x4 z = {0.f, 0.f, 0.f, 0.f};
#pragma unroll
            for (int rb = 1; rb < 9; ++rb) {
                bf16x4 kc = cvt4(ak[rb]), vc = cvt4(av[rb]), qb = cvt4(aq[rb - 1]);
                f32x4 sc = mfma16(kc, qb, z), sp = mfma16(kp, qb, z);
                float c0v = par ? sc[1] : sc[0], c1v = par ? sc[3] : sc[2];
                float p0v = par ? sp[1] : sp[0], p1v = par ? sp[3] : sp[2];
                if (rb == 1 && s0 == 0) { p0v = -4e9f; p1v = -4e9f; }
                float l0 = rbf(uc0 ? c0v : p0v) * 0.25f, l1 = rbf(uc1 ? c1v : p1v) * 0.25f;
                float m = xmax16_32(fmaxf(l0, l1));
                float e0 = __expf(l0 - m), e1 = __expf(l1 - m);
                float inv = __builtin_amdgcn_rcpf(xsum16_32(e0 + e1));
                __bf16 b0 = (__bf16)(e0 * inv), b1 = (__bf16)(e1 * inv);
                unsigned t0 = (unsigned)__builtin_bit_cast(unsigned short, b0) << (16 * par);
                unsigned t1 = (unsigned)__builtin_bit_cast(unsigned short, b1) << (16 * par);
                u32x2 pc = {uc0 ? t0 : 0u, uc1 ? t1 : 0u}, pp = {uc0 ? 0u : t0, uc1 ? 0u : t1};
                f32x4 ov = mfma16(vc, __builtin_bit_cast(bf16x4, pc), z);
                ov = mfma16(vp, __builtin_bit_cast(bf16x4, pp), ov);
                bf16x4 ob = cvt4(ov);
                *(bf16x4*)(o + (size_t)(s0 + (rb - 1) * 16 + j) * DM + head * 16 + 4 * g) = ob;
                kp = kc; vp = vc;
            }
        }
    }
}

__global__ __launch_bounds__(512, 2) void k2(const __bf16* __restrict__ cre, const __bf16* __restrict__ cim, const __bf16* __restrict__ wo,
                                             char* __restrict__ ws, __bf16* __restrict__ out) {
    __shared__ __attribute__((aligned(1024))) char smem_[163840];
    LAS char* smem = (LAS char*)smem_;
    const int tid = threadIdx.x, lane = tid & 63;
    const int wave = __builtin_amdgcn_readfirstlane(tid >> 6);
    const int bid = blockIdx.x, xcd = bid & 7, idx = bid >> 3;
    const int q = xcd & 3, tt = (xcd >> 2) * 8 + (idx >> 2), et = idx & 3;
    const int t0 = tt * 128, e0 = et * 128;
    const int s4 = (xcd * 4 + (idx >> 3)) * 64, n4 = (idx & 7) * 64;
    constexpr int NS = 5, NST3 = 16, NST4 = 4;
    if (wave >= 4) {
        const int lw = wave - 4;
        rsrc_t ra = mk_rsrc(ws + WS_ABT, 1024 * SEQ * 2), rb = mk_rsrc((q >> 1) ? cim : cre, SEQ * SEQ * 2);
        rsrc_t ro = mk_rsrc(ws + WS_O, SEQ * DM * 2), rw = mk_rsrc(wo, DM * DM * 2);
        NTLoad nl = nt_lane(lane, lw, 4096, 4096);
        const int sa = ((q >> 1) * 512 + e0) * 4096 + (q & 1) * 2048, sb = t0 * 4096 + (q & 1) * 2048;
        const int vo = (lane >> 3) * 1024 + (((lane & 7) ^ (4 * (lw & 1) + (lane >> 4))) * 16);
        const int vw = (lane >> 3) * 1024 + (((lane & 7) ^ (2 * (((lane >> 4) & 1) | ((lw & 1) << 1)))) * 16);
        auto issue4 = [&](int slot, int st) {
            LAS char* sl = smem + slot * SLOT;
#pragma unroll
            for (int h = 0; h < 2; ++h) {
                const int kt = st * 2 + h;
#pragma unroll
                for (int u = 0; u < 2; ++u) {
                    glds<0>(rw, sl + h * 16384 + 8192 + (lw + 4 * u) * 1024, vw, (kt * 64 + 8 * lw + 32 * u) * 1024 + n4 * 2);
                    glds<0>(ro, sl + h * 16384 + (lw + 4 * u) * 1024, vo, (s4 + 8 * lw + 32 * u) * 1024 + kt * 128);
                }
            }
        };
#pragma unroll
        for (int i = 0; i < NS - 1; ++i) nt_issue<0, 0>(smem + i * SLOT, lw, ra, rb, nl, sa + i * 128, sb + i * 128, 4096, 4096);
        int slot = NS - 1;
#pragma unroll 1
        for (int gi = 0; gi < NST3 - (NS - 1); ++gi) {
            bar_vm<8 * (NS - 2)>();
            nt_issue<0, 0>(smem + slot * SLOT, lw, ra, rb, nl, sa + (gi + NS - 1) * 128, sb + (gi + NS - 1) * 128, 4096, 4096);
            slot = (slot + 1 == NS) ? 0 : slot + 1;
        }
#pragma unroll 1
        for (int gi = 0; gi < NST4; ++gi) {
            bar_vm<8 * (NS - 2)>();
            issue4(slot, gi);
            slot = (slot + 1 == NS) ? 0 : slot + 1;
        }
        bar_vm<24>(); bar_vm<16>(); bar_vm<8>(); bar_vm<0>();
    } else {
        const int r = lane & 15, g = lane >> 4;
        f32x4 acc[4][4] = {};
        bar_lgkm();
        int slot = nt_consume<NS, true>(acc, smem, 0, NST3, wave, lane);
        const int tile = tt * 4 + et;
        {
            rsrc_t rp = mk_rsrc(ws + WS_PART, 64 * 16 * 4 * 4096);
#pragma unroll
            for (int n = 0; n < 4; ++n) {
                if (n != q) {
                    const int base = (((tile * 4 + n) * 4 + q) * 4 + wave) * 4096;
#pragma unroll
                    for (int m = 0; m < 4; ++m)
                        __builtin_amdgcn_raw_buffer_store_b128(__builtin_bit_cast(u32x4, acc[m][n]), rp, lane * 16 + m * 1024, base, 16);
                }
            }
        }
        f32x4 a4[2][2] = {};
        {
            const int c0 = ((g ^ (r >> 1)) << 4);
            const int oo = ((wave >> 1) * 32 + r) * 128;
            const int qq = (lane & 15) >> 2, p = lane & 3;
            const int swz = 2 * ((qq >> 1) | ((g & 1) << 1));
            const int ow0 = 8192 + g * 1024 + qq * 128 + ((((wave & 1) * 4 + (p >> 1)) ^ swz) << 4) + 8 * (p & 1);
            const int ow1 = 8192 + g * 1024 + qq * 128 + ((((wave & 1) * 4 + 2 + (p >> 1)) ^ swz) << 4) + 8 * (p & 1);
#pragma unroll 1
            for (int st = 0; st < NST4; ++st) {
                LAS char* sl = smem + slot * SLOT;
#pragma unroll
                for (int h = 0; h < 2; ++h)
#pragma unroll
                    for (int kk = 0; kk < 2; ++kk) {
                        bf16x8 w0 = lds_tr2(sl + h * 16384 + ow0 + kk * 4096), w1 = lds_tr2(sl + h * 16384 + ow1 + kk * 4096);
                        bf16x8 o0 = lds_b128(sl + h * 16384 + oo + (c0 ^ (kk * 64))), o1 = lds_b128(sl + h * 16384 + oo + (c0 ^ (kk * 64)) + 2048);
                        a4[0][0] = mfma32(w0, o0, a4[0][0]);
                        a4[0][1] = mfma32(w0, o1, a4[0][1]);
                        a4[1][0] = mfma32(w1, o0, a4[1][0]);
                        a4[1][1] = mfma32(w1, o1, a4[1][1]);
                    }
                slot = (slot + 1 == NS) ? 0 : slot + 1;
                if (st == 0) {
                    asm volatile("s_waitcnt vmcnt(0)" ::: "memory");
                    if (lane < 4 && lane != q) {
                        GAS unsigned* f = (GAS unsigned*)(ws + WS_FLAGS) + ((tile * 4 + lane) * 4 + wave) * 16 + q;
                        __hip_atomic_store(f, 1u, __ATOMIC_RELAXED, __HIP_MEMORY_SCOPE_AGENT);
                    }
                }
                if (st + 1 < NST4) bar_lgkm();
            }
#pragma unroll
            for (int m = 0; m < 2; ++m)
#pragma unroll
                for (int n = 0; n < 2; ++n)
                    *(bf16x4*)(out + (size_t)(s4 + (wave >> 1) * 32 + n * 16 + r) * 1024 + 512 + n4 + (wave & 1) * 32 + m * 16 + 4 * g) = cvt4(a4[m][n]);
        }
        {
            GAS unsigned* f = (GAS unsigned*)(ws + WS_FLAGS) + ((tile * 4 + q) * 4 + wave) * 16 + (lane & 3);
            unsigned spins = 0;
            for (;;) {
                unsigned v = __hip_atomic_load(f, __ATOMIC_RELAXED, __HIP_MEMORY_SCOPE_AGENT);
                bool ok = (v == 1u) || ((lane & 3) == q);
                if (__all(ok)) break;
                if (++spins > (1u << 22)) break;
                __builtin_amdgcn_s_sleep(1);
            }
            __builtin_amdgcn_fence(__ATOMIC_ACQUIRE, "agent");
            rsrc_t rp = mk_rsrc(ws + WS_PART, 64 * 16 * 4 * 4096);
            f32x4 own[4];
#pragma unroll
            for (int m = 0; m < 4; ++m) own[m] = (q == 0) ? acc[m][0] : (q == 1) ? acc[m][1] : (q == 2) ? acc[m][2] : acc[m][3];
            u32x4 pv[3][4];
#pragma unroll
            for (int k = 0; k < 3; ++k) {
                const int src = (q + 1 + k) & 3;
                const int base = (((tile * 4 + q) * 4 + src) * 4 + wave) * 4096;
#pragma unroll
                for (int m = 0; m < 4; ++m) pv[k][m] = __builtin_amdgcn_raw_buffer_load_b128(rp, lane * 16 + m * 1024, base, 16);
            }
#pragma unroll
            for (int k = 0; k < 3; ++k)
#pragma unroll
                for (int m = 0; m < 4; ++m) own[m] += __builtin_bit_cast(f32x4, pv[k][m]);
            const int trow = t0 + (wave >> 1) * 64 + q * 16 + r;
#pragma unroll
            for (int u = 0; u < 2; ++u)
                *(u32x4*)(out + (size_t)trow * 1024 + e0 + (wave & 1) * 64 + 32 * u + 8 * g) = pack8(own[2 * u], own[2 * u + 1]);
        }
    }
}

extern "C" void kernel_launch(void* const* d_in, const int* in_sizes, int n_in,
                              void* d_out, int out_size, void* d_ws, size_t ws_size,
                              hipStream_t stream) {
    const __bf16* xa = (const __bf16*)d_in[0];
    const __bf16* cre = (const __bf16*)d_in[1];
    const __bf16* cim = (const __bf16*)d_in[2];
    const __bf16* hre = (const __bf16*)d_in[3];
    const __bf16* him = (const __bf16*)d_in[4];
    const __bf16* xb = (const __bf16*)d_in[5];
    const __bf16* wqkv = (const __bf16*)d_in[6];
    const __bf16* wo = (const __bf16*)d_in[7];
    k1<<<256, 512, 0, stream>>>(xa, hre, him, xb, wqkv, (char*)d_ws);
    k2<<<256, 512, 0, stream>>>(cre, cim, wo, (char*)d_ws, (__bf16*)d_out);
}
```

```cpp
#include <hip/hip_runtime.h>
#include <hip/hip_bf16.h>
#include <stdint.h>

#define LAS __attribute__((address_space(3)))
#define GAS __attribute__((address_space(1)))
typedef __attribute__((ext_vector_type(8))) __bf16 bf16x8;
typedef __attribute__((ext_vector_type(4))) __bf16 bf16x4;
typedef __attribute__((ext_vector_type(4))) short s16x4;
typedef __attribute__((ext_vector_type(4))) float f32x4;
typedef __attribute__((ext_vector_type(8))) float f32x8;
typedef __attribute__((ext_vector_type(4))) unsigned u32x4;
typedef __attribute__((ext_vector_type(2))) unsigned u32x2;
typedef __amdgpu_buffer_rsrc_t rsrc_t;

#define NOSTAMPS 1
#define STAMP_N 16
#define STAMP_OFF (64u<<20)
#define STAMP(k) do{}while(0)
constexpr int SEQ = 2048, DM = 512;
constexpr int SLOT = 32768;

constexpr size_t WS_FLAGS = 0;
constexpr size_t WS_ABT   = 1u << 20;
constexpr size_t WS_O     = WS_ABT + (4u << 20);
constexpr size_t WS_PART  = WS_O + (2u << 20);

__device__ __forceinline__ rsrc_t mk_rsrc(const void* p, unsigned bytes) {
    return __builtin_amdgcn_make_buffer_rsrc((void*)p, 0, bytes, 0x00020000);
}
template <int AUX>
__device__ __forceinline__ void glds(rsrc_t r, LAS char* dst, int voff, int soff) {
    __builtin_amdgcn_raw_ptr_buffer_load_lds(r, (LAS void*)dst, 16, voff, soff, 0, AUX);
}
template <int N> __device__ __forceinline__ void bar_vm() {
    asm volatile("s_waitcnt vmcnt(%0)\n\ts_barrier" ::"n"(N) : "memory");
}
__device__ __forceinline__ void bar_lgkm() {
    asm volatile("s_waitcnt lgkmcnt(0)\n\ts_barrier" ::: "memory");
}
__device__ __forceinline__ f32x4 mfma32(bf16x8 a, bf16x8 b, f32x4 c) {
    return __builtin_amdgcn_mfma_f32_16x16x32_bf16(a, b, c, 0, 0, 0);
}
__device__ __forceinline__ f32x4 mfma16(bf16x4 a, bf16x4 b, f32x4 c) {
    return __builtin_amdgcn_mfma_f32_16x16x16bf16_1k(__builtin_bit_cast(s16x4, a), __builtin_bit_cast(s16x4, b), c, 0, 0, 0);
}
__device__ __forceinline__ bf16x8 lds_b128(LAS char* p) { return *(LAS bf16x8*)p; }
__device__ __forceinline__ bf16x8 lds_tr2(LAS char* p) {
    bf16x4 t0 = __builtin_amdgcn_ds_read_tr16_b64_v4bf16((LAS bf16x4*)p);
    bf16x4 t1 = __builtin_amdgcn_ds_read_tr16_b64_v4bf16((LAS bf16x4*)(p + 512));
    return __builtin_shufflevector(t0, t1, 0, 1, 2, 3, 4, 5, 6, 7);
}
__device__ __forceinline__ bf16x4 cvt4(f32x4 v) { return __builtin_convertvector(v, bf16x4); }
__device__ __forceinline__ u32x4 pack8(f32x4 lo, f32x4 hi) {
    bf16x4 a = cvt4(lo), b = cvt4(hi);
    bf16x8 c = __builtin_shufflevector(a, b, 0, 1, 2, 3, 4, 5, 6, 7);
    return __builtin_bit_cast(u32x4, c);
}
__device__ __forceinline__ float rbf(float x) { return (float)(__bf16)x; }
__device__ __forceinline__ float xmax16_32(float x) {
    unsigned u = __float_as_uint(x);
    auto r = __builtin_amdgcn_permlane16_swap(u, u, false, false);
    unsigned a = r[0], b = r[1];
    x = fmaxf(__uint_as_float(a), __uint_as_float(b));
    u = __float_as_uint(x);
    auto r2 = __builtin_amdgcn_permlane32_swap(u, u, false, false);
    a = r2[0]; b = r2[1];
    return fmaxf(__uint_as_float(a), __uint_as_float(b));
}
__device__ __forceinline__ float xsum16_32(float x) {
    unsigned u = __float_as_uint(x);
    auto r = __builtin_amdgcn_permlane16_swap(u, u, false, false);
    unsigned a = r[0], b = r[1];
    x = __uint_as_float(a) + __uint_as_float(b);
    u = __float_as_uint(x);
    auto r2 = __builtin_amdgcn_permlane32_swap(u, u, false, false);
    a = r2[0]; b = r2[1];
    return __uint_as_float(a) + __uint_as_float(b);
}

struct NTLoad {
    int va, vb;
};
__device__ __forceinline__ NTLoad nt_lane(int lane, int lw, int lda_b, int ldb_b) {
    NTLoad r;
    const int ch = ((lane & 7) ^ (4 * (lw & 1) + (lane >> 4))) * 16;
    r.va = (8 * (lane >> 5) + ((lane >> 3) & 3) + 16 * (lw & 1) + 4 * (lw >> 1)) * lda_b + ch;
    r.vb = (8 * lw + (lane >> 3)) * ldb_b + ch;
    return r;
}
template <int AUXA, int AUXB>
__device__ __forceinline__ void nt_issue(LAS char* slot, int lw, rsrc_t ra, rsrc_t rb, NTLoad nl, int sa, int sb, int lda_b, int ldb_b) {
#pragma unroll
    for (int u = 0; u < 4; ++u) {
        glds<AUXB>(rb, slot + 16384 + (lw + 4 * u) * 1024, nl.vb, sb + 32 * u * ldb_b);
        glds<AUXA>(ra, slot + (lw + 4 * u) * 1024, nl.va, sa + 32 * u * lda_b);
    }
}
struct Frag { bf16x8 a[4], b[4]; };
#define SGB() do { _Pragma("unroll") for (int _i = 0; _i < 8; ++_i) { __builtin_amdgcn_sched_group_barrier(0x8, 1, 0); __builtin_amdgcn_sched_group_barrier(0x100, 1, 0); } __builtin_amdgcn_sched_group_barrier(0x8, 8, 0); } while (0)
__device__ __forceinline__ void nt_read(Frag& f, LAS char* pa, LAS char* pb) {
    f.a[0] = lds_b128(pa);
#pragma unroll
    for (int n = 0; n < 4; ++n) f.b[n] = lds_b128(pb + n * 2048);
#pragma unroll
    for (int m = 1; m < 4; ++m) f.a[m] = lds_b128(pa + m * 2048);
}
__device__ __forceinline__ void nt_mma(f32x4 (&acc)[4][4], const Frag& f) {
#pragma unroll
    for (int m = 0; m < 4; ++m)
#pragma unroll
        for (int n = 0; n < 4; ++n) acc[m][n] = mfma32(f.a[m], f.b[n], acc[m][n]);
}
template <int NS, bool LASTBAR>
__device__ __forceinline__ int nt_consume(f32x4 (&acc)[4][4], LAS char* smem, int slot, int nst, int w, int lane) {
    const int r = lane & 15, g = lane >> 4;
    const int c0 = ((g ^ (r >> 1)) << 4), c1 = c0 ^ 64;
    const int oa = ((w & 1) * 64 + r) * 128, ob = 16384 + ((w >> 1) * 64 + r) * 128;
    Frag f0, f1;
    LAS char* s = smem + slot * SLOT;
    nt_read(f0, s + oa + c0, s + ob + c0);
#pragma unroll 1
    for (int it = 0; it < nst - 1; ++it) {
        __builtin_amdgcn_sched_barrier(0);
        nt_read(f1, s + oa + c1, s + ob + c1);
        nt_mma(acc, f0);
        SGB();
        __builtin_amdgcn_sched_barrier(0);
        slot = (slot + 1 == NS) ? 0 : slot + 1;
        s = smem + slot * SLOT;
        bar_lgkm();
        __builtin_amdgcn_sched_barrier(0);
        nt_read(f0, s + oa + c0, s + ob + c0);
        nt_mma(acc, f1);
        SGB();
        __builtin_amdgcn_sched_barrier(0);
    }
    nt_read(f1, s + oa + c1, s + ob + c1);
    nt_mma(acc, f0);
    SGB();
    __builtin_amdgcn_sched_barrier(0);
    slot = (slot + 1 == NS) ? 0 : slot + 1;
    if (LASTBAR) bar_lgkm();
    __builtin_amdgcn_sched_barrier(0);
    nt_mma(acc, f1);
    return slot;
}

constexpr int T2_SLOT = 43008;

__global__ __launch_bounds__(512, 2) void k1(const __bf16* __restrict__ xa, const __bf16* __restrict__ hre, const __bf16* __restrict__ him,
                                             const __bf16* __restrict__ xb, const __bf16* __restrict__ wqkv, char* __restrict__ ws) {
    __shared__ __attribute__((aligned(1024))) char smem_[163840];
    LAS char* smem = (LAS char*)smem_;
    const int tid = threadIdx.x, lane = tid & 63;
    const int wave = __builtin_amdgcn_readfirstlane(tid >> 6);
    const int bid = blockIdx.x, xcd = bid & 7, idx = bid >> 3;
    if (wave==0) STAMP(0);
    if (tid < 64) ((unsigned*)(ws + WS_FLAGS))[bid * 64 + tid] = 0u;

    if ((idx & 1) == 0) {
        const int q = xcd & 3, half = xcd >> 2, id = idx >> 1;
        const int st = (q & 1) * 8 + half * 4 + (id >> 2), et = id & 3;
        const int s0 = st * 128, e0 = et * 128;
        const __bf16* h = (q >> 1) ? him : hre;
        constexpr int NS = 5, NST = 8;
        if (wave >= 4) {
            const int lw = wave - 4;
            rsrc_t ra = mk_rsrc(xa, SEQ * DM * 2), rb = mk_rsrc(h, DM * DM * 2);
            NTLoad nl = nt_lane(lane, lw, 1024, 1024);
            int sa = s0 * 1024, sb = e0 * 1024;
#pragma unroll
            for (int i = 0; i < NS - 1; ++i) nt_issue<0, 0>(smem + i * SLOT, lw, ra, rb, nl, sa + i * 128, sb + i * 128, 1024, 1024);
            int slot = NS - 1;
#pragma unroll 1
            for (int gi = 0; gi < NST - (NS - 1); ++gi) {
                bar_vm<8 * (NS - 2)>();
                nt_issue<0, 0>(smem + slot * SLOT, lw, ra, rb, nl, sa + (gi + NS - 1) * 128, sb + (gi + NS - 1) * 128, 1024, 1024);
                slot = (slot + 1 == NS) ? 0 : slot + 1;
            }
            bar_vm<24>(); bar_vm<16>(); bar_vm<8>(); bar_vm<0>();
        } else {
            f32x4 acc[4][4] = {};
            bar_lgkm();
            if (wave==0) STAMP(1);
            nt_consume<NS, false>(acc, smem, 0, NST, wave, lane);
            if (wave==0) STAMP(2);
            const float sg = (q >> 1) ? -1.f : 1.f;
            __bf16* abT = (__bf16*)(ws + WS_ABT);
            const int r = lane & 15, g = lane >> 4;
#pragma unroll
            for (int n = 0; n < 4; ++n) {
                const int erow = (q >> 1) * 512 + e0 + (wave >> 1) * 64 + n * 16 + r;
#pragma unroll
                for (int u = 0; u < 2; ++u) {
                    u32x4 v = pack8(acc[2 * u][n] * sg, acc[2 * u + 1][n] * sg);
                    *(u32x4*)(abT + (size_t)erow * SEQ + s0 + (wave & 1) * 64 + 32 * u + 8 * g) = v;
                }
            }
            asm volatile("s_waitcnt vmcnt(0)":::"memory");
            if (wave==0) STAMP(3);
        }
    } else {
        const int id = idx >> 1;
        const int rt = (xcd >> 1) * 4 + (id >> 2), hg = (xcd & 1) * 4 + (id & 3);
        const int s0 = rt * 128;
        constexpr int NS = 3, NST = 8;
        if (wave >= 4) {
            const int lw = wave - 4;
            rsrc_t rx = mk_rsrc(xb, SEQ * DM * 2), rw = mk_rsrc(wqkv, DM * 3 * DM * 2);
            const int vx = (lane >> 3) * 1024 + (((lane & 7) ^ (4 * (lw & 1) + (lane >> 4))) * 16);
            const int vw = (lane >> 3) * 3072 + (((lane & 7) ^ (2 * (((lane >> 4) & 1) | ((lw & 1) << 1)))) * 16);
            const int sxm = (s0 + 8 * lw) * 1024;
            const int sxh = ((s0 >= 16 ? s0 - 16 : 0) + 8 * lw) * 1024;
            const int sw = (8 * lw) * 3072 + hg * 128;
            auto issue = [&](int slot, int kt) {
                LAS char* sl = smem + slot * T2_SLOT;
#pragma unroll
                for (int v = 0; v < 2; ++v)
#pragma unroll
                    for (int cg = 0; cg < 3; ++cg)
                        glds<0>(rw, sl + 18432 + ((lw + 4 * v) * 3 + cg) * 1024, vw, sw + (kt * 64 + 32 * v) * 3072 + cg * 1024);
#pragma unroll
                for (int u = 0; u < 4; ++u) glds<0>(rx, sl + 2048 + (lw + 4 * u) * 1024, vx, sxm + 32 * u * 1024 + kt * 128);
                if (lw < 2) glds<0>(rx, sl + lw * 1024, vx, sxh + kt * 128);
            };
            issue(0, 0); issue(1, 1);
            int slot = 2;
#pragma unroll 1
            for (int gi = 0; gi < NST - 2; ++gi) {
                bar_vm<10>();
                issue(slot, gi + 2);
                slot = (slot + 1 == NS) ? 0 : slot + 1;
            }
            bar_vm<10>(); bar_vm<0>();
        } else {
            const int r = lane & 15, g = lane >> 4;
            const int c0 = ((g ^ (r >> 1)) << 4);
            const int ox = r * 128;
            const int qq = (lane & 15) >> 2, p = lane & 3;
            const int ow = 18432 + g * 3072 + qq * 128 + (((2 * wave + (p >> 1)) ^ (2 * ((qq >> 1) | ((g & 1) << 1)))) << 4) + 8 * (p & 1);
            f32x4 aq[8] = {}, ak[9] = {}, av[9] = {};
            int slot = 0;
#pragma unroll 1
            for (int it = 0; it < NST; ++it) {
                bar_lgkm();
                LAS char* sl = smem + slot * T2_SLOT;
#pragma unroll
                for (int kk = 0; kk < 2; ++kk) {
                    bf16x8 wq = lds_tr2(sl + ow + kk * 12288), wk = lds_tr2(sl + ow + kk * 12288 + 1024), wv = lds_tr2(sl + ow + kk * 12288 + 2048);
#pragma unroll
                    for (int rb = 0; rb < 9; ++rb) {
                        bf16x8 xf = lds_b128(sl + ox + (c0 ^ (kk * 64)) + rb * 2048);
                        if (rb > 0) aq[rb - 1] = mfma32(wq, xf, aq[rb - 1]);
                        ak[rb] = mfma32(wk, xf, ak[rb]);
                        av[rb] = mfma32(xf, wv, av[rb]);
                    }
                }
                slot = (slot + 1 == NS) ? 0 : slot + 1;
            }
            if (wave==0) STAMP(4);
            const int j = r, par = j & 1;
            const bool uc0 = (4 * g + par) <= j, uc1 = (4 * g + par + 2) <= j;
            __bf16* o = (__bf16*)(ws + WS_O);
            const int head = hg * 4 + wave;
            bf16x4 kp = cvt4(ak[0]), vp = cvt4(av[0]);
            const f32x4 z = {0.f, 0.f, 0.f, 0.f};
#pragma unroll
            for (int rb = 1; rb < 9; ++rb) {
                bf16x4 kc = cvt4(ak[rb]), vc = cvt4(av[rb]), qb = cvt4(aq[rb - 1]);
                f32x4 sc = mfma16(kc, qb, z), sp = mfma16(kp, qb, z);
                float c0v = par ? sc[1] : sc[0], c1v = par ? sc[3] : sc[2];
                float p0v = par ? sp[1] : sp[0], p1v = par ? sp[3] : sp[2];
                if (rb == 1 && s0 == 0) { p0v = -4e9f; p1v = -4e9f; }
                float l0 = rbf(uc0 ? c0v : p0v) * 0.25f, l1 = rbf(uc1 ? c1v : p1v) * 0.25f;
                float m = xmax16_32(fmaxf(l0, l1));
                float e0 = __expf(l0 - m), e1 = __expf(l1 - m);
                float inv = __builtin_amdgcn_rcpf(xsum16_32(e0 + e1));
                __bf16 b0 = (__bf16)(e0 * inv), b1 = (__bf16)(e1 * inv);
                unsigned t0 = (unsigned)__builtin_bit_cast(unsigned short, b0) << (16 * par);
                unsigned t1 = (unsigned)__builtin_bit_cast(unsigned short, b1) << (16 * par);
                u32x2 pc = {uc0 ? t0 : 0u, uc1 ? t1 : 0u}, pp = {uc0 ? 0u : t0, uc1 ? 0u : t1};
                f32x4 ov = mfma16(vc, __builtin_bit_cast(bf16x4, pc), z);
                ov = mfma16(vp, __builtin_bit_cast(bf16x4, pp), ov);
                bf16x4 ob = cvt4(ov);
                *(bf16x4*)(o + (size_t)(s0 + (rb - 1) * 16 + j) * DM + head * 16 + 4 * g) = ob;
                kp = kc; vp = vc;
            }
            asm volatile("s_waitcnt vmcnt(0)":::"memory");
            if (wave==0) STAMP(5);
        }
    }
}

__global__ __launch_bounds__(512, 2) void k2(const __bf16* __restrict__ cre, const __bf16* __restrict__ cim, const __bf16* __restrict__ wo,
                                             char* __restrict__ ws, __bf16* __restrict__ out) {
    __shared__ __attribute__((aligned(1024))) char smem_[163840];
    LAS char* smem = (LAS char*)smem_;
    const int tid = threadIdx.x, lane = tid & 63;
    const int wave = __builtin_amdgcn_readfirstlane(tid >> 6);
    const int bid = blockIdx.x, xcd = bid & 7, idx = bid >> 3;
    const int q = xcd & 3, tt = (xcd >> 2) * 8 + (idx >> 2), et = idx & 3;
    const int t0 = tt * 128, e0 = et * 128;
    const int s4 = (xcd * 4 + (idx >> 3)) * 64, n4 = (idx & 7) * 64;
    constexpr int NS = 5, NST3 = 16, NST4 = 4;
    if (wave==0) STAMP(8);
    if (wave >= 4) {
        const int lw = wave - 4;
        rsrc_t ra = mk_rsrc(ws + WS_ABT, 1024 * SEQ * 2), rb = mk_rsrc((q >> 1) ? cim : cre, SEQ * SEQ * 2);
        rsrc_t ro = mk_rsrc(ws + WS_O, SEQ * DM * 2), rw = mk_rsrc(wo, DM * DM * 2);
        NTLoad nl = nt_lane(lane, lw, 4096, 4096);
        const int sa = ((q >> 1) * 512 + e0) * 4096 + (q & 1) * 2048, sb = t0 * 4096 + (q & 1) * 2048;
        const int vo = (lane >> 3) * 1024 + (((lane & 7) ^ (4 * (lw & 1) + (lane >> 4))) * 16);
        const int vw = (lane >> 3) * 1024 + (((lane & 7) ^ (2 * (((lane >> 4) & 1) | ((lw & 1) << 1)))) * 16);
        auto issue4 = [&](int slot, int st) {
            LAS char* sl = smem + slot * SLOT;
#pragma unroll
            for (int h = 0; h < 2; ++h) {
                const int kt = st * 2 + h;
#pragma unroll
                for (int u = 0; u < 2; ++u) {
                    glds<0>(rw, sl + h * 16384 + 8192 + (lw + 4 * u) * 1024, vw, (kt * 64 + 8 * lw + 32 * u) * 1024 + n4 * 2);
                    glds<0>(ro, sl + h * 16384 + (lw + 4 * u) * 1024, vo, (s4 + 8 * lw + 32 * u) * 1024 + kt * 128);
                }
            }
        };
#pragma unroll
        for (int i = 0; i < NS - 1; ++i) nt_issue<0, 0>(smem + i * SLOT, lw, ra, rb, nl, sa + i * 128, sb + i * 128, 4096, 4096);
        int slot = NS - 1;
#pragma unroll 1
        for (int gi = 0; gi < NST3 - (NS - 1); ++gi) {
            bar_vm<8 * (NS - 2)>();
            nt_issue<0, 0>(smem + slot * SLOT, lw, ra, rb, nl, sa + (gi + NS - 1) * 128, sb + (gi + NS - 1) * 128, 4096, 4096);
            slot = (slot + 1 == NS) ? 0 : slot + 1;
        }
#pragma unroll 1
        for (int gi = 0; gi < NST4; ++gi) {
            bar_vm<8 * (NS - 2)>();
            issue4(slot, gi);
            slot = (slot + 1 == NS) ? 0 : slot + 1;
        }
        bar_vm<24>(); bar_vm<16>(); bar_vm<8>(); bar_vm<0>();
    } else {
        const int r = lane & 15, g = lane >> 4;
        f32x4 acc[4][4] = {};
        bar_lgkm();
        if (wave==0) STAMP(9);
        int slot = nt_consume<NS, true>(acc, smem, 0, NST3, wave, lane);
        if (wave==0) STAMP(10);
        const int tile = tt * 4 + et;
        {
            rsrc_t rp = mk_rsrc(ws + WS_PART, 64 * 16 * 4 * 4096);
#pragma unroll
            for (int n = 0; n < 4; ++n) {
                if (n != q) {
                    const int base = (((tile * 4 + n) * 4 + q) * 4 + wave) * 4096;
#pragma unroll
                    for (int m = 0; m < 4; ++m)
                        __builtin_amdgcn_raw_buffer_store_b128(__builtin_bit_cast(u32x4, acc[m][n]), rp, lane * 16 + m * 1024, base, 16);
                }
            }
        }
        f32x4 a4[2][2] = {};
        {
            const int c0 = ((g ^ (r >> 1)) << 4);
            const int oo = ((wave >> 1) * 32 + r) * 128;
            const int qq = (lane & 15) >> 2, p = lane & 3;
            const int swz = 2 * ((qq >> 1) | ((g & 1) << 1));
            const int ow0 = 8192 + g * 1024 + qq * 128 + ((((wave & 1) * 4 + (p >> 1)) ^ swz) << 4) + 8 * (p & 1);
            const int ow1 = 8192 + g * 1024 + qq * 128 + ((((wave & 1) * 4 + 2 + (p >> 1)) ^ swz) << 4) + 8 * (p & 1);
#pragma unroll 1
            for (int st = 0; st < NST4; ++st) {
                LAS char* sl = smem + slot * SLOT;
#pragma unroll
                for (int h = 0; h < 2; ++h)
#pragma unroll
                    for (int kk = 0; kk < 2; ++kk) {
                        bf16x8 w0 = lds_tr2(sl + h * 16384 + ow0 + kk * 4096), w1 = lds_tr2(sl + h * 16384 + ow1 + kk * 4096);
                        bf16x8 o0 = lds_b128(sl + h * 16384 + oo + (c0 ^ (kk * 64))), o1 = lds_b128(sl + h * 16384 + oo + (c0 ^ (kk * 64)) + 2048);
                        a4[0][0] = mfma32(w0, o0, a4[0][0]);
                        a4[0][1] = mfma32(w0, o1, a4[0][1]);
                        a4[1][0] = mfma32(w1, o0, a4[1][0]);
                        a4[1][1] = mfma32(w1, o1, a4[1][1]);
                    }
                slot = (slot + 1 == NS) ? 0 : slot + 1;
                if (st == 0) {
                    asm volatile("s_waitcnt vmcnt(0)" ::: "memory");
                    if (lane < 4 && lane != q) {
                        GAS unsigned* f = (GAS unsigned*)(ws + WS_FLAGS) + ((tile * 4 + lane) * 4 + wave) * 16 + q;
                        __hip_atomic_store(f, 1u, __ATOMIC_RELAXED, __HIP_MEMORY_SCOPE_AGENT);
                    }
                }
                if (st + 1 < NST4) bar_lgkm();
            }
#pragma unroll
            for (int m = 0; m < 2; ++m)
#pragma unroll
                for (int n = 0; n < 2; ++n)
                    *(bf16x4*)(out + (size_t)(s4 + (wave >> 1) * 32 + n * 16 + r) * 1024 + 512 + n4 + (wave & 1) * 32 + m * 16 + 4 * g) = cvt4(a4[m][n]);
        }
        if (wave==0) STAMP(11);
        {
            GAS unsigned* f = (GAS unsigned*)(ws + WS_FLAGS) + ((tile * 4 + q) * 4 + wave) * 16 + (lane & 3);
            unsigned spins = 0;
            for (;;) {
                unsigned v = __hip_atomic_load(f, __ATOMIC_RELAXED, __HIP_MEMORY_SCOPE_AGENT);
                bool ok = (v == 1u) || ((lane & 3) == q);
                if (__all(ok)) break;
                if (++spins > (1u << 22)) break;
                __builtin_amdgcn_s_sleep(1);
            }
            asm volatile("" ::: "memory");
            if (wave==0) STAMP(12);
            rsrc_t rp = mk_rsrc(ws + WS_PART, 64 * 16 * 4 * 4096);
            f32x4 own[4];
#pragma unroll
            for (int m = 0; m < 4; ++m) own[m] = (q == 0) ? acc[m][0] : (q == 1) ? acc[m][1] : (q == 2) ? acc[m][2] : acc[m][3];
            u32x4 pv[3][4];
#pragma unroll
            for (int k = 0; k < 3; ++k) {
                const int src = (q + 1 + k) & 3;
                const int base = (((tile * 4 + q) * 4 + src) * 4 + wave) * 4096;
#pragma unroll
                for (int m = 0; m < 4; ++m) pv[k][m] = __builtin_amdgcn_raw_buffer_load_b128(rp, lane * 16 + m * 1024, base, 16);
            }
#pragma unroll
            for (int k = 0; k < 3; ++k)
#pragma unroll
                for (int m = 0; m < 4; ++m) own[m] += __builtin_bit_cast(f32x4, pv[k][m]);
            const int trow = t0 + (wave >> 1) * 64 + q * 16 + r;
#pragma unroll
            for (int u = 0; u < 2; ++u)
                *(u32x4*)(out + (size_t)trow * 1024 + e0 + (wave & 1) * 64 + 32 * u + 8 * g) = pack8(own[2 * u], own[2 * u + 1]);
            asm volatile("s_waitcnt vmcnt(0)":::"memory");
            if (wave==0) STAMP(13);
        }
    }
}

extern "C" void kernel_launch(void* const* d_in, const int* in_sizes, int n_in,
                              void* d_out, int out_size, void* d_ws, size_t ws_size,
                              hipStream_t stream) {
    const __bf16* xa = (const __bf16*)d_in[0];
    const __bf16* cre = (const __bf16*)d_in[1];
    const __bf16* cim = (const __bf16*)d_in[2];
    const __bf16* hre = (const __bf16*)d_in[3];
    const __bf16* him = (const __bf16*)d_in[4];
    const __bf16* xb = (const __bf16*)d_in[5];
    const __bf16* wqkv = (const __bf16*)d_in[6];
    const __bf16* wo = (const __bf16*)d_in[7];
    k1<<<256, 512, 0, stream>>>(xa, hre, him, xb, wqkv, (char*)d_ws);
    k2<<<256, 512, 0, stream>>>(cre, cim, wo, (char*)d_ws, (__bf16*)d_out);
}
```

```cpp
#include <hip/hip_runtime.h>
#include <hip/hip_bf16.h>
#include <stdint.h>

#define LAS __attribute__((address_space(3)))
#define GAS __attribute__((address_space(1)))
typedef __attribute__((ext_vector_type(8))) __bf16 bf16x8;
typedef __attribute__((ext_vector_type(4))) __bf16 bf16x4;
typedef __attribute__((ext_vector_type(4))) short s16x4;
typedef __attribute__((ext_vector_type(4))) float f32x4;
typedef __attribute__((ext_vector_type(8))) float f32x8;
typedef __attribute__((ext_vector_type(4))) unsigned u32x4;
typedef __attribute__((ext_vector_type(2))) unsigned u32x2;
typedef __amdgpu_buffer_rsrc_t rsrc_t;

#define NOSTAMPS 1
#define STAMP_N 16
#define STAMP_OFF (64u<<20)
#define STAMP(k) do{}while(0)
constexpr int SEQ = 2048, DM = 512;
constexpr int SLOT = 32768;

constexpr size_t WS_FLAGS = 0;
constexpr size_t WS_ABT   = 1u << 20;
constexpr size_t WS_O     = WS_ABT + (4u << 20);
constexpr size_t WS_PART  = WS_O + (2u << 20);

__device__ __forceinline__ rsrc_t mk_rsrc(const void* p, unsigned bytes) {
    return __builtin_amdgcn_make_buffer_rsrc((void*)p, 0, bytes, 0x00020000);
}
template <int AUX>
__device__ __forceinline__ void glds(rsrc_t r, LAS char* dst, int voff, int soff) {
    __builtin_amdgcn_raw_ptr_buffer_load_lds(r, (LAS void*)dst, 16, voff, soff, 0, AUX);
}
template <int N> __device__ __forceinline__ void bar_vm() {
    asm volatile("s_waitcnt vmcnt(%0)\n\ts_barrier" ::"n"(N) : "memory");
}
__device__ __forceinline__ void bar_lgkm() {
    asm volatile("s_waitcnt lgkmcnt(0)\n\ts_barrier" ::: "memory");
}
__device__ __forceinline__ f32x4 mfma32(bf16x8 a, bf16x8 b, f32x4 c) {
    return __builtin_amdgcn_mfma_f32_16x16x32_bf16(a, b, c, 0, 0, 0);
}
__device__ __forceinline__ f32x4 mfma16(bf16x4 a, bf16x4 b, f32x4 c) {
    return __builtin_amdgcn_mfma_f32_16x16x16bf16_1k(__builtin_bit_cast(s16x4, a), __builtin_bit_cast(s16x4, b), c, 0, 0, 0);
}
__device__ __forceinline__ bf16x8 lds_b128(LAS char* p) { return *(LAS bf16x8*)p; }
__device__ __forceinline__ bf16x8 lds_tr2(LAS char* p) {
    bf16x4 t0 = __builtin_amdgcn_ds_read_tr16_b64_v4bf16((LAS bf16x4*)p);
    bf16x4 t1 = __builtin_amdgcn_ds_read_tr16_b64_v4bf16((LAS bf16x4*)(p + 512));
    return __builtin_shufflevector(t0, t1, 0, 1, 2, 3, 4, 5, 6, 7);
}
__device__ __forceinline__ bf16x4 cvt4(f32x4 v) { return __builtin_convertvector(v, bf16x4); }
__device__ __forceinline__ u32x4 pack8(f32x4 lo, f32x4 hi) {
    bf16x4 a = cvt4(lo), b = cvt4(hi);
    bf16x8 c = __builtin_shufflevector(a, b, 0, 1, 2, 3, 4, 5, 6, 7);
    return __builtin_bit_cast(u32x4, c);
}
__device__ __forceinline__ float rbf(float x) { return (float)(__bf16)x; }
__device__ __forceinline__ float xmax16_32(float x) {
    unsigned u = __float_as_uint(x);
    auto r = __builtin_amdgcn_permlane16_swap(u, u, false, false);
    unsigned a = r[0], b = r[1];
    x = fmaxf(__uint_as_float(a), __uint_as_float(b));
    u = __float_as_uint(x);
    auto r2 = __builtin_amdgcn_permlane32_swap(u, u, false, false);
    a = r2[0]; b = r2[1];
    return fmaxf(__uint_as_float(a), __uint_as_float(b));
}
__device__ __forceinline__ float xsum16_32(float x) {
    unsigned u = __float_as_uint(x);
    auto r = __builtin_amdgcn_permlane16_swap(u, u, false, false);
    unsigned a = r[0], b = r[1];
    x = __uint_as_float(a) + __uint_as_float(b);
    u = __float_as_uint(x);
    auto r2 = __builtin_amdgcn_permlane32_swap(u, u, false, false);
    a = r2[0]; b = r2[1];
    return __uint_as_float(a) + __uint_as_float(b);
}

struct NTLoad {
    int va, vb;
};
__device__ __forceinline__ NTLoad nt_lane(int lane, int lw, int lda_b, int ldb_b) {
    NTLoad r;
    const int ch = ((lane & 7) ^ (4 * (lw & 1) + (lane >> 4))) * 16;
    r.va = (8 * (lane >> 5) + ((lane >> 3) & 3) + 16 * (lw & 1) + 4 * (lw >> 1)) * lda_b + ch;
    r.vb = (8 * lw + (lane >> 3)) * ldb_b + ch;
    return r;
}
template <int AUXA, int AUXB>
__device__ __forceinline__ void nt_issue(LAS char* slot, int lw, rsrc_t ra, rsrc_t rb, NTLoad nl, int sa, int sb, int lda_b, int ldb_b) {
#pragma unroll
    for (int u = 0; u < 4; ++u) {
        glds<AUXB>(rb, slot + 16384 + (lw + 4 * u) * 1024, nl.vb, sb + 32 * u * ldb_b);
        glds<AUXA>(ra, slot + (lw + 4 * u) * 1024, nl.va, sa + 32 * u * lda_b);
    }
}
struct Frag { bf16x8 a[4], b[4]; };
#define SGB() do { _Pragma("unroll") for (int _i = 0; _i < 8; ++_i) { __builtin_amdgcn_sched_group_barrier(0x8, 1, 0); __builtin_amdgcn_sched_group_barrier(0x100, 1, 0); } __builtin_amdgcn_sched_group_barrier(0x8, 8, 0); } while (0)
__device__ __forceinline__ void nt_read(Frag& f, LAS char* pa, LAS char* pb) {
    f.a[0] = lds_b128(pa);
#pragma unroll
    for (int n = 0; n < 4; ++n) f.b[n] = lds_b128(pb + n * 2048);
#pragma unroll
    for (int m = 1; m < 4; ++m) f.a[m] = lds_b128(pa + m * 2048);
}
__device__ __forceinline__ void nt_mma(f32x4 (&acc)[4][4], const Frag& f) {
#pragma unroll
    for (int m = 0; m < 4; ++m)
#pragma unroll
        for (int n = 0; n < 4; ++n) acc[m][n] = mfma32(f.a[m], f.b[n], acc[m][n]);
}
template <int NS, bool LASTBAR>
__device__ __forceinline__ int nt_consume(f32x4 (&acc)[4][4], LAS char* smem, int slot, int nst, int w, int lane) {
    const int r = lane & 15, g = lane >> 4;
    const int c0 = ((g ^ (r >> 1)) << 4), c1 = c0 ^ 64;
    const int oa = ((w & 1) * 64 + r) * 128, ob = 16384 + ((w >> 1) * 64 + r) * 128;
    Frag f0, f1;
    LAS char* s = smem + slot * SLOT;
    nt_read(f0, s + oa + c0, s + ob + c0);
#pragma unroll 1
    for (int it = 0; it < nst - 1; ++it) {
        __builtin_amdgcn_sched_barrier(0);
        nt_read(f1, s + oa + c1, s + ob + c1);
        nt_mma(acc, f0);
        SGB();
        __builtin_amdgcn_sched_barrier(0);
        slot = (slot + 1 == NS) ? 0 : slot + 1;
        s = smem + slot * SLOT;
        bar_lgkm();
        __builtin_amdgcn_sched_barrier(0);
        nt_read(f0, s + oa + c0, s + ob + c0);
        nt_mma(acc, f1);
        SGB();
        __builtin_amdgcn_sched_barrier(0);
    }
    nt_read(f1, s + oa + c1, s + ob + c1);
    nt_mma(acc, f0);
    SGB();
    __builtin_amdgcn_sched_barrier(0);
    slot = (slot + 1 == NS) ? 0 : slot + 1;
    if (LASTBAR) bar_lgkm();
    __builtin_amdgcn_sched_barrier(0);
    nt_mma(acc, f1);
    return slot;
}

constexpr int T2_SLOT = 43008;

__global__ __launch_bounds__(512, 2) void k1(const __bf16* __restrict__ xa, const __bf16* __restrict__ hre, const __bf16* __restrict__ him,
                                             const __bf16* __restrict__ xb, const __bf16* __restrict__ wqkv, char* __restrict__ ws) {
    __shared__ __attribute__((aligned(1024))) char smem_[163840];
    LAS char* smem = (LAS char*)smem_;
    const int tid = threadIdx.x, lane = tid & 63;
    const int wave = __builtin_amdgcn_readfirstlane(tid >> 6);
    const int bid = blockIdx.x, xcd = bid & 7, idx = bid >> 3;
    if (wave==0) STAMP(0);
    if (tid < 64) ((unsigned*)(ws + WS_FLAGS))[bid * 64 + tid] = 0u;

    if ((idx & 1) == 0) {
        const int q = xcd & 3, half = xcd >> 2, id = idx >> 1;
        const int st = (q & 1) * 8 + half * 4 + (id >> 2), et = id & 3;
        const int s0 = st * 128, e0 = et * 128;
        const __bf16* h = (q >> 1) ? him : hre;
        constexpr int NS = 5, NST = 8;
        if (wave >= 4) {
            const int lw = wave - 4;
            rsrc_t ra = mk_rsrc(xa, SEQ * DM * 2), rb = mk_rsrc(h, DM * DM * 2);
            NTLoad nl = nt_lane(lane, lw, 1024, 1024);
            int sa = s0 * 1024, sb = e0 * 1024;
#pragma unroll
            for (int i = 0; i < NS - 1; ++i) nt_issue<0, 0>(smem + i * SLOT, lw, ra, rb, nl, sa + i * 128, sb + i * 128, 1024, 1024);
            int slot = NS - 1;
#pragma unroll 1
            for (int gi = 0; gi < NST - (NS - 1); ++gi) {
                bar_vm<8 * (NS - 2)>();
                nt_issue<0, 0>(smem + slot * SLOT, lw, ra, rb, nl, sa + (gi + NS - 1) * 128, sb + (gi + NS - 1) * 128, 1024, 1024);
                slot = (slot + 1 == NS) ? 0 : slot + 1;
            }
            bar_vm<24>(); bar_vm<16>(); bar_vm<8>(); bar_vm<0>();
        } else {
            f32x4 acc[4][4] = {};
            bar_lgkm();
            if (wave==0) STAMP(1);
            nt_consume<NS, false>(acc, smem, 0, NST, wave, lane);
            if (wave==0) STAMP(2);
            const float sg = (q >> 1) ? -1.f : 1.f;
            __bf16* abT = (__bf16*)(ws + WS_ABT);
            const int r = lane & 15, g = lane >> 4;
#pragma unroll
            for (int n = 0; n < 4; ++n) {
                const int erow = (q >> 1) * 512 + e0 + (wave >> 1) * 64 + n * 16 + r;
#pragma unroll
                for (int u = 0; u < 2; ++u) {
                    u32x4 v = pack8(acc[2 * u][n] * sg, acc[2 * u + 1][n] * sg);
                    *(u32x4*)(abT + (size_t)erow * SEQ + s0 + (wave & 1) * 64 + 32 * u + 8 * g) = v;
                }
            }
            asm volatile("s_waitcnt vmcnt(0)":::"memory");
            if (wave==0) STAMP(3);
        }
    } else {
        const int id = idx >> 1;
        const int rt = (xcd >> 1) * 4 + (id >> 2), hg = (xcd & 1) * 4 + (id & 3);
        const int s0 = rt * 128;
        constexpr int NS = 3, NST = 8;
        if (wave >= 4) {
            const int lw = wave - 4;
            rsrc_t rx = mk_rsrc(xb, SEQ * DM * 2), rw = mk_rsrc(wqkv, DM * 3 * DM * 2);
            const int vx = (lane >> 3) * 1024 + (((lane & 7) ^ (4 * (lw & 1) + (lane >> 4))) * 16);
            const int vw = (lane >> 3) * 3072 + (((lane & 7) ^ (2 * (((lane >> 4) & 1) | ((lw & 1) << 1)))) * 16);
            const int sxm = (s0 + 8 * lw) * 1024;
            const int sxh = ((s0 >= 16 ? s0 - 16 : 0) + 8 * lw) * 1024;
            const int sw = (8 * lw) * 3072 + hg * 128;
            auto issue = [&](int slot, int kt) {
                LAS char* sl = smem + slot * T2_SLOT;
#pragma unroll
                for (int v = 0; v < 2; ++v)
#pragma unroll
                    for (int cg = 0; cg < 3; ++cg)
                        glds<0>(rw, sl + 18432 + ((lw + 4 * v) * 3 + cg) * 1024, vw, sw + (kt * 64 + 32 * v) * 3072 + cg * 1024);
#pragma unroll
                for (int u = 0; u < 4; ++u) glds<0>(rx, sl + 2048 + (lw + 4 * u) * 1024, vx, sxm + 32 * u * 1024 + kt * 128);
                if (lw < 2) glds<0>(rx, sl + lw * 1024, vx, sxh + kt * 128);
            };
            issue(0, 0); issue(1, 1);
            int slot = 2;
#pragma unroll 1
            for (int gi = 0; gi < NST - 2; ++gi) {
                bar_vm<10>();
                issue(slot, gi + 2);
                slot = (slot + 1 == NS) ? 0 : slot + 1;
            }
            bar_vm<10>(); bar_vm<0>();
        } else {
            const int r = lane & 15, g = lane >> 4;
            const int c0 = ((g ^ (r >> 1)) << 4);
            const int ox = r * 128;
            const int qq = (lane & 15) >> 2, p = lane & 3;
            const int ow = 18432 + g * 3072 + qq * 128 + (((2 * wave + (p >> 1)) ^ (2 * ((qq >> 1) | ((g & 1) << 1)))) << 4) + 8 * (p & 1);
            f32x4 aq[8] = {}, ak[9] = {}, av[9] = {};
            int slot = 0;
#pragma unroll 1
            for (int it = 0; it < NST; ++it) {
                bar_lgkm();
                LAS char* sl = smem + slot * T2_SLOT;
#pragma unroll
                for (int kk = 0; kk < 2; ++kk) {
                    bf16x8 wq = lds_tr2(sl + ow + kk * 12288), wk = lds_tr2(sl + ow + kk * 12288 + 1024), wv = lds_tr2(sl + ow + kk * 12288 + 2048);
#pragma unroll
                    for (int rb = 0; rb < 9; ++rb) {
                        bf16x8 xf = lds_b128(sl + ox + (c0 ^ (kk * 64)) + rb * 2048);
                        if (rb > 0) aq[rb - 1] = mfma32(wq, xf, aq[rb - 1]);
                        ak[rb] = mfma32(wk, xf, ak[rb]);
                        av[rb] = mfma32(xf, wv, av[rb]);
                    }
                }
                slot = (slot + 1 == NS) ? 0 : slot + 1;
            }
            if (wave==0) STAMP(4);
            const int j = r, par = j & 1;
            const bool uc0 = (4 * g + par) <= j, uc1 = (4 * g + par + 2) <= j;
            __bf16* o = (__bf16*)(ws + WS_O);
            const int head = hg * 4 + wave;
            bf16x4 kp = cvt4(ak[0]), vp = cvt4(av[0]);
            const f32x4 z = {0.f, 0.f, 0.f, 0.f};
#pragma unroll
            for (int rb = 1; rb < 9; ++rb) {
                bf16x4 kc = cvt4(ak[rb]), vc = cvt4(av[rb]), qb = cvt4(aq[rb - 1]);
                f32x4 sc = mfma16(kc, qb, z), sp = mfma16(kp, qb, z);
                float c0v = par ? sc[1] : sc[0], c1v = par ? sc[3] : sc[2];
                float p0v = par ? sp[1] : sp[0], p1v = par ? sp[3] : sp[2];
                if (rb == 1 && s0 == 0) { p0v = -4e9f; p1v = -4e9f; }
                float l0 = rbf(uc0 ? c0v : p0v) * 0.25f, l1 = rbf(uc1 ? c1v : p1v) * 0.25f;
                float m = xmax16_32(fmaxf(l0, l1));
                float e0 = __expf(l0 - m), e1 = __expf(l1 - m);
                float inv = __builtin_amdgcn_rcpf(xsum16_32(e0 + e1));
                __bf16 b0 = (__bf16)(e0 * inv), b1 = (__bf16)(e1 * inv);
                unsigned t0 = (unsigned)__builtin_bit_cast(unsigned short, b0) << (16 * par);
                unsigned t1 = (unsigned)__builtin_bit_cast(unsigned short, b1) << (16 * par);
                u32x2 pc = {uc0 ? t0 : 0u, uc1 ? t1 : 0u}, pp = {uc0 ? 0u : t0, uc1 ? 0u : t1};
                f32x4 ov = mfma16(vc, __builtin_bit_cast(bf16x4, pc), z);
                ov = mfma16(vp, __builtin_bit_cast(bf16x4, pp), ov);
                bf16x4 ob = cvt4(ov);
                *(bf16x4*)(o + (size_t)(s0 + (rb - 1) * 16 + j) * DM + head * 16 + 4 * g) = ob;
                kp = kc; vp = vc;
            }
            asm volatile("s_waitcnt vmcnt(0)":::"memory");
            if (wave==0) STAMP(5);
        }
    }
}

__global__ __launch_bounds__(512, 2) void k2(const __bf16* __restrict__ cre, const __bf16* __restrict__ cim, const __bf16* __restrict__ wo,
                                             char* __restrict__ ws, __bf16* __restrict__ out) {
    __shared__ __attribute__((aligned(1024))) char smem_[163840];
    LAS char* smem = (LAS char*)smem_;
    const int tid = threadIdx.x, lane = tid & 63;
    const int wave = __builtin_amdgcn_readfirstlane(tid >> 6);
    const int bid = blockIdx.x, xcd = bid & 7, idx = bid >> 3;
    const int q = xcd & 3, tt = (xcd >> 2) * 8 + (idx >> 2), et = idx & 3;
    const int t0 = tt * 128, e0 = et * 128;
    const int s4 = (xcd * 4 + (idx >> 3)) * 64, n4 = (idx & 7) * 64;
    constexpr int NS = 5, NST3 = 16, NST4 = 4;
    if (wave==0) STAMP(8);
    if (wave >= 4) {
        const int lw = wave - 4;
        rsrc_t ra = mk_rsrc(ws + WS_ABT, 1024 * SEQ * 2), rb = mk_rsrc((q >> 1) ? cim : cre, SEQ * SEQ * 2);
        rsrc_t ro = mk_rsrc(ws + WS_O, SEQ * DM * 2), rw = mk_rsrc(wo, DM * DM * 2);
        NTLoad nl = nt_lane(lane, lw, 4096, 4096);
        const int sa = ((q >> 1) * 512 + e0) * 4096 + (q & 1) * 2048, sb = t0 * 4096 + (q & 1) * 2048;
        const int vo = (lane >> 3) * 1024 + (((lane & 7) ^ (4 * (lw & 1) + (lane >> 4))) * 16);
        const int vw = (lane >> 3) * 1024 + (((lane & 7) ^ (2 * (((lane >> 4) & 1) | ((lw & 1) << 1)))) * 16);
        auto issue4 = [&](int slot, int st) {
            LAS char* sl = smem + slot * SLOT;
#pragma unroll
            for (int h = 0; h < 2; ++h) {
                const int kt = st * 2 + h;
#pragma unroll
                for (int u = 0; u < 2; ++u) {
                    glds<0>(rw, sl + h * 16384 + 8192 + (lw + 4 * u) * 1024, vw, (kt * 64 + 8 * lw + 32 * u) * 1024 + n4 * 2);
                    glds<0>(ro, sl + h * 16384 + (lw + 4 * u) * 1024, vo, (s4 + 8 * lw + 32 * u) * 1024 + kt * 128);
                }
            }
        };
#pragma unroll
        for (int i = 0; i < NS - 1; ++i) nt_issue<0, 0>(smem + i * SLOT, lw, ra, rb, nl, sa + i * 128, sb + i * 128, 4096, 4096);
        int slot = NS - 1;
#pragma unroll 1
        for (int gi = 0; gi < NST3 - (NS - 1); ++gi) {
            bar_vm<8 * (NS - 2)>();
            nt_issue<0, 0>(smem + slot * SLOT, lw, ra, rb, nl, sa + (gi + NS - 1) * 128, sb + (gi + NS - 1) * 128, 4096, 4096);
            slot = (slot + 1 == NS) ? 0 : slot + 1;
        }
#pragma unroll 1
        for (int gi = 0; gi < NST4; ++gi) {
            bar_vm<8 * (NS - 2)>();
            issue4(slot, gi);
            slot = (slot + 1 == NS) ? 0 : slot + 1;
        }
        bar_vm<24>(); bar_vm<16>(); bar_vm<8>(); bar_vm<0>();
    } else {
        const int r = lane & 15, g = lane >> 4;
        f32x4 acc[4][4] = {};
        bar_lgkm();
        if (wave==0) STAMP(9);
        int slot = nt_consume<NS, true>(acc, smem, 0, NST3, wave, lane);
        if (wave==0) STAMP(10);
        const int tile = tt * 4 + et;
        {
            rsrc_t rp = mk_rsrc(ws + WS_PART, 64 * 16 * 4 * 4096);
#pragma unroll
            for (int n = 0; n < 4; ++n) {
                if (n != q) {
                    const int base = (((tile * 4 + n) * 4 + q) * 4 + wave) * 4096;
#pragma unroll
                    for (int m = 0; m < 4; ++m)
                        __builtin_amdgcn_raw_buffer_store_b128(__builtin_bit_cast(u32x4, acc[m][n]), rp, lane * 16 + m * 1024, base, 16);
                }
            }
        }
        f32x4 a4[2][2] = {};
        {
            const int c0 = ((g ^ (r >> 1)) << 4);
            const int oo = ((wave >> 1) * 32 + r) * 128;
            const int qq = (lane & 15) >> 2, p = lane & 3;
            const int swz = 2 * ((qq >> 1) | ((g & 1) << 1));
            const int ow0 = 8192 + g * 1024 + qq * 128 + ((((wave & 1) * 4 + (p >> 1)) ^ swz) << 4) + 8 * (p & 1);
            const int ow1 = 8192 + g * 1024 + qq * 128 + ((((wave & 1) * 4 + 2 + (p >> 1)) ^ swz) << 4) + 8 * (p & 1);
#pragma unroll 1
            for (int st = 0; st < NST4; ++st) {
                LAS char* sl = smem + slot * SLOT;
#pragma unroll
                for (int h = 0; h < 2; ++h)
#pragma unroll
                    for (int kk = 0; kk < 2; ++kk) {
                        bf16x8 w0 = lds_tr2(sl + h * 16384 + ow0 + kk * 4096), w1 = lds_tr2(sl + h * 16384 + ow1 + kk * 4096);
                        bf16x8 o0 = lds_b128(sl + h * 16384 + oo + (c0 ^ (kk * 64))), o1 = lds_b128(sl + h * 16384 + oo + (c0 ^ (kk * 64)) + 2048);
                        a4[0][0] = mfma32(w0, o0, a4[0][0]);
                        a4[0][1] = mfma32(w0, o1, a4[0][1]);
                        a4[1][0] = mfma32(w1, o0, a4[1][0]);
                        a4[1][1] = mfma32(w1, o1, a4[1][1]);
                    }
                slot = (slot + 1 == NS) ? 0 : slot + 1;
                if (st == 0) {
                    asm volatile("s_waitcnt vmcnt(0)" ::: "memory");
                    if (lane < 4 && lane != q) {
                        GAS unsigned* f = (GAS unsigned*)(ws + WS_FLAGS) + ((tile * 4 + lane) * 4 + wave) * 16 + q;
                        __hip_atomic_store(f, 1u, __ATOMIC_RELAXED, __HIP_MEMORY_SCOPE_AGENT);
                    }
                }
                if (st + 1 < NST4) bar_lgkm();
            }
#pragma unroll
            for (int m = 0; m < 2; ++m)
#pragma unroll
                for (int n = 0; n < 2; ++n)
                    *(bf16x4*)(out + (size_t)(s4 + (wave >> 1) * 32 + n * 16 + r) * 1024 + 512 + n4 + (wave & 1) * 32 + m * 16 + 4 * g) = cvt4(a4[m][n]);
        }
        if (wave==0) STAMP(11);
        {
            GAS unsigned* f = (GAS unsigned*)(ws + WS_FLAGS) + ((tile * 4 + q) * 4 + wave) * 16 + (lane & 3);
            unsigned spins = 0;
            for (;;) {
                unsigned v = __hip_atomic_load(f, __ATOMIC_RELAXED, __HIP_MEMORY_SCOPE_AGENT);
                bool ok = (v == 1u) || ((lane & 3) == q);
                if (__all(ok)) break;
                if (++spins > (1u << 22)) break;
                __builtin_amdgcn_s_sleep(1);
            }
            asm volatile("" ::: "memory");
            if (wave==0) STAMP(12);
            rsrc_t rp = mk_rsrc(ws + WS_PART, 64 * 16 * 4 * 4096);
            f32x4 own[4];
#pragma unroll
            for (int m = 0; m < 4; ++m) own[m] = (q == 0) ? acc[m][0] : (q == 1) ? acc[m][1] : (q == 2) ? acc[m][2] : acc[m][3];
            u32x4 pv[3][4];
#pragma unroll
            for (int k = 0; k < 3; ++k) {
                const int src = (q + 1 + k) & 3;
                const int base = (((tile * 4 + q) * 4 + src) * 4 + wave) * 4096;
#pragma unroll
                for (int m = 0; m < 4; ++m) pv[k][m] = __builtin_amdgcn_raw_buffer_load_b128(rp, lane * 16 + m * 1024, base, 16);
            }
#pragma unroll
            for (int k = 0; k < 3; ++k)
#pragma unroll
                for (int m = 0; m < 4; ++m) own[m] += __builtin_bit_cast(f32x4, pv[k][m]);
            const int trow = t0 + (wave >> 1) * 64 + q * 16 + r;
#pragma unroll
            for (int u = 0; u < 2; ++u)
                *(u32x4*)(out + (size_t)trow * 1024 + e0 + (wave & 1) * 64 + 32 * u + 8 * g) = pack8(own[2 * u], own[2 * u + 1]);
            asm volatile("s_waitcnt vmcnt(0)":::"memory");
            if (wave==0) STAMP(13);
        }
    }
}

__global__ void k_warmcode(unsigned* p) {
    unsigned long long pc; int off;
    asm volatile("s_getpc_b64 %0\n.Lhere_wc:\n\ts_mov_b32 %1, .Lhere_wc-_Z2k1PKDF16bS0_S0_S0_S0_Pc" : "=s"(pc), "=s"(off));
    const unsigned* base = (const unsigned*)(pc - (unsigned)off);
    unsigned acc = 0;
    for (int i = threadIdx.x * 16; i * 4 < off; i += 256 * 16) acc ^= base[i];
    if (acc == 0x12345678u) p[0] = acc;
}
extern "C" void kernel_launch(void* const* d_in, const int* in_sizes, int n_in,
                              void* d_out, int out_size, void* d_ws, size_t ws_size,
                              hipStream_t stream) {
    const __bf16* xa = (const __bf16*)d_in[0];
    const __bf16* cre = (const __bf16*)d_in[1];
    const __bf16* cim = (const __bf16*)d_in[2];
    const __bf16* hre = (const __bf16*)d_in[3];
    const __bf16* him = (const __bf16*)d_in[4];
    const __bf16* xb = (const __bf16*)d_in[5];
    const __bf16* wqkv = (const __bf16*)d_in[6];
    const __bf16* wo = (const __bf16*)d_in[7];
    k_warmcode<<<64, 256, 0, stream>>>((unsigned*)((char*)d_ws + (100u<<20)));
    k1<<<256, 512, 0, stream>>>(xa, hre, him, xb, wqkv, (char*)d_ws);
    k2<<<256, 512, 0, stream>>>(cre, cim, wo, (char*)d_ws, (__bf16*)d_out);
}
```

```cpp
#include <hip/hip_runtime.h>
#include <hip/hip_bf16.h>
#include <stdint.h>

#define LAS __attribute__((address_space(3)))
#define GAS __attribute__((address_space(1)))
typedef __attribute__((ext_vector_type(8))) __bf16 bf16x8;
typedef __attribute__((ext_vector_type(4))) __bf16 bf16x4;
typedef __attribute__((ext_vector_type(4))) short s16x4;
typedef __attribute__((ext_vector_type(4))) float f32x4;
typedef __attribute__((ext_vector_type(4))) unsigned u32x4;
typedef __attribute__((ext_vector_type(2))) unsigned u32x2;
typedef __amdgpu_buffer_rsrc_t rsrc_t;

#define NOSTAMPS 1
#define STAMP_N 16
#define STAMP_OFF (64u<<20)
#define STAMP(k) do{}while(0)

constexpr int SEQ = 2048, DM = 512;
constexpr int SLOT = 32768;
constexpr int T2_SLOT = 43008;
constexpr int T2_BASE = 32768;

constexpr unsigned WS_FLAGS = 0;
constexpr unsigned WS_ABT   = 1u << 20;
constexpr unsigned WS_O     = WS_ABT + (4u << 20);
constexpr unsigned WS_PART  = WS_O + (2u << 20);
constexpr int FW = 32;
constexpr int F1_IDX = 1, F2_IDX = 17, F3_IDX = 33, NFLAGS = 33 + 256;
constexpr int NT = 2;
constexpr unsigned SPIN_CAP = 1u << 17;

__device__ __forceinline__ rsrc_t mk_rsrc(const void* p, unsigned bytes) {
    return __builtin_amdgcn_make_buffer_rsrc((void*)p, 0, bytes, 0x00020000);
}
template <int AUX>
__device__ __forceinline__ void glds(rsrc_t r, LAS char* dst, int voff, int soff) {
    __builtin_amdgcn_raw_ptr_buffer_load_lds(r, (LAS void*)dst, 16, voff, soff, 0, AUX);
}
template <int N> __device__ __forceinline__ void bar_vm() {
    asm volatile("s_waitcnt vmcnt(%0)\n\ts_barrier" ::"n"(N) : "memory");
}
__device__ __forceinline__ void bar_only() { asm volatile("s_barrier" ::: "memory"); }
__device__ __forceinline__ void bar_lgkm() {
    asm volatile("s_waitcnt lgkmcnt(0)\n\ts_barrier" ::: "memory");
}
__device__ __forceinline__ void drain_vm() { asm volatile("s_waitcnt vmcnt(0)" ::: "memory"); }
__device__ __forceinline__ f32x4 mfma32(bf16x8 a, bf16x8 b, f32x4 c) {
    return __builtin_amdgcn_mfma_f32_16x16x32_bf16(a, b, c, 0, 0, 0);
}
__device__ __forceinline__ f32x4 mfma16(bf16x4 a, bf16x4 b, f32x4 c) {
    return __builtin_amdgcn_mfma_f32_16x16x16bf16_1k(__builtin_bit_cast(s16x4, a), __builtin_bit_cast(s16x4, b), c, 0, 0, 0);
}
__device__ __forceinline__ bf16x8 lds_b128(LAS char* p) { return *(LAS bf16x8*)p; }
__device__ __forceinline__ bf16x8 lds_tr2(LAS char* p) {
    bf16x4 t0 = __builtin_amdgcn_ds_read_tr16_b64_v4bf16((LAS bf16x4*)p);
    bf16x4 t1 = __builtin_amdgcn_ds_read_tr16_b64_v4bf16((LAS bf16x4*)(p + 512));
    return __builtin_shufflevector(t0, t1, 0, 1, 2, 3, 4, 5, 6, 7);
}
__device__ __forceinline__ bf16x4 cvt4(f32x4 v) { return __builtin_convertvector(v, bf16x4); }
__device__ __forceinline__ u32x4 pack8(f32x4 lo, f32x4 hi) {
    bf16x4 a = cvt4(lo), b = cvt4(hi);
    bf16x8 c = __builtin_shufflevector(a, b, 0, 1, 2, 3, 4, 5, 6, 7);
    return __builtin_bit_cast(u32x4, c);
}
__device__ __forceinline__ void unpack8_add(u32x4 v, f32x4& lo, f32x4& hi) {
    unsigned a = v[0], b = v[1], c = v[2], d = v[3];
    lo[0] += __uint_as_float(a << 16); lo[1] += __uint_as_float(a & 0xffff0000u);
    lo[2] += __uint_as_float(b << 16); lo[3] += __uint_as_float(b & 0xffff0000u);
    hi[0] += __uint_as_float(c << 16); hi[1] += __uint_as_float(c & 0xffff0000u);
    hi[2] += __uint_as_float(d << 16); hi[3] += __uint_as_float(d & 0xffff0000u);
}
__device__ __forceinline__ float rbf(float x) { return (float)(__bf16)x; }
__device__ __forceinline__ float xmax16_32(float x) {
    unsigned u = __float_as_uint(x);
    auto r = __builtin_amdgcn_permlane16_swap(u, u, false, false);
    unsigned a = r[0], b = r[1];
    x = fmaxf(__uint_as_float(a), __uint_as_float(b));
    u = __float_as_uint(x);
    auto r2 = __builtin_amdgcn_permlane32_swap(u, u, false, false);
    a = r2[0]; b = r2[1];
    return fmaxf(__uint_as_float(a), __uint_as_float(b));
}
__device__ __forceinline__ float xsum16_32(float x) {
    unsigned u = __float_as_uint(x);
    auto r = __builtin_amdgcn_permlane16_swap(u, u, false, false);
    unsigned a = r[0], b = r[1];
    x = __uint_as_float(a) + __uint_as_float(b);
    u = __float_as_uint(x);
    auto r2 = __builtin_amdgcn_permlane32_swap(u, u, false, false);
    a = r2[0]; b = r2[1];
    return __uint_as_float(a) + __uint_as_float(b);
}
__device__ __forceinline__ unsigned ld_flag(GAS unsigned* f) {
    return __builtin_amdgcn_readfirstlane(__hip_atomic_load(f, __ATOMIC_RELAXED, __HIP_MEMORY_SCOPE_AGENT));
}
__device__ __forceinline__ void add_flag(GAS unsigned* f, int lane) {
    if (lane == 0) (void)__hip_atomic_fetch_add(f, 1u, __ATOMIC_RELAXED, __HIP_MEMORY_SCOPE_AGENT);
}
#define LD_FLAG_ASYNC(REG, f) asm volatile("global_load_dword " REG ", %0, off sc1" ::"v"(f) : "memory", REG)
#define RD_FLAG(REG, out) asm volatile("v_readfirstlane_b32 %0, " REG : "=s"(out)::"memory")
__device__ __forceinline__ void add_flag_async(GAS unsigned* f, int lane) {
    unsigned one = 1u;
    if (lane == 0) asm volatile("global_atomic_add %0, %1, off" ::"v"(f), "v"(one) : "memory");
}
__device__ __forceinline__ void wait_ge(GAS unsigned* f, unsigned target, GAS unsigned* hb, int lane) {
    unsigned spins = 0;
    while (ld_flag(f) < target) {
        ++spins;
        if ((spins & 31u) == 0u) {
            add_flag(hb, lane);
            if (spins > SPIN_CAP) break;
        }
        __builtin_amdgcn_s_sleep(1);
    }
    asm volatile("" ::: "memory");
}
struct Frag { bf16x8 a[4], b[4]; };
#define SGB() do { _Pragma("unroll") for (int _i = 0; _i < 8; ++_i) { __builtin_amdgcn_sched_group_barrier(0x8, 1, 0); __builtin_amdgcn_sched_group_barrier(0x100, 1, 0); } __builtin_amdgcn_sched_group_barrier(0x8, 8, 0); } while (0)
__device__ __forceinline__ void nt_read(Frag& f, LAS char* pa, LAS char* pb) {
    f.a[0] = lds_b128(pa);
#pragma unroll
    for (int n = 0; n < 4; ++n) f.b[n] = lds_b128(pb + n * 2048);
#pragma unroll
    for (int m = 1; m < 4; ++m) f.a[m] = lds_b128(pa + m * 2048);
}
__device__ __forceinline__ void nt_mma(f32x4 (&acc)[4][4], const Frag& f) {
#pragma unroll
    for (int m = 0; m < 4; ++m)
#pragma unroll
        for (int n = 0; n < 4; ++n) acc[m][n] = mfma32(f.a[m], f.b[n], acc[m][n]);
}
__device__ __forceinline__ int nt_consume(f32x4 (&acc)[4][4], LAS char* smem, int slot, int nst, int w, int lane) {
    const int r = lane & 15, g = lane >> 4;
    const int c0 = ((g ^ (r >> 1)) << 4), c1 = c0 ^ 64;
    const int oa = ((w & 1) * 64 + r) * 128, ob = 16384 + ((w >> 1) * 64 + r) * 128;
    Frag f0, f1;
    LAS char* s = smem + slot * SLOT;
    nt_read(f0, s + oa + c0, s + ob + c0);
#pragma unroll 1
    for (int it = 0; it < nst - 1; ++it) {
        __builtin_amdgcn_sched_barrier(0);
        nt_read(f1, s + oa + c1, s + ob + c1);
        nt_mma(acc, f0);
        SGB();
        __builtin_amdgcn_sched_barrier(0);
        slot = (slot + 1 == 5) ? 0 : slot + 1;
        s = smem + slot * SLOT;
        bar_lgkm();
        __builtin_amdgcn_sched_barrier(0);
        nt_read(f0, s + oa + c0, s + ob + c0);
        nt_mma(acc, f1);
        SGB();
        __builtin_amdgcn_sched_barrier(0);
    }
    nt_read(f1, s + oa + c1, s + ob + c1);
    nt_mma(acc, f0);
    SGB();
    __builtin_amdgcn_sched_barrier(0);
    slot = (slot + 1 == 5) ? 0 : slot + 1;
    bar_lgkm();
    __builtin_amdgcn_sched_barrier(0);
    nt_mma(acc, f1);
    return slot;
}

struct Frag2 { bf16x8 x[9], w[3]; };
__device__ __forceinline__ void t2_read(Frag2& f, LAS char* px, LAS char* pw) {
    f.w[1] = lds_tr2(pw + 1024);
    f.x[0] = lds_b128(px);
    f.w[2] = lds_tr2(pw + 2048);
    f.w[0] = lds_tr2(pw);
#pragma unroll
    for (int rb = 1; rb < 9; ++rb) f.x[rb] = lds_b128(px + rb * 2048);
}
__device__ __forceinline__ void t2_mma(f32x4 (&aq)[8], f32x4 (&ak)[9], f32x4 (&av)[9], const Frag2& f) {
#pragma unroll
    for (int rb = 0; rb < 9; ++rb) {
        ak[rb] = mfma32(f.w[1], f.x[rb], ak[rb]);
        av[rb] = mfma32(f.x[rb], f.w[2], av[rb]);
        if (rb > 0) aq[rb - 1] = mfma32(f.w[0], f.x[rb], aq[rb - 1]);
    }
}
#define SGB2() do { _Pragma("unroll") for (int _i = 0; _i < 15; ++_i) { __builtin_amdgcn_sched_group_barrier(0x8, 1, 0); __builtin_amdgcn_sched_group_barrier(0x100, 1, 0); } __builtin_amdgcn_sched_group_barrier(0x8, 11, 0); } while (0)

__global__ __launch_bounds__(512, 2) void fused(const __bf16* __restrict__ xa, const __bf16* __restrict__ cre, const __bf16* __restrict__ cim,
                                                const __bf16* __restrict__ hre, const __bf16* __restrict__ him, const __bf16* __restrict__ xb,
                                                const __bf16* __restrict__ wqkv, const __bf16* __restrict__ wo,
                                                char* __restrict__ ws, __bf16* __restrict__ out) {
    __shared__ __attribute__((aligned(1024))) char smem_[163840];
    LAS char* smem = (LAS char*)smem_;
    const int tid = threadIdx.x, lane = tid & 63;
    const int wave = __builtin_amdgcn_readfirstlane(tid >> 6);
    const int bid = blockIdx.x, xcd = bid & 7, idx = bid >> 3;
    const int q = xcd & 3, th = xcd >> 2;
    const bool typeA = q < 2;
    const int ttl = idx >> 2, et = idx & 3, tt = th * 8 + ttl;
    const int t0 = tt * 128, e0 = et * 128;
    const int pg = (q & 1) + 2 * th;
    const int s1 = typeA ? ((pg & 1) * 8 + ttl) * 128 : ((pg >> 1) * 8 + ttl) * 128;
    const int hg = (pg & 1) * 4 + et;
    const int s4 = (xcd * 4 + (idx >> 3)) * 64, n4 = (idx & 7) * 64;
    GAS unsigned* flags = (GAS unsigned*)(ws + WS_FLAGS);
    const int slot3 = typeA ? 3 : 0;
    const int rot1 = (ttl + 2 * et) & 7, rot3 = (2 * ttl + 4 * et) & 15;

    if (wave >= 6) {
        const int lw = wave - 6;
        const int row = 8 * lw + (lane >> 3);
        const int ch = ((lane & 7) ^ (4 * lw + (lane >> 4))) * 16;
        const int cht = ((lane & 7) ^ (2 * (((lane >> 4) & 1) | (lw << 1)))) * 16;
        const bool master = (bid == 0 && lw == 0);
        rsrc_t rc = mk_rsrc((q >> 1) ? cim : cre, SEQ * SEQ * 2);
        const int vb = row * 4096 + ch;
        const int sb = t0 * 4096 + (q & 1) * 2048;
        auto issueC = [&](int st, int slot) {
            const int so = sb + ((st + rot3) & 15) * 128;
#pragma unroll
            for (int u = 0; u < 8; ++u) glds<NT>(rc, smem + slot * SLOT + 16384 + (lw + 2 * u) * 1024, vb, so + 16 * ((u & 4) | ((u + q) & 3)) * 4096);
        };
        int slot = 0;
        if (typeA) {
            rsrc_t rh = mk_rsrc((pg >> 1) ? him : hre, DM * DM * 2);
            const int vh = row * 1024 + ch;
#pragma unroll 1
            for (int st = 0; st < 8; ++st) {
                if (st == 5) bar_vm<32>();
                if (st >= 5) { bar_vm<24>(); if (master) add_flag_async(flags, lane); }
                const int so = e0 * 1024 + ((st + rot1) & 7) * 128;
#pragma unroll
                for (int u = 0; u < 8; ++u) glds<NT>(rh, smem + slot * SLOT + 16384 + (lw + 2 * u) * 1024, vh, so + 16 * u * 1024);
                slot = (slot + 1 == 5) ? 0 : slot + 1;
            }
        } else {
            rsrc_t rq = mk_rsrc(wqkv, DM * 3 * DM * 2);
            const int vq = row * 3072 + cht;
            int pos = 1;
#pragma unroll 1
            for (int st = 0; st < 8; ++st) {
                if (st == 3) bar_vm<24>();
                if (st >= 3) bar_vm<12>();
                LAS char* sl = smem + T2_BASE + pos * T2_SLOT;
                const int so = ((st + rot1) & 7) * 64 * 3072 + hg * 128;
#pragma unroll
                for (int v = 0; v < 4; ++v)
#pragma unroll
                    for (int cg = 0; cg < 3; ++cg)
                        glds<NT>(rq, sl + 18432 + ((lw + 2 * v) * 3 + cg) * 1024, vq, so + 16 * v * 3072 + cg * 1024);
                pos = (pos + 1 == 3) ? 0 : pos + 1;
            }
        }
#pragma unroll 1
        for (int st = 0; st < 16; ++st) {
            if (st >= 5) {
                if (st == 5) bar_vm<32>();
                bar_vm<24>();
            } else if (typeA) {
                if (st < 4) bar_vm<24>(); else bar_only();
                if (master) add_flag_async(flags, lane);
            } else {
                if (st == 1) bar_vm<20>();
                if (st == 2) bar_vm<16>();
                if (st == 3) bar_only();
            }
            issueC(st, slot);
            slot = (slot + 1 == 5) ? 0 : slot + 1;
        }
        rsrc_t rw = mk_rsrc(wo, DM * DM * 2);
        const int vw = row * 1024 + cht;
#pragma unroll 1
        for (int st = 0; st < 4; ++st) {
            bar_vm<24>();
#pragma unroll
            for (int h = 0; h < 2; ++h)
#pragma unroll
                for (int u = 0; u < 4; ++u)
                    glds<NT>(rw, smem + slot * SLOT + h * 16384 + 8192 + (lw + 2 * u) * 1024, vw, ((st * 2 + h) * 64 + 16 * u) * 1024 + n4 * 2);
            slot = (slot + 1 == 5) ? 0 : slot + 1;
        }
        bar_vm<24>(); bar_vm<16>(); bar_vm<8>(); bar_vm<0>();
    } else if (wave >= 4) {
        const int lw = wave - 4;
        const int ch = ((lane & 7) ^ (4 * lw + (lane >> 4))) * 16;
        const int prow = 8 * (lane >> 5) + ((lane >> 3) & 3) + 16 * lw;
        const int row = 8 * lw + (lane >> 3);
        if (typeA) {
            rsrc_t rx = mk_rsrc(xa, SEQ * DM * 2);
            const int va = prow * 1024 + ch;
            int sl = 0;
#pragma unroll 1
            for (int st = 0; st < 8; ++st) {
                if (st == 5) { LD_FLAG_ASYNC("v254", flags); bar_vm<33>(); }
                if (st >= 5) bar_vm<24>();
                const int so = s1 * 1024 + ((st + rot1) & 7) * 128;
#pragma unroll
                for (int u = 0; u < 8; ++u) glds<NT>(rx, smem + sl * SLOT + (lw + 2 * u) * 1024, va, so + (32 * (u >> 1) + 4 * (u & 1)) * 1024);
                sl = (sl + 1 == 5) ? 0 : sl + 1;
            }
            bar_vm<24>(); bar_vm<16>(); bar_vm<8>();
        } else {
            rsrc_t rx = mk_rsrc(xb, SEQ * DM * 2);
            const int vx = row * 1024 + ch;
            const int sxm = s1 * 1024, sxh = (s1 >= 16 ? s1 - 16 : 0) * 1024;
            int pos = 1;
#pragma unroll 1
            for (int st = 0; st < 8; ++st) {
                if (st == 3) { LD_FLAG_ASYNC("v254", flags); bar_vm<19>(); }
                if (st >= 3) bar_vm<9>();
                LAS char* sl = smem + T2_BASE + pos * T2_SLOT;
                const int ko = ((st + rot1) & 7) * 128;
                glds<NT>(rx, sl + lw * 1024, vx, sxh + ko);
#pragma unroll
                for (int u = 0; u < 8; ++u) glds<NT>(rx, sl + 2048 + (lw + 2 * u) * 1024, vx, sxm + 16 * u * 1024 + ko);
                pos = (pos + 1 == 3) ? 0 : pos + 1;
            }
            bar_vm<9>();
        }
        LD_FLAG_ASYNC("v253", flags);
        bar_vm<1>();
        drain_vm();
        if (bid != 0 && lw == 0) {
            unsigned a, b, spins = 0;
            RD_FLAG("v254", a); RD_FLAG("v253", b);
            while (a == b) {
                __builtin_amdgcn_s_sleep(1);
                b = ld_flag(flags);
                if (++spins > SPIN_CAP) break;
            }
        }
        bar_only();
        rsrc_t ra = mk_rsrc(ws + WS_ABT, 1024 * SEQ * 2), ro = mk_rsrc(ws + WS_O, SEQ * DM * 2);
        const int va = prow * 4096 + ch;
        const int sa = ((q >> 1) * 512 + e0) * 4096 + (q & 1) * 2048;
        const int vo = row * 1024 + ch;
        wait_ge(flags + (F1_IDX + q * 4 + et) * FW, 32u, flags, lane);
        if (wave == 4) STAMP(6);
        int slot = slot3;
        GAS unsigned* f2 = flags + (F2_IDX + (s4 >> 7)) * FW;
#pragma unroll 1
        for (int st = 0; st < 16; ++st) {
            if (st == 5) bar_vm<32>();
            if (st >= 5) bar_vm<24>();
            const int so = sa + ((st + rot3) & 15) * 128;
#pragma unroll
            for (int u = 0; u < 8; ++u) glds<16>(ra, smem + slot * SLOT + (lw + 2 * u) * 1024, va, so + (32 * (u >> 1) + 4 * (u & 1)) * 4096);
            slot = (slot + 1 == 5) ? 0 : slot + 1;
            if (st == 12) LD_FLAG_ASYNC("v252", f2);
        }
#pragma unroll 1
        for (int st = 0; st < 4; ++st) {
            bar_vm<24>();
            if (st == 0) {
                unsigned f2v;
                RD_FLAG("v252", f2v);
                if (f2v < 32u) wait_ge(f2, 32u, flags, lane);
            }
#pragma unroll
            for (int h = 0; h < 2; ++h)
#pragma unroll
                for (int u = 0; u < 4; ++u)
                    glds<16>(ro, smem + slot * SLOT + h * 16384 + (lw + 2 * u) * 1024, vo, (s4 + 16 * u) * 1024 + (st * 2 + h) * 128);
            slot = (slot + 1 == 5) ? 0 : slot + 1;
        }
        bar_vm<24>(); bar_vm<16>(); bar_vm<8>(); bar_vm<0>();
    } else {
        const int r = lane & 15, g = lane >> 4;
        if (wave == 0) STAMP(0);
        if (wave == 0 && bid == 0) {
#pragma unroll
            for (int i = 0; i < 5; ++i) {
                const int fi = 1 + i * 64 + lane;
                if (fi < NFLAGS) __hip_atomic_store(flags + fi * FW, 0u, __ATOMIC_RELAXED, __HIP_MEMORY_SCOPE_AGENT);
            }
            drain_vm();
            add_flag(flags, lane);
        }
        if (typeA) {
            f32x4 acc[4][4] = {};
            bar_lgkm();
            if (wave == 0) STAMP(1);
            nt_consume(acc, smem, 0, 8, wave, lane);
            if (wave == 0) STAMP(2);
            const float sg = (pg >> 1) ? -1.f : 1.f;
            rsrc_t rs = mk_rsrc(ws + WS_ABT, 1024 * SEQ * 2);
            const int vs = r * 4096 + g * 16;
            const int ss = ((pg >> 1) * 512 + e0 + (wave >> 1) * 64) * 4096 + (s1 + (wave & 1) * 64) * 2;
#pragma unroll
            for (int n = 0; n < 4; ++n)
#pragma unroll
                for (int u = 0; u < 2; ++u)
                    __builtin_amdgcn_raw_buffer_store_b128(pack8(acc[2 * u][n] * sg, acc[2 * u + 1][n] * sg), rs, vs + ss + n * 16 * 4096 + u * 64, 0, 16);
            drain_vm();
            add_flag(flags + (F1_IDX + pg * 4 + et) * FW, lane);
            if (wave == 0) STAMP(3);
        } else {
            const int c0 = ((g ^ (r >> 1)) << 4);
            const int ox = r * 128;
            const int qq = (lane & 15) >> 2, p = lane & 3;
            const int ow = 18432 + g * 3072 + qq * 128 + (((2 * wave + (p >> 1)) ^ (2 * ((qq >> 1) | ((g & 1) << 1)))) << 4) + 8 * (p & 1);
            f32x4 aq[8] = {}, ak[9] = {}, av[9] = {};
            Frag2 f0, f1;
            int pos = 1;
            LAS char* sl = smem + T2_BASE + pos * T2_SLOT;
            bar_lgkm();
            if (wave == 0) STAMP(1);
            t2_read(f0, sl + ox + c0, sl + ow);
#pragma unroll 1
            for (int it = 0; it < 7; ++it) {
                __builtin_amdgcn_sched_barrier(0);
                t2_read(f1, sl + ox + (c0 ^ 64), sl + ow + 12288);
                t2_mma(aq, ak, av, f0);
                SGB2();
                __builtin_amdgcn_sched_barrier(0);
                pos = (pos + 1 == 3) ? 0 : pos + 1;
                sl = smem + T2_BASE + pos * T2_SLOT;
                bar_lgkm();
                __builtin_amdgcn_sched_barrier(0);
                t2_read(f0, sl + ox + c0, sl + ow);
                t2_mma(aq, ak, av, f1);
                SGB2();
                __builtin_amdgcn_sched_barrier(0);
            }
            t2_read(f1, sl + ox + (c0 ^ 64), sl + ow + 12288);
            t2_mma(aq, ak, av, f0);
            SGB2();
            __builtin_amdgcn_sched_barrier(0);
            bar_lgkm();
            __builtin_amdgcn_sched_barrier(0);
            t2_mma(aq, ak, av, f1);
            if (wave == 0) STAMP(4);
            const int j = r, par = j & 1;
            const bool uc0 = (4 * g + par) <= j, uc1 = (4 * g + par + 2) <= j;
            const int head = hg * 4 + wave;
            rsrc_t rs = mk_rsrc(ws + WS_O, SEQ * DM * 2);
            const int vs = j * 1024 + g * 8;
            const int ss = s1 * 1024 + head * 32;
            bf16x4 kp = cvt4(ak[0]), vp = cvt4(av[0]);
            const f32x4 z = {0.f, 0.f, 0.f, 0.f};
#pragma unroll
            for (int rb = 1; rb < 9; ++rb) {
                bf16x4 kc = cvt4(ak[rb]), vc = cvt4(av[rb]), qb = cvt4(aq[rb - 1]);
                f32x4 sc = mfma16(kc, qb, z), sp = mfma16(kp, qb, z);
                float c0v = par ? sc[1] : sc[0], c1v = par ? sc[3] : sc[2];
                float p0v = par ? sp[1] : sp[0], p1v = par ? sp[3] : sp[2];
                if (rb == 1 && s1 == 0) { p0v = -4e9f; p1v = -4e9f; }
                float l0 = rbf(uc0 ? c0v : p0v) * 0.25f, l1 = rbf(uc1 ? c1v : p1v) * 0.25f;
                float m = xmax16_32(fmaxf(l0, l1));
                float e0v = __expf(l0 - m), e1v = __expf(l1 - m);
                float inv = __builtin_amdgcn_rcpf(xsum16_32(e0v + e1v));
                __bf16 b0 = (__bf16)(e0v * inv), b1 = (__bf16)(e1v * inv);
                unsigned t0v = (unsigned)__builtin_bit_cast(unsigned short, b0) << (16 * par);
                unsigned t1v = (unsigned)__builtin_bit_cast(unsigned short, b1) << (16 * par);
                u32x2 pc = {uc0 ? t0v : 0u, uc1 ? t1v : 0u}, pp = {uc0 ? 0u : t0v, uc1 ? 0u : t1v};
                f32x4 ov = mfma16(vc, __builtin_bit_cast(bf16x4, pc), z);
                ov = mfma16(vp, __builtin_bit_cast(bf16x4, pp), ov);
                __builtin_amdgcn_raw_buffer_store_b64(__builtin_bit_cast(u32x2, cvt4(ov)), rs, vs, ss + (rb - 1) * 16 * 1024, 16);
                kp = kc; vp = vc;
            }
            drain_vm();
            add_flag(flags + (F2_IDX + (s1 >> 7)) * FW, lane);
            if (wave == 0) STAMP(5);
        }
        f32x4 acc[4][4] = {};
        bar_lgkm();
        if (wave == 0) STAMP(9);
        int slot = nt_consume(acc, smem, slot3, 16, wave, lane);
        if (wave == 0) STAMP(10);
        const int tile = tt * 4 + et;
        rsrc_t rp = mk_rsrc(ws + WS_PART, 64 * 16 * 4 * 2048);
#pragma unroll
        for (int n = 1; n < 4; ++n) {
            const int base = (((tile * 4 + ((q + n) & 3)) * 4 + q) * 4 + wave) * 2048;
            __builtin_amdgcn_raw_buffer_store_b128(pack8(acc[0][n], acc[1][n]), rp, lane * 16 + base, 0, 16);
            __builtin_amdgcn_raw_buffer_store_b128(pack8(acc[2][n], acc[3][n]), rp, lane * 16 + base + 1024, 0, 16);
        }
        f32x4 own[4];
#pragma unroll
        for (int m = 0; m < 4; ++m) own[m] = acc[m][0];
        f32x4 a4[2][2] = {};
        {
            const int c0 = ((g ^ (r >> 1)) << 4);
            const int oo = ((wave >> 1) * 32 + r) * 128;
            const int qq = (lane & 15) >> 2, p = lane & 3;
            const int swz = 2 * ((qq >> 1) | ((g & 1) << 1));
            const int ow0 = 8192 + g * 1024 + qq * 128 + ((((wave & 1) * 4 + (p >> 1)) ^ swz) << 4) + 8 * (p & 1);
            const int ow1 = 8192 + g * 1024 + qq * 128 + ((((wave & 1) * 4 + 2 + (p >> 1)) ^ swz) << 4) + 8 * (p & 1);
#pragma unroll 1
            for (int st = 0; st < 4; ++st) {
                LAS char* sl = smem + slot * SLOT;
#pragma unroll
                for (int h = 0; h < 2; ++h)
#pragma unroll
                    for (int kk = 0; kk < 2; ++kk) {
                        bf16x8 w0 = lds_tr2(sl + h * 16384 + ow0 + kk * 4096), w1 = lds_tr2(sl + h * 16384 + ow1 + kk * 4096);
                        bf16x8 o0 = lds_b128(sl + h * 16384 + oo + (c0 ^ (kk * 64))), o1 = lds_b128(sl + h * 16384 + oo + (c0 ^ (kk * 64)) + 2048);
                        a4[0][0] = mfma32(w0, o0, a4[0][0]);
                        a4[0][1] = mfma32(w0, o1, a4[0][1]);
                        a4[1][0] = mfma32(w1, o0, a4[1][0]);
                        a4[1][1] = mfma32(w1, o1, a4[1][1]);
                    }
                slot = (slot + 1 == 5) ? 0 : slot + 1;
                if (st == 1) {
                    drain_vm();
                    if (lane < 4 && lane != q)
                        (void)__hip_atomic_fetch_add(flags + (F3_IDX + tile * 4 + lane) * FW, 1u, __ATOMIC_RELAXED, __HIP_MEMORY_SCOPE_AGENT);
                }
                if (st + 1 < 4) bar_lgkm();
            }
#pragma unroll
            for (int m = 0; m < 2; ++m)
#pragma unroll
                for (int n = 0; n < 2; ++n)
                    *(bf16x4*)(out + (size_t)(s4 + (wave >> 1) * 32 + n * 16 + r) * 1024 + 512 + n4 + (wave & 1) * 32 + m * 16 + 4 * g) = cvt4(a4[m][n]);
        }
        if (wave == 0) STAMP(11);
        wait_ge(flags + (F3_IDX + tile * 4 + q) * FW, 12u, flags, lane);
        if (wave == 0) STAMP(12);
        u32x4 pv[3][2];
#pragma unroll
        for (int k = 0; k < 3; ++k) {
            const int src = (q + 1 + k) & 3;
            const int base = (((tile * 4 + q) * 4 + src) * 4 + wave) * 2048;
            pv[k][0] = __builtin_amdgcn_raw_buffer_load_b128(rp, lane * 16, base, 16);
            pv[k][1] = __builtin_amdgcn_raw_buffer_load_b128(rp, lane * 16, base + 1024, 16);
        }
#pragma unroll
        for (int k = 0; k < 3; ++k) {
            unpack8_add(pv[k][0], own[0], own[1]);
            unpack8_add(pv[k][1], own[2], own[3]);
        }
        const int trow = t0 + (wave >> 1) * 64 + q * 16 + r;
#pragma unroll
        for (int u = 0; u < 2; ++u)
            *(u32x4*)(out + (size_t)trow * 1024 + e0 + (wave & 1) * 64 + 32 * u + 8 * g) = pack8(own[2 * u], own[2 * u + 1]);
    }
}

extern "C" void kernel_launch(void* const* d_in, const int* in_sizes, int n_in,
                              void* d_out, int out_size, void* d_ws, size_t ws_size,
                              hipStream_t stream) {
    fused<<<256, 512, 0, stream>>>((const __bf16*)d_in[0], (const __bf16*)d_in[1], (const __bf16*)d_in[2], (const __bf16*)d_in[3],
                                   (const __bf16*)d_in[4], (const __bf16*)d_in[5], (const __bf16*)d_in[6], (const __bf16*)d_in[7],
                                   (char*)d_ws, (__bf16*)d_out);
}
```

```cpp
#include <hip/hip_runtime.h>
#include <hip/hip_bf16.h>
#include <stdint.h>

#define LAS __attribute__((address_space(3)))
#define GAS __attribute__((address_space(1)))
typedef __attribute__((ext_vector_type(8))) __bf16 bf16x8;
typedef __attribute__((ext_vector_type(4))) __bf16 bf16x4;
typedef __attribute__((ext_vector_type(4))) short s16x4;
typedef __attribute__((ext_vector_type(4))) float f32x4;
typedef __attribute__((ext_vector_type(4))) unsigned u32x4;
typedef __attribute__((ext_vector_type(2))) unsigned u32x2;
typedef __amdgpu_buffer_rsrc_t rsrc_t;

#define NOSTAMPS 1
#define STAMP_N 16
#define STAMP_OFF (64u<<20)
#define STAMP(k) do{}while(0)

constexpr int SEQ = 2048, DM = 512;
constexpr int SLOT = 32768;
constexpr int T2_SLOT = 43008;
constexpr int T2_BASE = 32768;

constexpr unsigned WS_FLAGS = 0;
constexpr unsigned WS_ABT   = 1u << 20;
constexpr unsigned WS_O     = WS_ABT + (4u << 20);
constexpr unsigned WS_PART  = WS_O + (2u << 20);
constexpr int FW = 32;
constexpr int F1_IDX = 1, F2_IDX = 17, F3_IDX = 33, NFLAGS = 33 + 256;
constexpr int NT = 0;
constexpr unsigned SPIN_CAP = 1u << 17;

__device__ __forceinline__ rsrc_t mk_rsrc(const void* p, unsigned bytes) {
    return __builtin_amdgcn_make_buffer_rsrc((void*)p, 0, bytes, 0x00020000);
}
template <int AUX>
__device__ __forceinline__ void glds(rsrc_t r, LAS char* dst, int voff, int soff) {
    __builtin_amdgcn_raw_ptr_buffer_load_lds(r, (LAS void*)dst, 16, voff, soff, 0, AUX);
}
template <int N> __device__ __forceinline__ void bar_vm() {
    asm volatile("s_waitcnt vmcnt(%0)\n\ts_barrier" ::"n"(N) : "memory");
}
__device__ __forceinline__ void bar_only() { asm volatile("s_barrier" ::: "memory"); }
__device__ __forceinline__ void bar_lgkm() {
    asm volatile("s_waitcnt lgkmcnt(0)\n\ts_barrier" ::: "memory");
}
__device__ __forceinline__ void drain_vm() { asm volatile("s_waitcnt vmcnt(0)" ::: "memory"); }
__device__ __forceinline__ f32x4 mfma32(bf16x8 a, bf16x8 b, f32x4 c) {
    return __builtin_amdgcn_mfma_f32_16x16x32_bf16(a, b, c, 0, 0, 0);
}
__device__ __forceinline__ f32x4 mfma16(bf16x4 a, bf16x4 b, f32x4 c) {
    return __builtin_amdgcn_mfma_f32_16x16x16bf16_1k(__builtin_bit_cast(s16x4, a), __builtin_bit_cast(s16x4, b), c, 0, 0, 0);
}
__device__ __forceinline__ bf16x8 lds_b128(LAS char* p) { return *(LAS bf16x8*)p; }
__device__ __forceinline__ bf16x8 lds_tr2(LAS char* p) {
    bf16x4 t0 = __builtin_amdgcn_ds_read_tr16_b64_v4bf16((LAS bf16x4*)p);
    bf16x4 t1 = __builtin_amdgcn_ds_read_tr16_b64_v4bf16((LAS bf16x4*)(p + 512));
    return __builtin_shufflevector(t0, t1, 0, 1, 2, 3, 4, 5, 6, 7);
}
__device__ __forceinline__ bf16x4 cvt4(f32x4 v) { return __builtin_convertvector(v, bf16x4); }
__device__ __forceinline__ u32x4 pack8(f32x4 lo, f32x4 hi) {
    bf16x4 a = cvt4(lo), b = cvt4(hi);
    bf16x8 c = __builtin_shufflevector(a, b, 0, 1, 2, 3, 4, 5, 6, 7);
    return __builtin_bit_cast(u32x4, c);
}
__device__ __forceinline__ void unpack8_add(u32x4 v, f32x4& lo, f32x4& hi) {
    unsigned a = v[0], b = v[1], c = v[2], d = v[3];
    lo[0] += __uint_as_float(a << 16); lo[1] += __uint_as_float(a & 0xffff0000u);
    lo[2] += __uint_as_float(b << 16); lo[3] += __uint_as_float(b & 0xffff0000u);
    hi[0] += __uint_as_float(c << 16); hi[1] += __uint_as_float(c & 0xffff0000u);
    hi[2] += __uint_as_float(d << 16); hi[3] += __uint_as_float(d & 0xffff0000u);
}
__device__ __forceinline__ float rbf(float x) { return (float)(__bf16)x; }
__device__ __forceinline__ float xmax16_32(float x) {
    unsigned u = __float_as_uint(x);
    auto r = __builtin_amdgcn_permlane16_swap(u, u, false, false);
    unsigned a = r[0], b = r[1];
    x = fmaxf(__uint_as_float(a), __uint_as_float(b));
    u = __float_as_uint(x);
    auto r2 = __builtin_amdgcn_permlane32_swap(u, u, false, false);
    a = r2[0]; b = r2[1];
    return fmaxf(__uint_as_float(a), __uint_as_float(b));
}
__device__ __forceinline__ float xsum16_32(float x) {
    unsigned u = __float_as_uint(x);
    auto r = __builtin_amdgcn_permlane16_swap(u, u, false, false);
    unsigned a = r[0], b = r[1];
    x = __uint_as_float(a) + __uint_as_float(b);
    u = __float_as_uint(x);
    auto r2 = __builtin_amdgcn_permlane32_swap(u, u, false, false);
    a = r2[0]; b = r2[1];
    return __uint_as_float(a) + __uint_as_float(b);
}
__device__ __forceinline__ unsigned ld_flag(GAS unsigned* f) {
    return __builtin_amdgcn_readfirstlane(__hip_atomic_load(f, __ATOMIC_RELAXED, __HIP_MEMORY_SCOPE_AGENT));
}
__device__ __forceinline__ void add_flag(GAS unsigned* f, int lane) {
    if (lane == 0) (void)__hip_atomic_fetch_add(f, 1u, __ATOMIC_RELAXED, __HIP_MEMORY_SCOPE_AGENT);
}
#define LD_FLAG_ASYNC(REG, f) asm volatile("global_load_dword " REG ", %0, off sc1" ::"v"(f) : "memory", REG)
#define RD_FLAG(REG, out) asm volatile("v_readfirstlane_b32 %0, " REG : "=s"(out)::"memory")
__device__ __forceinline__ void add_flag_async(GAS unsigned* f, int lane) {
    unsigned one = 1u;
    if (lane == 0) asm volatile("global_atomic_add %0, %1, off" ::"v"(f), "v"(one) : "memory");
}
__device__ __forceinline__ void wait_ge(GAS unsigned* f, unsigned target, GAS unsigned* hb, int lane) {
    unsigned spins = 0;
    while (ld_flag(f) < target) {
        ++spins;
        if ((spins & 31u) == 0u) {
            add_flag(hb, lane);
            if (spins > SPIN_CAP) break;
        }
        __builtin_amdgcn_s_sleep(1);
    }
    asm volatile("" ::: "memory");
}
struct Frag { bf16x8 a[4], b[4]; };
#define SGB() do { _Pragma("unroll") for (int _i = 0; _i < 8; ++_i) { __builtin_amdgcn_sched_group_barrier(0x8, 1, 0); __builtin_amdgcn_sched_group_barrier(0x100, 1, 0); } __builtin_amdgcn_sched_group_barrier(0x8, 8, 0); } while (0)
__device__ __forceinline__ void nt_read(Frag& f, LAS char* pa, LAS char* pb) {
    f.a[0] = lds_b128(pa);
#pragma unroll
    for (int n = 0; n < 4; ++n) f.b[n] = lds_b128(pb + n * 2048);
#pragma unroll
    for (int m = 1; m < 4; ++m) f.a[m] = lds_b128(pa + m * 2048);
}
__device__ __forceinline__ void nt_mma(f32x4 (&acc)[4][4], const Frag& f) {
#pragma unroll
    for (int m = 0; m < 4; ++m)
#pragma unroll
        for (int n = 0; n < 4; ++n) acc[m][n] = mfma32(f.a[m], f.b[n], acc[m][n]);
}
__device__ __forceinline__ int nt_consume(f32x4 (&acc)[4][4], LAS char* smem, int slot, int nst, int w, int lane) {
    const int r = lane & 15, g = lane >> 4;
    const int c0 = ((g ^ (r >> 1)) << 4), c1 = c0 ^ 64;
    const int oa = ((w & 1) * 64 + r) * 128, ob = 16384 + ((w >> 1) * 64 + r) * 128;
    Frag f0, f1;
    LAS char* s = smem + slot * SLOT;
    nt_read(f0, s + oa + c0, s + ob + c0);
#pragma unroll 1
    for (int it = 0; it < nst - 1; ++it) {
        __builtin_amdgcn_sched_barrier(0);
        nt_read(f1, s + oa + c1, s + ob + c1);
        nt_mma(acc, f0);
        SGB();
        __builtin_amdgcn_sched_barrier(0);
        slot = (slot + 1 == 5) ? 0 : slot + 1;
        s = smem + slot * SLOT;
        bar_lgkm();
        __builtin_amdgcn_sched_barrier(0);
        nt_read(f0, s + oa + c0, s + ob + c0);
        nt_mma(acc, f1);
        SGB();
        __builtin_amdgcn_sched_barrier(0);
    }
    nt_read(f1, s + oa + c1, s + ob + c1);
    nt_mma(acc, f0);
    SGB();
    __builtin_amdgcn_sched_barrier(0);
    slot = (slot + 1 == 5) ? 0 : slot + 1;
    bar_lgkm();
    __builtin_amdgcn_sched_barrier(0);
    nt_mma(acc, f1);
    return slot;
}

struct Frag2 { bf16x8 x[9], w[3]; };
__device__ __forceinline__ void t2_read(Frag2& f, LAS char* px, LAS char* pw) {
    f.w[1] = lds_tr2(pw + 1024);
    f.x[0] = lds_b128(px);
    f.w[2] = lds_tr2(pw + 2048);
    f.w[0] = lds_tr2(pw);
#pragma unroll
    for (int rb = 1; rb < 9; ++rb) f.x[rb] = lds_b128(px + rb * 2048);
}
__device__ __forceinline__ void t2_mma(f32x4 (&aq)[8], f32x4 (&ak)[9], f32x4 (&av)[9], const Frag2& f) {
#pragma unroll
    for (int rb = 0; rb < 9; ++rb) {
        ak[rb] = mfma32(f.w[1], f.x[rb], ak[rb]);
        av[rb] = mfma32(f.x[rb], f.w[2], av[rb]);
        if (rb > 0) aq[rb - 1] = mfma32(f.w[0], f.x[rb], aq[rb - 1]);
    }
}
#define SGB2() do { _Pragma("unroll") for (int _i = 0; _i < 15; ++_i) { __builtin_amdgcn_sched_group_barrier(0x8, 1, 0); __builtin_amdgcn_sched_group_barrier(0x100, 1, 0); } __builtin_amdgcn_sched_group_barrier(0x8, 11, 0); } while (0)

__global__ __launch_bounds__(512, 2) void fused(const __bf16* __restrict__ xa, const __bf16* __restrict__ cre, const __bf16* __restrict__ cim,
                                                const __bf16* __restrict__ hre, const __bf16* __restrict__ him, const __bf16* __restrict__ xb,
                                                const __bf16* __restrict__ wqkv, const __bf16* __restrict__ wo,
                                                char* __restrict__ ws, __bf16* __restrict__ out) {
    __shared__ __attribute__((aligned(1024))) char smem_[163840];
    LAS char* smem = (LAS char*)smem_;
    const int tid = threadIdx.x, lane = tid & 63;
    const int wave = __builtin_amdgcn_readfirstlane(tid >> 6);
    const int bid = blockIdx.x, xcd = bid & 7, idx = bid >> 3;
    const int q = xcd & 3, th = xcd >> 2;
    const bool typeA = q < 2;
    const int ttl = idx >> 2, et = idx & 3, tt = th * 8 + ttl;
    const int t0 = tt * 128, e0 = et * 128;
    const int pg = (q & 1) + 2 * th;
    const int s1 = typeA ? ((pg & 1) * 8 + ttl) * 128 : ((pg >> 1) * 8 + ttl) * 128;
    const int hg = (pg & 1) * 4 + et;
    const int s4 = (xcd * 4 + (idx >> 3)) * 64, n4 = (idx & 7) * 64;
    GAS unsigned* flags = (GAS unsigned*)(ws + WS_FLAGS);
    const int slot3 = typeA ? 3 : 0;
    const int rot1 = (ttl + 2 * et) & 7, rot3 = (2 * ttl + 4 * et) & 15;

    if (wave >= 6) {
        const int lw = wave - 6;
        const int row = 8 * lw + (lane >> 3);
        const int ch = ((lane & 7) ^ (4 * lw + (lane >> 4))) * 16;
        const int cht = ((lane & 7) ^ (2 * (((lane >> 4) & 1) | (lw << 1)))) * 16;
        const bool master = (bid == 0 && lw == 0);
        rsrc_t rc = mk_rsrc((q >> 1) ? cim : cre, SEQ * SEQ * 2);
        const int vb = row * 4096 + ch;
        const int sb = t0 * 4096 + (q & 1) * 2048;
        auto issueC = [&](int st, int slot) {
            const int so = sb + ((st + rot3) & 15) * 128;
#pragma unroll
            for (int u = 0; u < 8; ++u) glds<NT>(rc, smem + slot * SLOT + 16384 + (lw + 2 * u) * 1024, vb, so + 16 * ((u & 4) | ((u + q) & 3)) * 4096);
        };
        int slot = 0;
        if (typeA) {
            rsrc_t rh = mk_rsrc((pg >> 1) ? him : hre, DM * DM * 2);
            const int vh = row * 1024 + ch;
#pragma unroll 1
            for (int st = 0; st < 8; ++st) {
                if (st == 5) bar_vm<32>();
                if (st >= 5) { bar_vm<24>(); if (master) add_flag_async(flags, lane); }
                const int so = e0 * 1024 + ((st + rot1) & 7) * 128;
#pragma unroll
                for (int u = 0; u < 8; ++u) glds<NT>(rh, smem + slot * SLOT + 16384 + (lw + 2 * u) * 1024, vh, so + 16 * u * 1024);
                slot = (slot + 1 == 5) ? 0 : slot + 1;
            }
        } else {
            rsrc_t rq = mk_rsrc(wqkv, DM * 3 * DM * 2);
            const int vq = row * 3072 + cht;
            int pos = 1;
#pragma unroll 1
            for (int st = 0; st < 8; ++st) {
                if (st == 3) bar_vm<24>();
                if (st >= 3) bar_vm<12>();
                LAS char* sl = smem + T2_BASE + pos * T2_SLOT;
                const int so = ((st + rot1) & 7) * 64 * 3072 + hg * 128;
#pragma unroll
                for (int v = 0; v < 4; ++v)
#pragma unroll
                    for (int cg = 0; cg < 3; ++cg)
                        glds<NT>(rq, sl + 18432 + ((lw + 2 * v) * 3 + cg) * 1024, vq, so + 16 * v * 3072 + cg * 1024);
                pos = (pos + 1 == 3) ? 0 : pos + 1;
            }
        }
#pragma unroll 1
        for (int st = 0; st < 16; ++st) {
            if (st >= 5) {
                if (st == 5) bar_vm<32>();
                bar_vm<24>();
            } else if (typeA) {
                if (st < 4) bar_vm<24>(); else bar_only();
                if (master) add_flag_async(flags, lane);
            } else {
                if (st == 1) bar_vm<20>();
                if (st == 2) bar_vm<16>();
                if (st == 3) bar_only();
            }
            issueC(st, slot);
            slot = (slot + 1 == 5) ? 0 : slot + 1;
        }
        rsrc_t rw = mk_rsrc(wo, DM * DM * 2);
        const int vw = row * 1024 + cht;
#pragma unroll 1
        for (int st = 0; st < 4; ++st) {
            bar_vm<24>();
#pragma unroll
            for (int h = 0; h < 2; ++h)
#pragma unroll
                for (int u = 0; u < 4; ++u)
                    glds<NT>(rw, smem + slot * SLOT + h * 16384 + 8192 + (lw + 2 * u) * 1024, vw, ((st * 2 + h) * 64 + 16 * u) * 1024 + n4 * 2);
            slot = (slot + 1 == 5) ? 0 : slot + 1;
        }
        bar_vm<24>(); bar_vm<16>(); bar_vm<8>(); bar_vm<0>();
    } else if (wave >= 4) {
        const int lw = wave - 4;
        const int ch = ((lane & 7) ^ (4 * lw + (lane >> 4))) * 16;
        const int prow = 8 * (lane >> 5) + ((lane >> 3) & 3) + 16 * lw;
        const int row = 8 * lw + (lane >> 3);
        if (typeA) {
            rsrc_t rx = mk_rsrc(xa, SEQ * DM * 2);
            const int va = prow * 1024 + ch;
            int sl = 0;
#pragma unroll 1
            for (int st = 0; st < 8; ++st) {
                if (st == 5) { LD_FLAG_ASYNC("v254", flags); bar_vm<33>(); }
                if (st >= 5) bar_vm<24>();
                const int so = s1 * 1024 + ((st + rot1) & 7) * 128;
#pragma unroll
                for (int u = 0; u < 8; ++u) glds<NT>(rx, smem + sl * SLOT + (lw + 2 * u) * 1024, va, so + (32 * (u >> 1) + 4 * (u & 1)) * 1024);
                sl = (sl + 1 == 5) ? 0 : sl + 1;
            }
            bar_vm<24>(); bar_vm<16>(); bar_vm<8>();
        } else {
            rsrc_t rx = mk_rsrc(xb, SEQ * DM * 2);
            const int vx = row * 1024 + ch;
            const int sxm = s1 * 1024, sxh = (s1 >= 16 ? s1 - 16 : 0) * 1024;
            int pos = 1;
#pragma unroll 1
            for (int st = 0; st < 8; ++st) {
                if (st == 3) { LD_FLAG_ASYNC("v254", flags); bar_vm<19>(); }
                if (st >= 3) bar_vm<9>();
                LAS char* sl = smem + T2_BASE + pos * T2_SLOT;
                const int ko = ((st + rot1) & 7) * 128;
                glds<NT>(rx, sl + lw * 1024, vx, sxh + ko);
#pragma unroll
                for (int u = 0; u < 8; ++u) glds<NT>(rx, sl + 2048 + (lw + 2 * u) * 1024, vx, sxm + 16 * u * 1024 + ko);
                pos = (pos + 1 == 3) ? 0 : pos + 1;
            }
            bar_vm<9>();
        }
        LD_FLAG_ASYNC("v253", flags);
        bar_vm<1>();
        drain_vm();
        if (bid != 0 && lw == 0) {
            unsigned a, b, spins = 0;
            RD_FLAG("v254", a); RD_FLAG("v253", b);
            while (a == b) {
                __builtin_amdgcn_s_sleep(1);
                b = ld_flag(flags);
                if (++spins > SPIN_CAP) break;
            }
        }
        bar_only();
        rsrc_t ra = mk_rsrc(ws + WS_ABT, 1024 * SEQ * 2), ro = mk_rsrc(ws + WS_O, SEQ * DM * 2);
        const int va = prow * 4096 + ch;
        const int sa = ((q >> 1) * 512 + e0) * 4096 + (q & 1) * 2048;
        const int vo = row * 1024 + ch;
        wait_ge(flags + (F1_IDX + q * 4 + et) * FW, 32u, flags, lane);
        if (wave == 4) STAMP(6);
        int slot = slot3;
        GAS unsigned* f2 = flags + (F2_IDX + (s4 >> 7)) * FW;
#pragma unroll 1
        for (int st = 0; st < 16; ++st) {
            if (st == 5) bar_vm<32>();
            if (st >= 5) bar_vm<24>();
            const int so = sa + ((st + rot3) & 15) * 128;
#pragma unroll
            for (int u = 0; u < 8; ++u) glds<16>(ra, smem + slot * SLOT + (lw + 2 * u) * 1024, va, so + (32 * (u >> 1) + 4 * (u & 1)) * 4096);
            slot = (slot + 1 == 5) ? 0 : slot + 1;
            if (st == 12) LD_FLAG_ASYNC("v252", f2);
        }
#pragma unroll 1
        for (int st = 0; st < 4; ++st) {
            bar_vm<24>();
            if (st == 0) {
                unsigned f2v;
                RD_FLAG("v252", f2v);
                if (f2v < 32u) wait_ge(f2, 32u, flags, lane);
            }
#pragma unroll
            for (int h = 0; h < 2; ++h)
#pragma unroll
                for (int u = 0; u < 4; ++u)
                    glds<16>(ro, smem + slot * SLOT + h * 16384 + (lw + 2 * u) * 1024, vo, (s4 + 16 * u) * 1024 + (st * 2 + h) * 128);
            slot = (slot + 1 == 5) ? 0 : slot + 1;
        }
        bar_vm<24>(); bar_vm<16>(); bar_vm<8>(); bar_vm<0>();
    } else {
        const int r = lane & 15, g = lane >> 4;
        if (wave == 0) STAMP(0);
        if (wave == 1) {
            unsigned long long pc; int off, len;
            asm volatile("s_getpc_b64 %0\n.Lpf_here:\n\ts_mov_b32 %1, .Lpf_here-_Z5fusedPKDF16bS0_S0_S0_S0_S0_S0_S0_PcPDF16b\n\ts_mov_b32 %2, _Z8code_endPj-_Z5fusedPKDF16bS0_S0_S0_S0_S0_S0_S0_PcPDF16b" : "=s"(pc), "=s"(off), "=s"(len));
            const unsigned* base = (const unsigned*)(pc - (unsigned)off);
            unsigned acc = 0;
            for (int i = lane * 16; i * 4 < len; i += 64 * 16) acc ^= base[i];
            asm volatile("" ::"v"(acc));
        }
        if (wave == 0 && bid == 0) {
#pragma unroll
            for (int i = 0; i < 5; ++i) {
                const int fi = 1 + i * 64 + lane;
                if (fi < NFLAGS) __hip_atomic_store(flags + fi * FW, 0u, __ATOMIC_RELAXED, __HIP_MEMORY_SCOPE_AGENT);
            }
            drain_vm();
            add_flag(flags, lane);
        }
        if (typeA) {
            f32x4 acc[4][4] = {};
            bar_lgkm();
            if (wave == 0) STAMP(1);
            nt_consume(acc, smem, 0, 8, wave, lane);
            if (wave == 0) STAMP(2);
            const float sg = (pg >> 1) ? -1.f : 1.f;
            rsrc_t rs = mk_rsrc(ws + WS_ABT, 1024 * SEQ * 2);
            const int vs = r * 4096 + g * 16;
            const int ss = ((pg >> 1) * 512 + e0 + (wave >> 1) * 64) * 4096 + (s1 + (wave & 1) * 64) * 2;
#pragma unroll
            for (int n = 0; n < 4; ++n)
#pragma unroll
                for (int u = 0; u < 2; ++u)
                    __builtin_amdgcn_raw_buffer_store_b128(pack8(acc[2 * u][n] * sg, acc[2 * u + 1][n] * sg), rs, vs + ss + n * 16 * 4096 + u * 64, 0, 16);
            drain_vm();
            add_flag(flags + (F1_IDX + pg * 4 + et) * FW, lane);
            if (wave == 0) STAMP(3);
        } else {
            const int c0 = ((g ^ (r >> 1)) << 4);
            const int ox = r * 128;
            const int qq = (lane & 15) >> 2, p = lane & 3;
            const int ow = 18432 + g * 3072 + qq * 128 + (((2 * wave + (p >> 1)) ^ (2 * ((qq >> 1) | ((g & 1) << 1)))) << 4) + 8 * (p & 1);
            f32x4 aq[8] = {}, ak[9] = {}, av[9] = {};
            Frag2 f0, f1;
            int pos = 1;
            LAS char* sl = smem + T2_BASE + pos * T2_SLOT;
            bar_lgkm();
            if (wave == 0) STAMP(1);
            t2_read(f0, sl + ox + c0, sl + ow);
#pragma unroll 1
            for (int it = 0; it < 7; ++it) {
                __builtin_amdgcn_sched_barrier(0);
                t2_read(f1, sl + ox + (c0 ^ 64), sl + ow + 12288);
                t2_mma(aq, ak, av, f0);
                SGB2();
                __builtin_amdgcn_sched_barrier(0);
                pos = (pos + 1 == 3) ? 0 : pos + 1;
                sl = smem + T2_BASE + pos * T2_SLOT;
                bar_lgkm();
                __builtin_amdgcn_sched_barrier(0);
                t2_read(f0, sl + ox + c0, sl + ow);
                t2_mma(aq, ak, av, f1);
                SGB2();
                __builtin_amdgcn_sched_barrier(0);
            }
            t2_read(f1, sl + ox + (c0 ^ 64), sl + ow + 12288);
            t2_mma(aq, ak, av, f0);
            SGB2();
            __builtin_amdgcn_sched_barrier(0);
            bar_lgkm();
            __builtin_amdgcn_sched_barrier(0);
            t2_mma(aq, ak, av, f1);
            if (wave == 0) STAMP(4);
            const int j = r, par = j & 1;
            const bool uc0 = (4 * g + par) <= j, uc1 = (4 * g + par + 2) <= j;
            const int head = hg * 4 + wave;
            rsrc_t rs = mk_rsrc(ws + WS_O, SEQ * DM * 2);
            const int vs = j * 1024 + g * 8;
            const int ss = s1 * 1024 + head * 32;
            bf16x4 kp = cvt4(ak[0]), vp = cvt4(av[0]);
            const f32x4 z = {0.f, 0.f, 0.f, 0.f};
#pragma unroll
            for (int rb = 1; rb < 9; ++rb) {
                bf16x4 kc = cvt4(ak[rb]), vc = cvt4(av[rb]), qb = cvt4(aq[rb - 1]);
                f32x4 sc = mfma16(kc, qb, z), sp = mfma16(kp, qb, z);
                float c0v = par ? sc[1] : sc[0], c1v = par ? sc[3] : sc[2];
                float p0v = par ? sp[1] : sp[0], p1v = par ? sp[3] : sp[2];
                if (rb == 1 && s1 == 0) { p0v = -4e9f; p1v = -4e9f; }
                float l0 = rbf(uc0 ? c0v : p0v) * 0.25f, l1 = rbf(uc1 ? c1v : p1v) * 0.25f;
                float m = xmax16_32(fmaxf(l0, l1));
                float e0v = __expf(l0 - m), e1v = __expf(l1 - m);
                float inv = __builtin_amdgcn_rcpf(xsum16_32(e0v + e1v));
                __bf16 b0 = (__bf16)(e0v * inv), b1 = (__bf16)(e1v * inv);
                unsigned t0v = (unsigned)__builtin_bit_cast(unsigned short, b0) << (16 * par);
                unsigned t1v = (unsigned)__builtin_bit_cast(unsigned short, b1) << (16 * par);
                u32x2 pc = {uc0 ? t0v : 0u, uc1 ? t1v : 0u}, pp = {uc0 ? 0u : t0v, uc1 ? 0u : t1v};
                f32x4 ov = mfma16(vc, __builtin_bit_cast(bf16x4, pc), z);
                ov = mfma16(vp, __builtin_bit_cast(bf16x4, pp), ov);
                __builtin_amdgcn_raw_buffer_store_b64(__builtin_bit_cast(u32x2, cvt4(ov)), rs, vs, ss + (rb - 1) * 16 * 1024, 16);
                kp = kc; vp = vc;
            }
            drain_vm();
            add_flag(flags + (F2_IDX + (s1 >> 7)) * FW, lane);
            if (wave == 0) STAMP(5);
        }
        f32x4 acc[4][4] = {};
        bar_lgkm();
        if (wave == 0) STAMP(9);
        int slot = nt_consume(acc, smem, slot3, 16, wave, lane);
        if (wave == 0) STAMP(10);
        const int tile = tt * 4 + et;
        rsrc_t rp = mk_rsrc(ws + WS_PART, 64 * 16 * 4 * 2048);
#pragma unroll
        for (int n = 1; n < 4; ++n) {
            const int base = (((tile * 4 + ((q + n) & 3)) * 4 + q) * 4 + wave) * 2048;
            __builtin_amdgcn_raw_buffer_store_b128(pack8(acc[0][n], acc[1][n]), rp, lane * 16 + base, 0, 16);
            __builtin_amdgcn_raw_buffer_store_b128(pack8(acc[2][n], acc[3][n]), rp, lane * 16 + base + 1024, 0, 16);
        }
        f32x4 own[4];
#pragma unroll
        for (int m = 0; m < 4; ++m) own[m] = acc[m][0];
        f32x4 a4[2][2] = {};
        {
            const int c0 = ((g ^ (r >> 1)) << 4);
            const int oo = ((wave >> 1) * 32 + r) * 128;
            const int qq = (lane & 15) >> 2, p = lane & 3;
            const int swz = 2 * ((qq >> 1) | ((g & 1) << 1));
            const int ow0 = 8192 + g * 1024 + qq * 128 + ((((wave & 1) * 4 + (p >> 1)) ^ swz) << 4) + 8 * (p & 1);
            const int ow1 = 8192 + g * 1024 + qq * 128 + ((((wave & 1) * 4 + 2 + (p >> 1)) ^ swz) << 4) + 8 * (p & 1);
#pragma unroll 1
            for (int st = 0; st < 4; ++st) {
                LAS char* sl = smem + slot * SLOT;
#pragma unroll
                for (int h = 0; h < 2; ++h)
#pragma unroll
                    for (int kk = 0; kk < 2; ++kk) {
                        bf16x8 w0 = lds_tr2(sl + h * 16384 + ow0 + kk * 4096), w1 = lds_tr2(sl + h * 16384 + ow1 + kk * 4096);
                        bf16x8 o0 = lds_b128(sl + h * 16384 + oo + (c0 ^ (kk * 64))), o1 = lds_b128(sl + h * 16384 + oo + (c0 ^ (kk * 64)) + 2048);
                        a4[0][0] = mfma32(w0, o0, a4[0][0]);
                        a4[0][1] = mfma32(w0, o1, a4[0][1]);
                        a4[1][0] = mfma32(w1, o0, a4[1][0]);
                        a4[1][1] = mfma32(w1, o1, a4[1][1]);
                    }
                slot = (slot + 1 == 5) ? 0 : slot + 1;
                if (st == 1) {
                    drain_vm();
                    if (lane < 4 && lane != q)
                        (void)__hip_atomic_fetch_add(flags + (F3_IDX + tile * 4 + lane) * FW, 1u, __ATOMIC_RELAXED, __HIP_MEMORY_SCOPE_AGENT);
                }
                if (st + 1 < 4) bar_lgkm();
            }
#pragma unroll
            for (int m = 0; m < 2; ++m)
#pragma unroll
                for (int n = 0; n < 2; ++n)
                    *(bf16x4*)(out + (size_t)(s4 + (wave >> 1) * 32 + n * 16 + r) * 1024 + 512 + n4 + (wave & 1) * 32 + m * 16 + 4 * g) = cvt4(a4[m][n]);
        }
        if (wave == 0) STAMP(11);
        wait_ge(flags + (F3_IDX + tile * 4 + q) * FW, 12u, flags, lane);
        if (wave == 0) STAMP(12);
        u32x4 pv[3][2];
#pragma unroll
        for (int k = 0; k < 3; ++k) {
            const int src = (q + 1 + k) & 3;
            const int base = (((tile * 4 + q) * 4 + src) * 4 + wave) * 2048;
            pv[k][0] = __builtin_amdgcn_raw_buffer_load_b128(rp, lane * 16, base, 16);
            pv[k][1] = __builtin_amdgcn_raw_buffer_load_b128(rp, lane * 16, base + 1024, 16);
        }
#pragma unroll
        for (int k = 0; k < 3; ++k) {
            unpack8_add(pv[k][0], own[0], own[1]);
            unpack8_add(pv[k][1], own[2], own[3]);
        }
        const int trow = t0 + (wave >> 1) * 64 + q * 16 + r;
#pragma unroll
        for (int u = 0; u < 2; ++u)
            *(u32x4*)(out + (size_t)trow * 1024 + e0 + (wave & 1) * 64 + 32 * u + 8 * g) = pack8(own[2 * u], own[2 * u + 1]);
    }
}

__global__ void code_end(unsigned* p) { p[0] = 0; }

extern "C" void kernel_launch(void* const* d_in, const int* in_sizes, int n_in,
                              void* d_out, int out_size, void* d_ws, size_t ws_size,
                              hipStream_t stream) {
    fused<<<256, 512, 0, stream>>>((const __bf16*)d_in[0], (const __bf16*)d_in[1], (const __bf16*)d_in[2], (const __bf16*)d_in[3],
                                   (const __bf16*)d_in[4], (const __bf16*)d_in[5], (const __bf16*)d_in[6], (const __bf16*)d_in[7],
                                   (char*)d_ws, (__bf16*)d_out);
}
```

```cpp
#include <hip/hip_runtime.h>
#include <hip/hip_bf16.h>
#include <stdint.h>

#define LAS __attribute__((address_space(3)))
#define GAS __attribute__((address_space(1)))
typedef __attribute__((ext_vector_type(8))) __bf16 bf16x8;
typedef __attribute__((ext_vector_type(4))) __bf16 bf16x4;
typedef __attribute__((ext_vector_type(4))) short s16x4;
typedef __attribute__((ext_vector_type(4))) float f32x4;
typedef __attribute__((ext_vector_type(4))) unsigned u32x4;
typedef __attribute__((ext_vector_type(2))) unsigned u32x2;
typedef __amdgpu_buffer_rsrc_t rsrc_t;

#define NOSTAMPS 1
#define STAMP_N 16
#define STAMP_OFF (64u<<20)
#define STAMP(k) do{}while(0)

constexpr int SEQ = 2048, DM = 512;
constexpr int SLOT = 32768;
constexpr int T2_SLOT = 43008;
constexpr int T2_BASE = 32768;

constexpr unsigned WS_FLAGS = 0;
constexpr unsigned WS_ABT   = 1u << 20;
constexpr unsigned WS_O     = WS_ABT + (4u << 20);
constexpr unsigned WS_PART  = WS_O + (2u << 20);
constexpr int FW = 32;
constexpr int F1_IDX = 1, F2_IDX = 17, F3_IDX = 33, NFLAGS = 33 + 256;
constexpr int NT = 0;
constexpr unsigned SPIN_CAP = 1u << 17;

__device__ __forceinline__ rsrc_t mk_rsrc(const void* p, unsigned bytes) {
    return __builtin_amdgcn_make_buffer_rsrc((void*)p, 0, bytes, 0x00020000);
}
template <int AUX>
__device__ __forceinline__ void glds(rsrc_t r, LAS char* dst, int voff, int soff) {
    __builtin_amdgcn_raw_ptr_buffer_load_lds(r, (LAS void*)dst, 16, voff, soff, 0, AUX);
}
template <int N> __device__ __forceinline__ void bar_vm() {
    asm volatile("s_waitcnt vmcnt(%0)\n\ts_barrier" ::"n"(N) : "memory");
}
__device__ __forceinline__ void bar_only() { asm volatile("s_barrier" ::: "memory"); }
__device__ __forceinline__ void bar_lgkm() {
    asm volatile("s_waitcnt lgkmcnt(0)\n\ts_barrier" ::: "memory");
}
__device__ __forceinline__ void drain_vm() { asm volatile("s_waitcnt vmcnt(0)" ::: "memory"); }
__device__ __forceinline__ f32x4 mfma32(bf16x8 a, bf16x8 b, f32x4 c) {
    return __builtin_amdgcn_mfma_f32_16x16x32_bf16(a, b, c, 0, 0, 0);
}
__device__ __forceinline__ f32x4 mfma16(bf16x4 a, bf16x4 b, f32x4 c) {
    return __builtin_amdgcn_mfma_f32_16x16x16bf16_1k(__builtin_bit_cast(s16x4, a), __builtin_bit_cast(s16x4, b), c, 0, 0, 0);
}
__device__ __forceinline__ bf16x8 lds_b128(LAS char* p) { return *(LAS bf16x8*)p; }
__device__ __forceinline__ bf16x8 lds_tr2(LAS char* p) {
    bf16x4 t0 = __builtin_amdgcn_ds_read_tr16_b64_v4bf16((LAS bf16x4*)p);
    bf16x4 t1 = __builtin_amdgcn_ds_read_tr16_b64_v4bf16((LAS bf16x4*)(p + 512));
    return __builtin_shufflevector(t0, t1, 0, 1, 2, 3, 4, 5, 6, 7);
}
__device__ __forceinline__ bf16x4 cvt4(f32x4 v) { return __builtin_convertvector(v, bf16x4); }
__device__ __forceinline__ u32x4 pack8(f32x4 lo, f32x4 hi) {
    bf16x4 a = cvt4(lo), b = cvt4(hi);
    bf16x8 c = __builtin_shufflevector(a, b, 0, 1, 2, 3, 4, 5, 6, 7);
    return __builtin_bit_cast(u32x4, c);
}
__device__ __forceinline__ void unpack8_add(u32x4 v, f32x4& lo, f32x4& hi) {
    unsigned a = v[0], b = v[1], c = v[2], d = v[3];
    lo[0] += __uint_as_float(a << 16); lo[1] += __uint_as_float(a & 0xffff0000u);
    lo[2] += __uint_as_float(b << 16); lo[3] += __uint_as_float(b & 0xffff0000u);
    hi[0] += __uint_as_float(c << 16); hi[1] += __uint_as_float(c & 0xffff0000u);
    hi[2] += __uint_as_float(d << 16); hi[3] += __uint_as_float(d & 0xffff0000u);
}
__device__ __forceinline__ float rbf(float x) { return (float)(__bf16)x; }
__device__ __forceinline__ float xmax16_32(float x) {
    unsigned u = __float_as_uint(x);
    auto r = __builtin_amdgcn_permlane16_swap(u, u, false, false);
    unsigned a = r[0], b = r[1];
    x = fmaxf(__uint_as_float(a), __uint_as_float(b));
    u = __float_as_uint(x);
    auto r2 = __builtin_amdgcn_permlane32_swap(u, u, false, false);
    a = r2[0]; b = r2[1];
    return fmaxf(__uint_as_float(a), __uint_as_float(b));
}
__device__ __forceinline__ float xsum16_32(float x) {
    unsigned u = __float_as_uint(x);
    auto r = __builtin_amdgcn_permlane16_swap(u, u, false, false);
    unsigned a = r[0], b = r[1];
    x = __uint_as_float(a) + __uint_as_float(b);
    u = __float_as_uint(x);
    auto r2 = __builtin_amdgcn_permlane32_swap(u, u, false, false);
    a = r2[0]; b = r2[1];
    return __uint_as_float(a) + __uint_as_float(b);
}
__device__ __forceinline__ unsigned ld_flag(GAS unsigned* f) {
    return __builtin_amdgcn_readfirstlane(__hip_atomic_load(f, __ATOMIC_RELAXED, __HIP_MEMORY_SCOPE_AGENT));
}
__device__ __forceinline__ void add_flag(GAS unsigned* f, int lane) {
    if (lane == 0) (void)__hip_atomic_fetch_add(f, 1u, __ATOMIC_RELAXED, __HIP_MEMORY_SCOPE_AGENT);
}
#define LD_FLAG_ASYNC(REG, f) asm volatile("global_load_dword " REG ", %0, off sc1" ::"v"(f) : "memory", REG)
#define RD_FLAG(REG, out) asm volatile("v_readfirstlane_b32 %0, " REG : "=s"(out)::"memory")
__device__ __forceinline__ void add_flag_async(GAS unsigned* f, int lane) {
    unsigned one = 1u;
    if (lane == 0) asm volatile("global_atomic_add %0, %1, off" ::"v"(f), "v"(one) : "memory");
}
__device__ __forceinline__ void wait_ge(GAS unsigned* f, unsigned target, GAS unsigned* hb, int lane) {
    unsigned spins = 0;
    while (ld_flag(f) < target) {
        ++spins;
        if ((spins & 31u) == 0u) {
            add_flag(hb, lane);
            if (spins > SPIN_CAP) break;
        }
        __builtin_amdgcn_s_sleep(1);
    }
    asm volatile("" ::: "memory");
}
struct Frag { bf16x8 a[4], b[4]; };
#define SGB() do { _Pragma("unroll") for (int _i = 0; _i < 8; ++_i) { __builtin_amdgcn_sched_group_barrier(0x8, 1, 0); __builtin_amdgcn_sched_group_barrier(0x100, 1, 0); } __builtin_amdgcn_sched_group_barrier(0x8, 8, 0); } while (0)
__device__ __forceinline__ void nt_read(Frag& f, LAS char* pa, LAS char* pb) {
    f.a[0] = lds_b128(pa);
#pragma unroll
    for (int n = 0; n < 4; ++n) f.b[n] = lds_b128(pb + n * 2048);
#pragma unroll
    for (int m = 1; m < 4; ++m) f.a[m] = lds_b128(pa + m * 2048);
}
__device__ __forceinline__ void nt_mma(f32x4 (&acc)[4][4], const Frag& f) {
#pragma unroll
    for (int m = 0; m < 4; ++m)
#pragma unroll
        for (int n = 0; n < 4; ++n) acc[m][n] = mfma32(f.a[m], f.b[n], acc[m][n]);
}
__device__ __forceinline__ int nt_consume(f32x4 (&acc)[4][4], LAS char* smem, int slot, int nst, int w, int lane) {
    const int r = lane & 15, g = lane >> 4;
    const int c0 = ((g ^ (r >> 1)) << 4), c1 = c0 ^ 64;
    const int oa = ((w & 1) * 64 + r) * 128, ob = 16384 + ((w >> 1) * 64 + r) * 128;
    Frag f0, f1;
    LAS char* s = smem + slot * SLOT;
    nt_read(f0, s + oa + c0, s + ob + c0);
#pragma unroll 1
    for (int it = 0; it < nst - 1; ++it) {
        __builtin_amdgcn_sched_barrier(0);
        nt_read(f1, s + oa + c1, s + ob + c1);
        nt_mma(acc, f0);
        SGB();
        __builtin_amdgcn_sched_barrier(0);
        slot = (slot + 1 == 5) ? 0 : slot + 1;
        s = smem + slot * SLOT;
        bar_lgkm();
        __builtin_amdgcn_sched_barrier(0);
        nt_read(f0, s + oa + c0, s + ob + c0);
        nt_mma(acc, f1);
        SGB();
        __builtin_amdgcn_sched_barrier(0);
    }
    nt_read(f1, s + oa + c1, s + ob + c1);
    nt_mma(acc, f0);
    SGB();
    __builtin_amdgcn_sched_barrier(0);
    slot = (slot + 1 == 5) ? 0 : slot + 1;
    bar_lgkm();
    __builtin_amdgcn_sched_barrier(0);
    nt_mma(acc, f1);
    return slot;
}

struct Frag2 { bf16x8 x[9], w[3]; };
__device__ __forceinline__ void t2_read(Frag2& f, LAS char* px, LAS char* pw) {
    f.w[1] = lds_tr2(pw + 1024);
    f.x[0] = lds_b128(px);
    f.w[2] = lds_tr2(pw + 2048);
    f.w[0] = lds_tr2(pw);
#pragma unroll
    for (int rb = 1; rb < 9; ++rb) f.x[rb] = lds_b128(px + rb * 2048);
}
__device__ __forceinline__ void t2_mma(f32x4 (&aq)[8], f32x4 (&ak)[9], f32x4 (&av)[9], const Frag2& f) {
#pragma unroll
    for (int rb = 0; rb < 9; ++rb) {
        ak[rb] = mfma32(f.w[1], f.x[rb], ak[rb]);
        av[rb] = mfma32(f.x[rb], f.w[2], av[rb]);
        if (rb > 0) aq[rb - 1] = mfma32(f.w[0], f.x[rb], aq[rb - 1]);
    }
}
#define SGB2() do { _Pragma("unroll") for (int _i = 0; _i < 15; ++_i) { __builtin_amdgcn_sched_group_barrier(0x8, 1, 0); __builtin_amdgcn_sched_group_barrier(0x100, 1, 0); } __builtin_amdgcn_sched_group_barrier(0x8, 11, 0); } while (0)

__global__ __launch_bounds__(512, 2) void fused(const __bf16* __restrict__ xa, const __bf16* __restrict__ cre, const __bf16* __restrict__ cim,
                                                const __bf16* __restrict__ hre, const __bf16* __restrict__ him, const __bf16* __restrict__ xb,
                                                const __bf16* __restrict__ wqkv, const __bf16* __restrict__ wo,
                                                char* __restrict__ ws, __bf16* __restrict__ out) {
    __shared__ __attribute__((aligned(1024))) char smem_[163840];
    LAS char* smem = (LAS char*)smem_;
    const int tid = threadIdx.x, lane = tid & 63;
    const int wave = __builtin_amdgcn_readfirstlane(tid >> 6);
    const int bid = blockIdx.x, xcd = bid & 7, idx = bid >> 3;
    const int q = xcd & 3, th = xcd >> 2;
    const bool typeA = q < 2;
    const int ttl = idx >> 2, et = idx & 3, tt = th * 8 + ttl;
    const int t0 = tt * 128, e0 = et * 128;
    const int pg = (q & 1) + 2 * th;
    const int s1 = typeA ? ((pg & 1) * 8 + ttl) * 128 : ((pg >> 1) * 8 + ttl) * 128;
    const int hg = (pg & 1) * 4 + et;
    const int s4 = (xcd * 4 + (idx >> 3)) * 64, n4 = (idx & 7) * 64;
    GAS unsigned* flags = (GAS unsigned*)(ws + WS_FLAGS);
    const int slot3 = typeA ? 3 : 0;
    const int rot1 = (ttl + 2 * et) & 7, rot3 = (2 * ttl + 4 * et) & 15;

    if (wave >= 6) {
        const int lw = wave - 6;
        const int row = 8 * lw + (lane >> 3);
        const int ch = ((lane & 7) ^ (4 * lw + (lane >> 4))) * 16;
        const int cht = ((lane & 7) ^ (2 * (((lane >> 4) & 1) | (lw << 1)))) * 16;
        const bool master = (bid == 0 && lw == 0);
        rsrc_t rc = mk_rsrc((q >> 1) ? cim : cre, SEQ * SEQ * 2);
        const int vb = row * 4096 + ch;
        const int sb = t0 * 4096 + (q & 1) * 2048;
        auto issueC = [&](int st, int slot) {
            const int so = sb + ((st + rot3) & 15) * 128;
#pragma unroll
            for (int u = 0; u < 8; ++u) glds<NT>(rc, smem + slot * SLOT + 16384 + (lw + 2 * u) * 1024, vb, so + 16 * ((u & 4) | ((u + q) & 3)) * 4096);
        };
        int slot = 0;
        if (typeA) {
            rsrc_t rh = mk_rsrc((pg >> 1) ? him : hre, DM * DM * 2);
            const int vh = row * 1024 + ch;
#pragma unroll 1
            for (int st = 0; st < 8; ++st) {
                if (st == 5) bar_vm<32>();
                if (st >= 5) { bar_vm<24>(); if (master) add_flag_async(flags, lane); }
                const int so = e0 * 1024 + ((st + rot1) & 7) * 128;
#pragma unroll
                for (int u = 0; u < 8; ++u) glds<NT>(rh, smem + slot * SLOT + 16384 + (lw + 2 * u) * 1024, vh, so + 16 * u * 1024);
                slot = (slot + 1 == 5) ? 0 : slot + 1;
            }
        } else {
            rsrc_t rq = mk_rsrc(wqkv, DM * 3 * DM * 2);
            const int vq = row * 3072 + cht;
            int pos = 1;
#pragma unroll 1
            for (int st = 0; st < 8; ++st) {
                if (st == 3) bar_vm<24>();
                if (st >= 3) bar_vm<12>();
                LAS char* sl = smem + T2_BASE + pos * T2_SLOT;
                const int so = ((st + rot1) & 7) * 64 * 3072 + hg * 128;
#pragma unroll
                for (int v = 0; v < 4; ++v)
#pragma unroll
                    for (int cg = 0; cg < 3; ++cg)
                        glds<NT>(rq, sl + 18432 + ((lw + 2 * v) * 3 + cg) * 1024, vq, so + 16 * v * 3072 + cg * 1024);
                pos = (pos + 1 == 3) ? 0 : pos + 1;
            }
        }
#pragma unroll 1
        for (int st = 0; st < 16; ++st) {
            if (st >= 5) {
                if (st == 5) bar_vm<32>();
                bar_vm<24>();
            } else if (typeA) {
                if (st < 4) bar_vm<24>(); else bar_only();
                if (master) add_flag_async(flags, lane);
            } else {
                if (st == 1) bar_vm<20>();
                if (st == 2) bar_vm<16>();
                if (st == 3) bar_only();
            }
            issueC(st, slot);
            slot = (slot + 1 == 5) ? 0 : slot + 1;
        }
        rsrc_t rw = mk_rsrc(wo, DM * DM * 2);
        const int vw = row * 1024 + cht;
#pragma unroll 1
        for (int st = 0; st < 4; ++st) {
            bar_vm<24>();
#pragma unroll
            for (int h = 0; h < 2; ++h)
#pragma unroll
                for (int u = 0; u < 4; ++u)
                    glds<NT>(rw, smem + slot * SLOT + h * 16384 + 8192 + (lw + 2 * u) * 1024, vw, ((st * 2 + h) * 64 + 16 * u) * 1024 + n4 * 2);
            slot = (slot + 1 == 5) ? 0 : slot + 1;
        }
        bar_vm<24>(); bar_vm<16>(); bar_vm<8>(); bar_vm<0>();
    } else if (wave >= 4) {
        const int lw = wave - 4;
        const int ch = ((lane & 7) ^ (4 * lw + (lane >> 4))) * 16;
        const int prow = 8 * (lane >> 5) + ((lane >> 3) & 3) + 16 * lw;
        const int row = 8 * lw + (lane >> 3);
        if (typeA) {
            rsrc_t rx = mk_rsrc(xa, SEQ * DM * 2);
            const int va = prow * 1024 + ch;
            int sl = 0;
#pragma unroll 1
            for (int st = 0; st < 8; ++st) {
                if (st == 5) { LD_FLAG_ASYNC("v254", flags); bar_vm<33>(); }
                if (st >= 5) bar_vm<24>();
                const int so = s1 * 1024 + ((st + rot1) & 7) * 128;
#pragma unroll
                for (int u = 0; u < 8; ++u) glds<NT>(rx, smem + sl * SLOT + (lw + 2 * u) * 1024, va, so + (32 * (u >> 1) + 4 * (u & 1)) * 1024);
                sl = (sl + 1 == 5) ? 0 : sl + 1;
            }
            bar_vm<24>(); bar_vm<16>(); bar_vm<8>();
        } else {
            rsrc_t rx = mk_rsrc(xb, SEQ * DM * 2);
            const int vx = row * 1024 + ch;
            const int sxm = s1 * 1024, sxh = (s1 >= 16 ? s1 - 16 : 0) * 1024;
            int pos = 1;
#pragma unroll 1
            for (int st = 0; st < 8; ++st) {
                if (st == 3) { LD_FLAG_ASYNC("v254", flags); bar_vm<19>(); }
                if (st >= 3) bar_vm<9>();
                LAS char* sl = smem + T2_BASE + pos * T2_SLOT;
                const int ko = ((st + rot1) & 7) * 128;
                glds<NT>(rx, sl + lw * 1024, vx, sxh + ko);
#pragma unroll
                for (int u = 0; u < 8; ++u) glds<NT>(rx, sl + 2048 + (lw + 2 * u) * 1024, vx, sxm + 16 * u * 1024 + ko);
                pos = (pos + 1 == 3) ? 0 : pos + 1;
            }
            bar_vm<9>();
        }
        LD_FLAG_ASYNC("v253", flags);
        bar_vm<1>();
        drain_vm();
        if (bid != 0 && lw == 0) {
            unsigned a, b, spins = 0;
            RD_FLAG("v254", a); RD_FLAG("v253", b);
            while (a == b) {
                __builtin_amdgcn_s_sleep(1);
                b = ld_flag(flags);
                if (++spins > SPIN_CAP) break;
            }
        }
        bar_only();
        rsrc_t ra = mk_rsrc(ws + WS_ABT, 1024 * SEQ * 2), ro = mk_rsrc(ws + WS_O, SEQ * DM * 2);
        const int va = prow * 4096 + ch;
        const int sa = ((q >> 1) * 512 + e0) * 4096 + (q & 1) * 2048;
        const int vo = row * 1024 + ch;
        wait_ge(flags + (F1_IDX + q * 4 + et) * FW, 32u, flags, lane);
        if (wave == 4) STAMP(6);
        int slot = slot3;
        GAS unsigned* f2 = flags + (F2_IDX + (s4 >> 7)) * FW;
#pragma unroll 1
        for (int st = 0; st < 16; ++st) {
            if (st == 5) bar_vm<32>();
            if (st >= 5) bar_vm<24>();
            const int so = sa + ((st + rot3) & 15) * 128;
#pragma unroll
            for (int u = 0; u < 8; ++u) glds<16>(ra, smem + slot * SLOT + (lw + 2 * u) * 1024, va, so + (32 * (u >> 1) + 4 * (u & 1)) * 4096);
            slot = (slot + 1 == 5) ? 0 : slot + 1;
            if (st == 12) LD_FLAG_ASYNC("v252", f2);
        }
#pragma unroll 1
        for (int st = 0; st < 4; ++st) {
            bar_vm<24>();
            if (st == 0) {
                unsigned f2v;
                RD_FLAG("v252", f2v);
                if (f2v < 32u) wait_ge(f2, 32u, flags, lane);
            }
#pragma unroll
            for (int h = 0; h < 2; ++h)
#pragma unroll
                for (int u = 0; u < 4; ++u)
                    glds<16>(ro, smem + slot * SLOT + h * 16384 + (lw + 2 * u) * 1024, vo, (s4 + 16 * u) * 1024 + (st * 2 + h) * 128);
            slot = (slot + 1 == 5) ? 0 : slot + 1;
        }
        bar_vm<24>(); bar_vm<16>(); bar_vm<8>(); bar_vm<0>();
    } else {
        const int r = lane & 15, g = lane >> 4;
        if (wave == 0) STAMP(0);
        if (wave == 2) {
            unsigned acc = 0;
            const unsigned G = 1048576;
            rsrc_t r1 = mk_rsrc(ws, 15u << 20), r2 = mk_rsrc(out, 4u << 20), r3 = mk_rsrc((q >> 1) ? cim : cre, 8u << 20), r4 = mk_rsrc(wo, 512u << 10);
            for (unsigned o = lane * G; o < (15u << 20); o += 64 * G) acc ^= __builtin_amdgcn_raw_buffer_load_b32(r1, o + 64, 0, 16);
            for (unsigned o = lane * G; o < (4u << 20); o += 64 * G) acc ^= __builtin_amdgcn_raw_buffer_load_b32(r2, o, 0, 16);
            for (unsigned o = lane * G; o < (8u << 20); o += 64 * G) acc ^= __builtin_amdgcn_raw_buffer_load_b32(r3, o, 0, 16);
            for (unsigned o = lane * G; o < (512u << 10); o += 64 * G) acc ^= __builtin_amdgcn_raw_buffer_load_b32(r4, o, 0, 16);
            asm volatile("" ::"v"(acc));
        }
        if (wave == 0 && bid == 0) {
#pragma unroll
            for (int i = 0; i < 5; ++i) {
                const int fi = 1 + i * 64 + lane;
                if (fi < NFLAGS) __hip_atomic_store(flags + fi * FW, 0u, __ATOMIC_RELAXED, __HIP_MEMORY_SCOPE_AGENT);
            }
            drain_vm();
            add_flag(flags, lane);
        }
        if (typeA) {
            f32x4 acc[4][4] = {};
            bar_lgkm();
            if (wave == 0) STAMP(1);
            nt_consume(acc, smem, 0, 8, wave, lane);
            if (wave == 0) STAMP(2);
            const float sg = (pg >> 1) ? -1.f : 1.f;
            rsrc_t rs = mk_rsrc(ws + WS_ABT, 1024 * SEQ * 2);
            const int vs = r * 4096 + g * 16;
            const int ss = ((pg >> 1) * 512 + e0 + (wave >> 1) * 64) * 4096 + (s1 + (wave & 1) * 64) * 2;
#pragma unroll
            for (int n = 0; n < 4; ++n)
#pragma unroll
                for (int u = 0; u < 2; ++u)
                    __builtin_amdgcn_raw_buffer_store_b128(pack8(acc[2 * u][n] * sg, acc[2 * u + 1][n] * sg), rs, vs + ss + n * 16 * 4096 + u * 64, 0, 16);
            drain_vm();
            add_flag(flags + (F1_IDX + pg * 4 + et) * FW, lane);
            if (wave == 0) STAMP(3);
        } else {
            const int c0 = ((g ^ (r >> 1)) << 4);
            const int ox = r * 128;
            const int qq = (lane & 15) >> 2, p = lane & 3;
            const int ow = 18432 + g * 3072 + qq * 128 + (((2 * wave + (p >> 1)) ^ (2 * ((qq >> 1) | ((g & 1) << 1)))) << 4) + 8 * (p & 1);
            f32x4 aq[8] = {}, ak[9] = {}, av[9] = {};
            Frag2 f0, f1;
            int pos = 1;
            LAS char* sl = smem + T2_BASE + pos * T2_SLOT;
            bar_lgkm();
            if (wave == 0) STAMP(1);
            t2_read(f0, sl + ox + c0, sl + ow);
#pragma unroll 1
            for (int it = 0; it < 7; ++it) {
                __builtin_amdgcn_sched_barrier(0);
                t2_read(f1, sl + ox + (c0 ^ 64), sl + ow + 12288);
                t2_mma(aq, ak, av, f0);
                SGB2();
                __builtin_amdgcn_sched_barrier(0);
                pos = (pos + 1 == 3) ? 0 : pos + 1;
                sl = smem + T2_BASE + pos * T2_SLOT;
                bar_lgkm();
                __builtin_amdgcn_sched_barrier(0);
                t2_read(f0, sl + ox + c0, sl + ow);
                t2_mma(aq, ak, av, f1);
                SGB2();
                __builtin_amdgcn_sched_barrier(0);
            }
            t2_read(f1, sl + ox + (c0 ^ 64), sl + ow + 12288);
            t2_mma(aq, ak, av, f0);
            SGB2();
            __builtin_amdgcn_sched_barrier(0);
            bar_lgkm();
            __builtin_amdgcn_sched_barrier(0);
            t2_mma(aq, ak, av, f1);
            if (wave == 0) STAMP(4);
            const int j = r, par = j & 1;
            const bool uc0 = (4 * g + par) <= j, uc1 = (4 * g + par + 2) <= j;
            const int head = hg * 4 + wave;
            rsrc_t rs = mk_rsrc(ws + WS_O, SEQ * DM * 2);
            const int vs = j * 1024 + g * 8;
            const int ss = s1 * 1024 + head * 32;
            bf16x4 kp = cvt4(ak[0]), vp = cvt4(av[0]);
            const f32x4 z = {0.f, 0.f, 0.f, 0.f};
#pragma unroll
            for (int rb = 1; rb < 9; ++rb) {
                bf16x4 kc = cvt4(ak[rb]), vc = cvt4(av[rb]), qb = cvt4(aq[rb - 1]);
                f32x4 sc = mfma16(kc, qb, z), sp = mfma16(kp, qb, z);
                float c0v = par ? sc[1] : sc[0], c1v = par ? sc[3] : sc[2];
                float p0v = par ? sp[1] : sp[0], p1v = par ? sp[3] : sp[2];
                if (rb == 1 && s1 == 0) { p0v = -4e9f; p1v = -4e9f; }
                float l0 = rbf(uc0 ? c0v : p0v) * 0.25f, l1 = rbf(uc1 ? c1v : p1v) * 0.25f;
                float m = xmax16_32(fmaxf(l0, l1));
                float e0v = __expf(l0 - m), e1v = __expf(l1 - m);
                float inv = __builtin_amdgcn_rcpf(xsum16_32(e0v + e1v));
                __bf16 b0 = (__bf16)(e0v * inv), b1 = (__bf16)(e1v * inv);
                unsigned t0v = (unsigned)__builtin_bit_cast(unsigned short, b0) << (16 * par);
                unsigned t1v = (unsigned)__builtin_bit_cast(unsigned short, b1) << (16 * par);
                u32x2 pc = {uc0 ? t0v : 0u, uc1 ? t1v : 0u}, pp = {uc0 ? 0u : t0v, uc1 ? 0u : t1v};
                f32x4 ov = mfma16(vc, __builtin_bit_cast(bf16x4, pc), z);
                ov = mfma16(vp, __builtin_bit_cast(bf16x4, pp), ov);
                __builtin_amdgcn_raw_buffer_store_b64(__builtin_bit_cast(u32x2, cvt4(ov)), rs, vs, ss + (rb - 1) * 16 * 1024, 16);
                kp = kc; vp = vc;
            }
            drain_vm();
            add_flag(flags + (F2_IDX + (s1 >> 7)) * FW, lane);
            if (wave == 0) STAMP(5);
        }
        f32x4 acc[4][4] = {};
        bar_lgkm();
        if (wave == 0) STAMP(9);
        int slot = nt_consume(acc, smem, slot3, 16, wave, lane);
        if (wave == 0) STAMP(10);
        const int tile = tt * 4 + et;
        rsrc_t rp = mk_rsrc(ws + WS_PART, 64 * 16 * 4 * 2048);
#pragma unroll
        for (int n = 1; n < 4; ++n) {
            const int base = (((tile * 4 + ((q + n) & 3)) * 4 + q) * 4 + wave) * 2048;
            __builtin_amdgcn_raw_buffer_store_b128(pack8(acc[0][n], acc[1][n]), rp, lane * 16 + base, 0, 16);
            __builtin_amdgcn_raw_buffer_store_b128(pack8(acc[2][n], acc[3][n]), rp, lane * 16 + base + 1024, 0, 16);
        }
        f32x4 own[4];
#pragma unroll
        for (int m = 0; m < 4; ++m) own[m] = acc[m][0];
        f32x4 a4[2][2] = {};
        {
            const int c0 = ((g ^ (r >> 1)) << 4);
            const int oo = ((wave >> 1) * 32 + r) * 128;
            const int qq = (lane & 15) >> 2, p = lane & 3;
            const int swz = 2 * ((qq >> 1) | ((g & 1) << 1));
            const int ow0 = 8192 + g * 1024 + qq * 128 + ((((wave & 1) * 4 + (p >> 1)) ^ swz) << 4) + 8 * (p & 1);
            const int ow1 = 8192 + g * 1024 + qq * 128 + ((((wave & 1) * 4 + 2 + (p >> 1)) ^ swz) << 4) + 8 * (p & 1);
#pragma unroll 1
            for (int st = 0; st < 4; ++st) {
                LAS char* sl = smem + slot * SLOT;
#pragma unroll
                for (int h = 0; h < 2; ++h)
#pragma unroll
                    for (int kk = 0; kk < 2; ++kk) {
                        bf16x8 w0 = lds_tr2(sl + h * 16384 + ow0 + kk * 4096), w1 = lds_tr2(sl + h * 16384 + ow1 + kk * 4096);
                        bf16x8 o0 = lds_b128(sl + h * 16384 + oo + (c0 ^ (kk * 64))), o1 = lds_b128(sl + h * 16384 + oo + (c0 ^ (kk * 64)) + 2048);
                        a4[0][0] = mfma32(w0, o0, a4[0][0]);
                        a4[0][1] = mfma32(w0, o1, a4[0][1]);
                        a4[1][0] = mfma32(w1, o0, a4[1][0]);
                        a4[1][1] = mfma32(w1, o1, a4[1][1]);
                    }
                slot = (slot + 1 == 5) ? 0 : slot + 1;
                if (st == 1) {
                    drain_vm();
                    if (lane < 4 && lane != q)
                        (void)__hip_atomic_fetch_add(flags + (F3_IDX + tile * 4 + lane) * FW, 1u, __ATOMIC_RELAXED, __HIP_MEMORY_SCOPE_AGENT);
                }
                if (st + 1 < 4) bar_lgkm();
            }
#pragma unroll
            for (int m = 0; m < 2; ++m)
#pragma unroll
                for (int n = 0; n < 2; ++n)
                    *(bf16x4*)(out + (size_t)(s4 + (wave >> 1) * 32 + n * 16 + r) * 1024 + 512 + n4 + (wave & 1) * 32 + m * 16 + 4 * g) = cvt4(a4[m][n]);
        }
        if (wave == 0) STAMP(11);
        wait_ge(flags + (F3_IDX + tile * 4 + q) * FW, 12u, flags, lane);
        if (wave == 0) STAMP(12);
        u32x4 pv[3][2];
#pragma unroll
        for (int k = 0; k < 3; ++k) {
            const int src = (q + 1 + k) & 3;
            const int base = (((tile * 4 + q) * 4 + src) * 4 + wave) * 2048;
            pv[k][0] = __builtin_amdgcn_raw_buffer_load_b128(rp, lane * 16, base, 16);
            pv[k][1] = __builtin_amdgcn_raw_buffer_load_b128(rp, lane * 16, base + 1024, 16);
        }
#pragma unroll
        for (int k = 0; k < 3; ++k) {
            unpack8_add(pv[k][0], own[0], own[1]);
            unpack8_add(pv[k][1], own[2], own[3]);
        }
        const int trow = t0 + (wave >> 1) * 64 + q * 16 + r;
#pragma unroll
        for (int u = 0; u < 2; ++u)
            *(u32x4*)(out + (size_t)trow * 1024 + e0 + (wave & 1) * 64 + 32 * u + 8 * g) = pack8(own[2 * u], own[2 * u + 1]);
    }
}

extern "C" void kernel_launch(void* const* d_in, const int* in_sizes, int n_in,
                              void* d_out, int out_size, void* d_ws, size_t ws_size,
                              hipStream_t stream) {
    fused<<<256, 512, 0, stream>>>((const __bf16*)d_in[0], (const __bf16*)d_in[1], (const __bf16*)d_in[2], (const __bf16*)d_in[3],
                                   (const __bf16*)d_in[4], (const __bf16*)d_in[5], (const __bf16*)d_in[6], (const __bf16*)d_in[7],
                                   (char*)d_ws, (__bf16*)d_out);
}
```

```cpp
#define ROT3 (4*et)
#define ROT1 ((ttl+2*et)&7)
#include <hip/hip_runtime.h>
#include <hip/hip_bf16.h>
#include <stdint.h>

#define LAS __attribute__((address_space(3)))
#define GAS __attribute__((address_space(1)))
typedef __attribute__((ext_vector_type(8))) __bf16 bf16x8;
typedef __attribute__((ext_vector_type(4))) __bf16 bf16x4;
typedef __attribute__((ext_vector_type(2))) __bf16 bf16x2;
typedef __attribute__((ext_vector_type(2))) float f32x2;
typedef __attribute__((ext_vector_type(4))) short s16x4;
typedef __attribute__((ext_vector_type(4))) float f32x4;
typedef __attribute__((ext_vector_type(4))) unsigned u32x4;
typedef __attribute__((ext_vector_type(2))) unsigned u32x2;
typedef __amdgpu_buffer_rsrc_t rsrc_t;

#define NOSTAMPS 1
#define STAMP_N 16
#define STAMP_OFF (64u<<20)
#define STAMP(k) do{}while(0)

constexpr int SEQ = 2048, DM = 512;
constexpr int SLOT = 32768;
constexpr int T2_SLOT = 43008;
constexpr int T2_BASE = 32768;

constexpr unsigned WS_FLAGS = 0;
constexpr unsigned WS_ABT   = 1u << 20;
constexpr unsigned WS_O     = WS_ABT + (4u << 20);
constexpr unsigned WS_PART  = WS_O + (2u << 20);
constexpr int FW = 32;
constexpr int F1_IDX = 8, F2_IDX = 72, F3_IDX = 88, NFLAGS = 88 + 256;
constexpr int NT = 0;
#define PB 1
constexpr unsigned SPIN_CAP = 1u << 17;

__device__ __forceinline__ rsrc_t mk_rsrc(const void* p, unsigned bytes) {
    return __builtin_amdgcn_make_buffer_rsrc((void*)p, 0, bytes, 0x00020000);
}
template <int AUX>
__device__ __forceinline__ void glds(rsrc_t r, LAS char* dst, int voff, int soff) {
    __builtin_amdgcn_raw_ptr_buffer_load_lds(r, (LAS void*)dst, 16, voff, soff, 0, AUX);
}
template <int N> __device__ __forceinline__ void bar_vm() {
    asm volatile("s_waitcnt vmcnt(%0)\n\ts_barrier" ::"n"(N) : "memory");
}
__device__ __forceinline__ void bar_only() { asm volatile("s_barrier" ::: "memory"); }
__device__ __forceinline__ void bar_lgkm() {
    asm volatile("s_waitcnt lgkmcnt(0)\n\ts_barrier" ::: "memory");
}
__device__ __forceinline__ void drain_vm() { asm volatile("s_waitcnt vmcnt(0)" ::: "memory"); }
__device__ __forceinline__ f32x4 mfma32(bf16x8 a, bf16x8 b, f32x4 c) {
    return __builtin_amdgcn_mfma_f32_16x16x32_bf16(a, b, c, 0, 0, 0);
}
__device__ __forceinline__ f32x4 mfma16(u32x2 a, u32x2 b, f32x4 c) {
    return __builtin_amdgcn_mfma_f32_16x16x16bf16_1k(__builtin_bit_cast(s16x4, a), __builtin_bit_cast(s16x4, b), c, 0, 0, 0);
}
__device__ __forceinline__ bf16x8 lds_b128(LAS char* p) { return *(LAS bf16x8*)p; }
__device__ __forceinline__ bf16x8 lds_tr2(LAS char* p) {
    bf16x4 t0 = __builtin_amdgcn_ds_read_tr16_b64_v4bf16((LAS bf16x4*)p);
    bf16x4 t1 = __builtin_amdgcn_ds_read_tr16_b64_v4bf16((LAS bf16x4*)(p + 512));
    return __builtin_shufflevector(t0, t1, 0, 1, 2, 3, 4, 5, 6, 7);
}
__device__ __forceinline__ unsigned cvtpk(float a, float b) {
    f32x2 v = {a, b};
    unsigned u = __builtin_bit_cast(unsigned, __builtin_convertvector(v, bf16x2));
    asm("" : "+v"(u));
    return u;
}
__device__ __forceinline__ u32x2 cvt4u(f32x4 v) { u32x2 r = {cvtpk(v[0], v[1]), cvtpk(v[2], v[3])}; return r; }
__device__ __forceinline__ u32x4 pack8(f32x4 lo, f32x4 hi) {
    u32x4 r = {cvtpk(lo[0], lo[1]), cvtpk(lo[2], lo[3]), cvtpk(hi[0], hi[1]), cvtpk(hi[2], hi[3])};
    return r;
}
__device__ __forceinline__ void unpack8_add(u32x4 v, f32x4& lo, f32x4& hi) {
    unsigned a = v[0], b = v[1], c = v[2], d = v[3];
    lo[0] += __uint_as_float(a << 16); lo[1] += __uint_as_float(a & 0xffff0000u);
    lo[2] += __uint_as_float(b << 16); lo[3] += __uint_as_float(b & 0xffff0000u);
    hi[0] += __uint_as_float(c << 16); hi[1] += __uint_as_float(c & 0xffff0000u);
    hi[2] += __uint_as_float(d << 16); hi[3] += __uint_as_float(d & 0xffff0000u);
}
__device__ __forceinline__ float fmx(float a, float b) { return __builtin_elementwise_maximum(a, b); }
__device__ __forceinline__ void swap16(float& a, float& b) {
    auto r = __builtin_amdgcn_permlane16_swap(__float_as_uint(a), __float_as_uint(b), false, false);
    unsigned x = r[0], y = r[1]; a = __uint_as_float(x); b = __uint_as_float(y);
}
__device__ __forceinline__ void swap32(float& a, float& b) {
    auto r = __builtin_amdgcn_permlane32_swap(__float_as_uint(a), __float_as_uint(b), false, false);
    unsigned x = r[0], y = r[1]; a = __uint_as_float(x); b = __uint_as_float(y);
}
template <bool MAX> __device__ __forceinline__ float op2(float a, float b) { return MAX ? fmx(a, b) : a + b; }
template <bool MAX> __device__ __forceinline__ void allred4(float* v) {
    swap16(v[0], v[1]); float c = op2<MAX>(v[0], v[1]);
    swap16(v[2], v[3]); float d = op2<MAX>(v[2], v[3]);
    swap32(c, d); float e = op2<MAX>(c, d);
    if (!MAX) e = __builtin_amdgcn_rcpf(e);
    float e2 = e; swap32(e, e2);
    float f = e; swap16(e, f);
    float h = e2; swap16(e2, h);
    v[0] = e; v[1] = f; v[2] = e2; v[3] = h;
}
__device__ __forceinline__ unsigned ld_flag(GAS unsigned* f) {
    return __builtin_amdgcn_readfirstlane(__hip_atomic_load(f, __ATOMIC_RELAXED, __HIP_MEMORY_SCOPE_AGENT));
}
__device__ __forceinline__ void add_flag(GAS unsigned* f, int lane) {
    if (lane == 0) (void)__hip_atomic_fetch_add(f, 1u, __ATOMIC_RELAXED, __HIP_MEMORY_SCOPE_AGENT);
}
#define LD_FLAG_ASYNC(REG, f) asm volatile("global_load_dword " REG ", %0, off sc1" ::"v"(f) : "memory", REG)
#define RD_FLAG(REG, out) asm volatile("v_readfirstlane_b32 %0, " REG : "=s"(out)::"memory")
#define RD_VEC(REG, out) asm volatile("v_mov_b32 %0, " REG : "=v"(out)::"memory")
__device__ __forceinline__ void add_flag_async(GAS unsigned* f, int lane) {
    unsigned one = 1u;
    if (lane == 0) asm volatile("global_atomic_add %0, %1, off" ::"v"(f), "v"(one) : "memory");
}
__device__ __forceinline__ void wait_ge(GAS unsigned* f, unsigned target, GAS unsigned* hb, int lane) {
    unsigned spins = 0;
    while (ld_flag(f) < target) {
        ++spins;
        if ((spins & 31u) == 0u) {
            if (lane < 8) (void)__hip_atomic_fetch_add(hb + lane * FW, 1u, __ATOMIC_RELAXED, __HIP_MEMORY_SCOPE_AGENT);
            if (spins > SPIN_CAP) break;
        }
        __builtin_amdgcn_s_sleep(1);
    }
    asm volatile("" ::: "memory");
}
struct Frag { bf16x8 a[4], b[4]; };
#define SGB() do { _Pragma("unroll") for (int _i = 0; _i < 8; ++_i) { __builtin_amdgcn_sched_group_barrier(0x8, 1, 0); __builtin_amdgcn_sched_group_barrier(0x100, 1, 0); } __builtin_amdgcn_sched_group_barrier(0x8, 8, 0); } while (0)
__device__ __forceinline__ void nt_read(Frag& f, LAS char* pa, LAS char* pb) {
    f.a[0] = lds_b128(pa);
#pragma unroll
    for (int n = 0; n < 4; ++n) f.b[n] = lds_b128(pb + n * 2048);
#pragma unroll
    for (int m = 1; m < 4; ++m) f.a[m] = lds_b128(pa + m * 2048);
}
__device__ __forceinline__ void nt_mma(f32x4 (&acc)[4][4], const Frag& f) {
#pragma unroll
    for (int m = 0; m < 4; ++m)
#pragma unroll
        for (int n = 0; n < 4; ++n) acc[m][n] = mfma32(f.a[m], f.b[n], acc[m][n]);
}
template <bool LASTBAR, typename Hook>
__device__ __forceinline__ int nt_consume(f32x4 (&acc)[4][4], LAS char* smem, int slot, int nst, int w, int lane, Hook hook) {
    const int r = lane & 15, g = lane >> 4;
    const int c0 = ((g ^ (r >> 1)) << 4), c1 = c0 ^ 64;
    const int oa = ((w & 1) * 64 + r) * 128, ob = 16384 + ((w >> 1) * 64 + r) * 128;
    Frag f0, f1;
    LAS char* s = smem + slot * SLOT;
    nt_read(f0, s + oa + c0, s + ob + c0);
#pragma unroll 1
    for (int it = 0; it < nst - 1; ++it) {
        __builtin_amdgcn_sched_barrier(0);
        nt_read(f1, s + oa + c1, s + ob + c1);
        nt_mma(acc, f0);
        SGB();
        __builtin_amdgcn_sched_barrier(0);
        slot = (slot + 1 == 5) ? 0 : slot + 1;
        s = smem + slot * SLOT;
        hook(it);
        bar_lgkm();
        __builtin_amdgcn_sched_barrier(0);
        nt_read(f0, s + oa + c0, s + ob + c0);
        nt_mma(acc, f1);
        SGB();
        __builtin_amdgcn_sched_barrier(0);
    }
    nt_read(f1, s + oa + c1, s + ob + c1);
    nt_mma(acc, f0);
    SGB();
    __builtin_amdgcn_sched_barrier(0);
    slot = (slot + 1 == 5) ? 0 : slot + 1;
    if (LASTBAR) bar_lgkm();
    __builtin_amdgcn_sched_barrier(0);
    nt_mma(acc, f1);
    return slot;
}

#define PIN4(a, b, c, d) asm volatile("" : "+v"(a), "+v"(b), "+v"(c), "+v"(d))
__device__ __forceinline__ void t2_pin(f32x4 (&aq)[8], f32x4 (&ak)[9], f32x4 (&av)[9]) {
    asm volatile("" : "+v"(aq[0]), "+v"(aq[1]), "+v"(aq[2]), "+v"(aq[3]), "+v"(aq[4]), "+v"(aq[5]), "+v"(aq[6]), "+v"(aq[7]),
                      "+v"(ak[0]), "+v"(ak[1]), "+v"(ak[2]), "+v"(ak[3]), "+v"(ak[4]));
    asm volatile("" : "+v"(ak[5]), "+v"(ak[6]), "+v"(ak[7]), "+v"(ak[8]),
                      "+v"(av[0]), "+v"(av[1]), "+v"(av[2]), "+v"(av[3]), "+v"(av[4]), "+v"(av[5]), "+v"(av[6]), "+v"(av[7]), "+v"(av[8]));
}
struct Frag2 { bf16x8 x[9], w[3]; };
__device__ __forceinline__ void t2_read(Frag2& f, LAS char* px, LAS char* pw) {
    f.w[1] = lds_tr2(pw + 1024);
    f.x[0] = lds_b128(px);
    f.w[2] = lds_tr2(pw + 2048);
    f.w[0] = lds_tr2(pw);
#pragma unroll
    for (int rb = 1; rb < 9; ++rb) f.x[rb] = lds_b128(px + rb * 2048);
}
__device__ __forceinline__ void t2_mma(f32x4 (&aq)[8], f32x4 (&ak)[9], f32x4 (&av)[9], const Frag2& f) {
#pragma unroll
    for (int rb = 0; rb < 9; ++rb) {
        ak[rb] = mfma32(f.w[1], f.x[rb], ak[rb]);
        av[rb] = mfma32(f.x[rb], f.w[2], av[rb]);
        if (rb > 0) aq[rb - 1] = mfma32(f.w[0], f.x[rb], aq[rb - 1]);
    }
}
#define SGB2() do { _Pragma("unroll") for (int _i = 0; _i < 15; ++_i) { __builtin_amdgcn_sched_group_barrier(0x8, 1, 0); __builtin_amdgcn_sched_group_barrier(0x100, 1, 0); } __builtin_amdgcn_sched_group_barrier(0x8, 11, 0); } while (0)

__global__ __launch_bounds__(512, 2) void fused(const __bf16* __restrict__ xa, const __bf16* __restrict__ cre, const __bf16* __restrict__ cim,
                                                const __bf16* __restrict__ hre, const __bf16* __restrict__ him, const __bf16* __restrict__ xb,
                                                const __bf16* __restrict__ wqkv, const __bf16* __restrict__ wo,
                                                char* __restrict__ ws, __bf16* __restrict__ out) {
    __shared__ __attribute__((aligned(1024))) char smem_[163840];
    LAS char* smem = (LAS char*)smem_;
    const int tid = threadIdx.x, lane = tid & 63;
    const int wave = __builtin_amdgcn_readfirstlane(tid >> 6);
    const int bid = blockIdx.x, xcd = bid & 7, idx = bid >> 3;
    const int q = xcd & 3, th = xcd >> 2;
    const int ttl = idx >> 2, et = idx & 3, tt = th * 8 + ttl;
    const int t0 = tt * 128, e0 = et * 128;
    const bool typeA = (ttl & 1) == 0;
    const int pg = q;
    const int s1 = typeA ? ((q & 1) * 8 + th * 4 + (ttl >> 1)) * 128 : ((xcd >> 1) * 4 + (ttl >> 1)) * 128;
    const int hg = (xcd & 1) * 4 + et;
    const int s4 = (xcd * 4 + (idx >> 3)) * 64, n4 = (idx & 7) * 64;
    GAS unsigned* flags = (GAS unsigned*)(ws + WS_FLAGS);
    const int slot3 = typeA ? 3 : 0;
    const int rot1 = ROT1, rot3 = ROT3;

    if (wave >= 6) {
        const int lw = wave - 6;
        const int row = 8 * lw + (lane >> 3);
        const int ch = ((lane & 7) ^ (4 * lw + (lane >> 4))) * 16;
        const int cht = ((lane & 7) ^ (2 * (((lane >> 4) & 1) | (lw << 1)))) * 16;
        const bool master = (bid < 8 && lw == 0);
        GAS unsigned* myhb = flags + bid * FW;
        if (master && lane < 42) __hip_atomic_store(flags + (8 + bid * 42 + lane) * FW, 0u, __ATOMIC_RELAXED, __HIP_MEMORY_SCOPE_AGENT);
        rsrc_t rc = mk_rsrc((q >> 1) ? cim : cre, SEQ * SEQ * 2);
        const int vb = row * 4096 + ch;
        const int sb = t0 * 4096 + (q & 1) * 2048;
        auto issueC = [&](int st, int slot) {
            const int so = sb + ((st + rot3) & 15) * 128;
#pragma unroll
            for (int u = 0; u < 8; ++u) glds<NT>(rc, smem + slot * SLOT + 16384 + (lw + 2 * u) * 1024, vb, so + 16 * ((u & 4) | ((u + q) & 3)) * 4096);
        };
        int slot = 0;
        if (typeA) {
            rsrc_t rh = mk_rsrc((pg >> 1) ? him : hre, DM * DM * 2);
            const int vh = row * 1024 + ch;
#pragma unroll 1
            for (int st = 0; st < 8; ++st) {
                if (PB && (st == 1 || st == 2)) bar_only();
                if (st == 5) bar_vm<32>();
                if (st >= 5) { if (master) add_flag_async(myhb, lane); bar_vm<24>(); }
                const int so = e0 * 1024 + ((st + rot1) & 7) * 128;
#pragma unroll
                for (int u = 0; u < 8; ++u) glds<NT>(rh, smem + slot * SLOT + 16384 + (lw + 2 * u) * 1024, vh, so + 16 * u * 1024);
                slot = (slot + 1 == 5) ? 0 : slot + 1;
            }
        } else {
            rsrc_t rq = mk_rsrc(wqkv, DM * 3 * DM * 2);
            const int vq = row * 3072 + cht;
            int pos = 1;
#pragma unroll 1
            for (int st = 0; st < 8; ++st) {
                if (PB && (st == 1 || st == 2)) bar_only();
                if (st == 3) bar_vm<24>();
                if (st >= 3) { if (master && st < 6) add_flag_async(myhb, lane); bar_vm<12>(); }
                LAS char* sl = smem + T2_BASE + pos * T2_SLOT;
                const int so = ((st + rot1) & 7) * 64 * 3072 + hg * 128;
#pragma unroll
                for (int v = 0; v < 4; ++v)
#pragma unroll
                    for (int cg = 0; cg < 3; ++cg)
                        glds<NT>(rq, sl + 18432 + ((lw + 2 * v) * 3 + cg) * 1024, vq, so + 16 * v * 3072 + cg * 1024);
                pos = (pos + 1 == 3) ? 0 : pos + 1;
            }
        }
#pragma unroll 1
        for (int st = 0; st < 16; ++st) {
            if (st >= 5) {
                if (st == 5) { if (typeA) bar_only(); bar_vm<32>(); }
                bar_vm<24>();
            } else if (typeA) {
                if (st < 4) bar_vm<24>(); else bar_only();
            } else {
                if (st == 1) bar_vm<20>();
                if (st == 2) bar_vm<16>();
                if (st == 3) bar_only();
            }
            issueC(st, slot);
            slot = (slot + 1 == 5) ? 0 : slot + 1;
        }
        rsrc_t rw = mk_rsrc(wo, DM * DM * 2);
        const int vw = row * 1024 + cht;
#pragma unroll 1
        for (int st = 0; st < 4; ++st) {
            bar_vm<24>();
#pragma unroll
            for (int h = 0; h < 2; ++h)
#pragma unroll
                for (int u = 0; u < 4; ++u)
                    glds<NT>(rw, smem + slot * SLOT + h * 16384 + 8192 + (lw + 2 * u) * 1024, vw, ((st * 2 + h) * 64 + 16 * u) * 1024 + n4 * 2);
            slot = (slot + 1 == 5) ? 0 : slot + 1;
        }
        bar_vm<8>(); bar_vm<0>();
    } else if (wave >= 4) {
        const int lw = wave - 4;
        const int ch = ((lane & 7) ^ (4 * lw + (lane >> 4))) * 16;
        const int prow = 8 * (lane >> 5) + ((lane >> 3) & 3) + 16 * lw;
        const int row = 8 * lw + (lane >> 3);
        GAS unsigned* hbp = flags + (lane & 7) * FW;
        if (lw == 0) LD_FLAG_ASYNC("v254", hbp);
        if (typeA) {
            rsrc_t rx = mk_rsrc(xa, SEQ * DM * 2);
            const int va = prow * 1024 + ch;
            int sl = 0;
#pragma unroll 1
            for (int st = 0; st < 8; ++st) {
                if (PB && (st == 1 || st == 2)) bar_only();
                if (st == 5) bar_vm<32>();
                if (st >= 5) bar_vm<24>();
                if (st == 7 && lw == 0) LD_FLAG_ASYNC("v253", hbp);
                const int so = s1 * 1024 + ((st + rot1) & 7) * 128;
#pragma unroll
                for (int u = 0; u < 8; ++u) glds<NT>(rx, smem + sl * SLOT + (lw + 2 * u) * 1024, va, so + (32 * (u >> 1) + 4 * (u & 1)) * 1024);
                sl = (sl + 1 == 5) ? 0 : sl + 1;
            }
            bar_vm<24>(); bar_vm<16>(); bar_vm<8>();
        } else {
            rsrc_t rx = mk_rsrc(xb, SEQ * DM * 2);
            const int vx = row * 1024 + ch;
            const int sxm = s1 * 1024, sxh = (s1 >= 16 ? s1 - 16 : 0) * 1024;
            int pos = 1;
#pragma unroll 1
            for (int st = 0; st < 8; ++st) {
                if (PB && (st == 1 || st == 2)) bar_only();
                if (st == 3) bar_vm<18>();
                if (st >= 3) bar_vm<9>();
                if (st == 7 && lw == 0) LD_FLAG_ASYNC("v253", hbp);
                LAS char* sl = smem + T2_BASE + pos * T2_SLOT;
                const int ko = ((st + rot1) & 7) * 128;
                glds<NT>(rx, sl + lw * 1024, vx, sxh + ko);
#pragma unroll
                for (int u = 0; u < 8; ++u) glds<NT>(rx, sl + 2048 + (lw + 2 * u) * 1024, vx, sxm + 16 * u * 1024 + ko);
                pos = (pos + 1 == 3) ? 0 : pos + 1;
            }
            bar_vm<9>();
        }
        bar_vm<0>();
        if (lw == 0) {
            unsigned a, b, spins = 0;
            RD_VEC("v254", a); RD_VEC("v253", b);
            bool moved = (a != b) || ((lane & 7) == bid);
            while (!__all(moved)) {
                if (bid < 8) add_flag(flags + bid * FW, lane);
                __builtin_amdgcn_s_sleep(1);
                b = __hip_atomic_load(hbp, __ATOMIC_RELAXED, __HIP_MEMORY_SCOPE_AGENT);
                moved = moved || (a != b);
                if (++spins > SPIN_CAP) break;
            }
        }
        bar_only();
        rsrc_t ra = mk_rsrc(ws + WS_ABT, 1024 * SEQ * 2), ro = mk_rsrc(ws + WS_O, SEQ * DM * 2);
        const int va = prow * 64 + (ch & 48) + (ch >> 6) * 8192;
        const int sa = (((q >> 1) * 4 + et) * 64 + (q & 1) * 32) * 8192;
        const int vo = row * 32 + (ch & 16) + (ch >> 5) * 65536;
        if (typeA) {
            bar_only();
            if (lw == 0 && lane < 4) (void)__hip_atomic_fetch_add(flags + (F1_IDX + (pg * 4 + et) * 4 + lane) * FW, 1u, __ATOMIC_RELAXED, __HIP_MEMORY_SCOPE_AGENT);
        }
        wait_ge(flags + (F1_IDX + (q * 4 + et) * 4 + lw + 2 * th) * FW, 8u, flags, lane);
        if (wave == 4) STAMP(6);
        int slot = slot3;
        GAS unsigned* f2 = flags + (F2_IDX + (s4 >> 7)) * FW;
#pragma unroll 1
        for (int st = 0; st < 16; ++st) {
            if (st == 5) bar_vm<32>();
            if (st >= 5) bar_vm<24>();
            const int so = sa + ((st + rot3) & 15) * 16384;
#pragma unroll
            for (int u = 0; u < 8; ++u) glds<16>(ra, smem + slot * SLOT + (lw + 2 * u) * 1024, va, so + (32 * (u >> 1) + 4 * (u & 1)) * 64);
            slot = (slot + 1 == 5) ? 0 : slot + 1;
            if (st == 12) LD_FLAG_ASYNC("v252", f2);
        }
#pragma unroll 1
        for (int st = 0; st < 4; ++st) {
            bar_vm<24>();
            if (st == 0) {
                unsigned f2v;
                RD_FLAG("v252", f2v);
                if (f2v < 32u) wait_ge(f2, 32u, flags, lane);
            }
#pragma unroll
            for (int h = 0; h < 2; ++h)
#pragma unroll
                for (int u = 0; u < 4; ++u)
                    glds<16>(ro, smem + slot * SLOT + h * 16384 + (lw + 2 * u) * 1024, vo, (s4 + 16 * u) * 32 + (st * 2 + h) * 4 * 65536);
            slot = (slot + 1 == 5) ? 0 : slot + 1;
        }
        bar_vm<8>(); bar_vm<0>();
    } else {
        const int r = lane & 15, g = lane >> 4;
        if (wave == 0) STAMP(0);
        if (PB) { bar_only(); bar_only(); }
        if (typeA) {
            f32x4 acc[4][4] = {};
            bar_lgkm();
            if (wave == 0) STAMP(1);
            nt_consume<true>(acc, smem, 0, 8, wave, lane, [](int) {});
            if (wave == 0) STAMP(2);
            const unsigned sg = (pg >> 1) ? 0x80008000u : 0u;
            rsrc_t rs = mk_rsrc(ws + WS_ABT, 1024 * SEQ * 2);
            const int vs = r * 64 + g * 16;
            const int ss = (((pg >> 1) * 4 + et) * 64 + (s1 >> 5) + (wave & 1) * 2) * 8192 + (wave >> 1) * 4096;
#pragma unroll
            for (int n = 0; n < 4; ++n)
#pragma unroll
                for (int u = 0; u < 2; ++u)
                    __builtin_amdgcn_raw_buffer_store_b128(pack8(acc[2 * u][n], acc[2 * u + 1][n]) ^ sg, rs, vs + ss + n * 1024 + u * 8192, 0, 16);
            if (wave == 0) STAMP(7);
            bar_vm<0>();
            if (wave == 0) STAMP(3);
        } else {
            const int c0 = ((g ^ (r >> 1)) << 4);
            const int ox = r * 128;
            const int qq = (lane & 15) >> 2, p = lane & 3;
            const int ow = 18432 + g * 3072 + qq * 128 + (((2 * wave + (p >> 1)) ^ (2 * ((qq >> 1) | ((g & 1) << 1)))) << 4) + 8 * (p & 1);
            f32x4 aq[8] = {}, ak[9] = {}, av[9] = {};
            Frag2 f0, f1;
            int pos = 1;
            LAS char* sl = smem + T2_BASE + pos * T2_SLOT;
            bar_lgkm();
            if (wave == 0) STAMP(1);
            t2_read(f0, sl + ox + c0, sl + ow);
#pragma unroll 1
            for (int it = 0; it < 7; ++it) {
                __builtin_amdgcn_sched_barrier(0);
                t2_read(f1, sl + ox + (c0 ^ 64), sl + ow + 12288);
                t2_mma(aq, ak, av, f0);
                SGB2();
                __builtin_amdgcn_sched_barrier(0);
                t2_pin(aq, ak, av);
                pos = (pos + 1 == 3) ? 0 : pos + 1;
                sl = smem + T2_BASE + pos * T2_SLOT;
                bar_lgkm();
                __builtin_amdgcn_sched_barrier(0);
                t2_read(f0, sl + ox + c0, sl + ow);
                t2_mma(aq, ak, av, f1);
                SGB2();
                __builtin_amdgcn_sched_barrier(0);
            }
            t2_read(f1, sl + ox + (c0 ^ 64), sl + ow + 12288);
            t2_mma(aq, ak, av, f0);
            SGB2();
            __builtin_amdgcn_sched_barrier(0);
            t2_pin(aq, ak, av);
            bar_lgkm();
            __builtin_amdgcn_sched_barrier(0);
            t2_mma(aq, ak, av, f1);
            if (wave == 0) STAMP(4);
            const int j = r, par = j & 1;
            const bool uc0 = (4 * g + par) <= j, uc1 = (4 * g + par + 2) <= j;
            const unsigned s0v = par ? 0x01000c0cu : 0x0c0c0100u, s1v = par ? 0x03020c0cu : 0x0c0c0302u, zs = 0x0c0c0c0cu;
            const unsigned selc0 = uc0 ? s0v : zs, selp0 = uc0 ? zs : s0v, selc1 = uc1 ? s1v : zs, selp1 = uc1 ? zs : s1v;
            rsrc_t rs = mk_rsrc(ws + WS_O, SEQ * DM * 2);
            const int vs = j * 32 + g * 8;
            const int ss = (hg * 4 + wave) * 65536 + s1 * 32;
            const f32x4 z = {0.f, 0.f, 0.f, 0.f};
            const float C = 0.25f * 1.44269504088896f;
            u32x2 kb[9], vb[9];
            float l0[8], l1[8], m[8];
#pragma unroll
            for (int i = 0; i < 9; ++i) kb[i] = cvt4u(ak[i]);
#pragma unroll
            for (int b = 0; b < 8; ++b) {
                u32x2 qb = cvt4u(aq[b]);
                f32x4 sc = mfma16(kb[b + 1], qb, z), sp = mfma16(kb[b], qb, z);
                float c0v = par ? sc[1] : sc[0], c1v = par ? sc[3] : sc[2];
                float p0v = par ? sp[1] : sp[0], p1v = par ? sp[3] : sp[2];
                if (b == 0 && s1 == 0) { p0v = -4e9f; p1v = -4e9f; }
                unsigned pk = cvtpk(uc0 ? c0v : p0v, uc1 ? c1v : p1v);
                l0[b] = __uint_as_float(pk << 16); l1[b] = __uint_as_float(pk & 0xffff0000u);
                m[b] = fmx(l0[b], l1[b]);
            }
            allred4<true>(m); allred4<true>(m + 4);
#pragma unroll
            for (int i = 0; i < 9; ++i) vb[i] = cvt4u(av[i]);
#pragma unroll
            for (int b = 0; b < 8; ++b) {
                l0[b] = __builtin_amdgcn_exp2f((l0[b] - m[b]) * C); l1[b] = __builtin_amdgcn_exp2f((l1[b] - m[b]) * C);
                m[b] = l0[b] + l1[b];
            }
            allred4<false>(m); allred4<false>(m + 4);
            f32x4 ov[8];
#pragma unroll
            for (int b = 0; b < 8; ++b) {
                unsigned P = cvtpk(l0[b] * m[b], l1[b] * m[b]);
                u32x2 pc = {__builtin_amdgcn_perm(0u, P, selc0), __builtin_amdgcn_perm(0u, P, selc1)};
                u32x2 pp = {__builtin_amdgcn_perm(0u, P, selp0), __builtin_amdgcn_perm(0u, P, selp1)};
                ov[b] = mfma16(vb[b], pp, mfma16(vb[b + 1], pc, z));
            }
#pragma unroll
            for (int b = 0; b < 8; ++b) __builtin_amdgcn_raw_buffer_store_b64(cvt4u(ov[b]), rs, vs, ss + b * 512, 16);
            if (wave == 0) STAMP(5);
        }
        f32x4 acc[4][4] = {};
        bar_lgkm();
        if (wave == 0) STAMP(9);
        int slot = nt_consume<false>(acc, smem, slot3, 16, wave, lane, [&](int it) {
            if (it == 2 && !typeA) {
                drain_vm();
                add_flag_async(flags + (F2_IDX + (s1 >> 7)) * FW, lane);
            }
        });
        if (wave == 0) STAMP(10);
        const int tile = tt * 4 + et;
        rsrc_t rp = mk_rsrc(ws + WS_PART, 64 * 16 * 4 * 2048);
#pragma unroll
        for (int n = 1; n < 4; ++n) {
            const int base = (((tile * 4 + ((q + n) & 3)) * 4 + q) * 4 + wave) * 2048;
            __builtin_amdgcn_raw_buffer_store_b128(pack8(acc[0][n], acc[1][n]), rp, lane * 16 + base, 0, 16);
            __builtin_amdgcn_raw_buffer_store_b128(pack8(acc[2][n], acc[3][n]), rp, lane * 16 + base + 1024, 0, 16);
        }
        f32x4 own[4];
#pragma unroll
        for (int m = 0; m < 4; ++m) own[m] = acc[m][0];
        f32x4 a4[2][2] = {};
        {
            const int c0 = ((g ^ (r >> 1)) << 4);
            const int oo = ((wave >> 1) * 32 + r) * 128;
            const int qq = (lane & 15) >> 2, p = lane & 3;
            const int swz = 2 * ((qq >> 1) | ((g & 1) << 1));
            const int ow0 = 8192 + g * 1024 + qq * 128 + ((((wave & 1) * 4 + (p >> 1)) ^ swz) << 4) + 8 * (p & 1);
            const int ow1 = ow0 ^ 32;
            bar_only();
#pragma unroll
            for (int st = 0; st < 4; ++st) {
                if (st == 3) {
                    drain_vm();
                    if (lane < 4 && lane != q)
                        (void)__hip_atomic_fetch_add(flags + (F3_IDX + tile * 4 + lane) * FW, 1u, __ATOMIC_RELAXED, __HIP_MEMORY_SCOPE_AGENT);
                    bar_only();
                }
                LAS char* sl = smem + slot * SLOT;
#pragma unroll
                for (int h = 0; h < 2; ++h)
#pragma unroll
                    for (int kk = 0; kk < 2; ++kk) {
                        bf16x8 w0 = lds_tr2(sl + h * 16384 + ow0 + kk * 4096), w1 = lds_tr2(sl + h * 16384 + ow1 + kk * 4096);
                        bf16x8 o0 = lds_b128(sl + h * 16384 + oo + (c0 ^ (kk * 64))), o1 = lds_b128(sl + h * 16384 + oo + (c0 ^ (kk * 64)) + 2048);
                        a4[0][0] = mfma32(w0, o0, a4[0][0]);
                        a4[0][1] = mfma32(w0, o1, a4[0][1]);
                        a4[1][0] = mfma32(w1, o0, a4[1][0]);
                        a4[1][1] = mfma32(w1, o1, a4[1][1]);
                    }
                slot = (slot + 1 == 5) ? 0 : slot + 1;
            }
#pragma unroll
            for (int n = 0; n < 2; ++n) {
                u32x2 lo = cvt4u(a4[0][n]), hi = cvt4u(a4[1][n]);
                auto r0 = __builtin_amdgcn_permlane16_swap(lo[0], hi[0], false, false);
                auto r1 = __builtin_amdgcn_permlane16_swap(lo[1], hi[1], false, false);
                u32x4 v = {r0[0], r1[0], r0[1], r1[1]};
                *(u32x4*)(out + (size_t)(s4 + (wave >> 1) * 32 + n * 16 + r) * 1024 + 512 + n4 + (wave & 1) * 32 + (g & 1) * 16 + (g >> 1) * 8) = v;
            }
        }
        if (wave == 0) STAMP(11);
        wait_ge(flags + (F3_IDX + tile * 4 + q) * FW, 12u, flags, lane);
        if (wave == 0) STAMP(12);
        u32x4 pv[3][2];
#pragma unroll
        for (int k = 0; k < 3; ++k) {
            const int src = (q + 1 + k) & 3;
            const int base = (((tile * 4 + q) * 4 + src) * 4 + wave) * 2048;
            pv[k][0] = __builtin_amdgcn_raw_buffer_load_b128(rp, lane * 16, base, 16);
            pv[k][1] = __builtin_amdgcn_raw_buffer_load_b128(rp, lane * 16, base + 1024, 16);
        }
#pragma unroll
        for (int k = 0; k < 3; ++k) {
            unpack8_add(pv[k][0], own[0], own[1]);
            unpack8_add(pv[k][1], own[2], own[3]);
        }
        const int trow = t0 + (wave >> 1) * 64 + q * 16 + r;
#pragma unroll
        for (int u = 0; u < 2; ++u)
            *(u32x4*)(out + (size_t)trow * 1024 + e0 + (wave & 1) * 64 + 32 * u + 8 * g) = pack8(own[2 * u], own[2 * u + 1]);
    }
}

extern "C" void kernel_launch(void* const* d_in, const int* in_sizes, int n_in,
                              void* d_out, int out_size, void* d_ws, size_t ws_size,
                              hipStream_t stream) {
    fused<<<256, 512, 0, stream>>>((const __bf16*)d_in[0], (const __bf16*)d_in[1], (const __bf16*)d_in[2], (const __bf16*)d_in[3],
                                   (const __bf16*)d_in[4], (const __bf16*)d_in[5], (const __bf16*)d_in[6], (const __bf16*)d_in[7],
                                   (char*)d_ws, (__bf16*)d_out);
}
```

```cpp
#define SA 0
#define SB 13
#define MULT 0
#define DBID 9
#include <hip/hip_runtime.h>
#include <hip/hip_bf16.h>
#include <stdint.h>

#define LAS __attribute__((address_space(3)))
#define GAS __attribute__((address_space(1)))
typedef __attribute__((ext_vector_type(8))) __bf16 bf16x8;
typedef __attribute__((ext_vector_type(4))) __bf16 bf16x4;
typedef __attribute__((ext_vector_type(2))) __bf16 bf16x2;
typedef __attribute__((ext_vector_type(2))) float f32x2;
typedef __attribute__((ext_vector_type(4))) short s16x4;
typedef __attribute__((ext_vector_type(4))) float f32x4;
typedef __attribute__((ext_vector_type(4))) unsigned u32x4;
typedef __attribute__((ext_vector_type(2))) unsigned u32x2;
typedef __amdgpu_buffer_rsrc_t rsrc_t;

#define STAMP_N 16
#define STAMP_OFF (64u<<20)
#define STAMP(k) do{ if ((k)==SA) tsa=__builtin_amdgcn_s_memrealtime(); if ((k)==SB) tsb=__builtin_amdgcn_s_memrealtime(); }while(0)

constexpr int SEQ = 2048, DM = 512;
constexpr int SLOT = 32768;
constexpr int T2_SLOT = 43008;
constexpr int T2_BASE = 32768;

constexpr unsigned WS_FLAGS = 0;
constexpr unsigned WS_ABT   = 1u << 20;
constexpr unsigned WS_O     = WS_ABT + (4u << 20);
constexpr unsigned WS_PART  = WS_O + (2u << 20);
constexpr int FW = 32;
constexpr int F1_IDX = 8, F2_IDX = 72, F3_IDX = 88, NFLAGS = 88 + 256;
constexpr int NT = 0;
#define PB 1
constexpr unsigned SPIN_CAP = 1u << 17;

__device__ __forceinline__ rsrc_t mk_rsrc(const void* p, unsigned bytes) {
    return __builtin_amdgcn_make_buffer_rsrc((void*)p, 0, bytes, 0x00020000);
}
template <int AUX>
__device__ __forceinline__ void glds(rsrc_t r, LAS char* dst, int voff, int soff) {
    __builtin_amdgcn_raw_ptr_buffer_load_lds(r, (LAS void*)dst, 16, voff, soff, 0, AUX);
}
template <int N> __device__ __forceinline__ void bar_vm() {
    asm volatile("s_waitcnt vmcnt(%0)\n\ts_barrier" ::"n"(N) : "memory");
}
__device__ __forceinline__ void bar_only() { asm volatile("s_barrier" ::: "memory"); }
__device__ __forceinline__ void bar_lgkm() {
    asm volatile("s_waitcnt lgkmcnt(0)\n\ts_barrier" ::: "memory");
}
__device__ __forceinline__ void drain_vm() { asm volatile("s_waitcnt vmcnt(0)" ::: "memory"); }
__device__ __forceinline__ f32x4 mfma32(bf16x8 a, bf16x8 b, f32x4 c) {
    return __builtin_amdgcn_mfma_f32_16x16x32_bf16(a, b, c, 0, 0, 0);
}
__device__ __forceinline__ f32x4 mfma16(u32x2 a, u32x2 b, f32x4 c) {
    return __builtin_amdgcn_mfma_f32_16x16x16bf16_1k(__builtin_bit_cast(s16x4, a), __builtin_bit_cast(s16x4, b), c, 0, 0, 0);
}
__device__ __forceinline__ bf16x8 lds_b128(LAS char* p) { return *(LAS bf16x8*)p; }
__device__ __forceinline__ bf16x8 lds_tr2(LAS char* p) {
    bf16x4 t0 = __builtin_amdgcn_ds_read_tr16_b64_v4bf16((LAS bf16x4*)p);
    bf16x4 t1 = __builtin_amdgcn_ds_read_tr16_b64_v4bf16((LAS bf16x4*)(p + 512));
    return __builtin_shufflevector(t0, t1, 0, 1, 2, 3, 4, 5, 6, 7);
}
__device__ __forceinline__ unsigned cvtpk(float a, float b) {
    f32x2 v = {a, b};
    unsigned u = __builtin_bit_cast(unsigned, __builtin_convertvector(v, bf16x2));
    asm("" : "+v"(u));
    return u;
}
__device__ __forceinline__ u32x2 cvt4u(f32x4 v) { u32x2 r = {cvtpk(v[0], v[1]), cvtpk(v[2], v[3])}; return r; }
__device__ __forceinline__ u32x4 pack8(f32x4 lo, f32x4 hi) {
    u32x4 r = {cvtpk(lo[0], lo[1]), cvtpk(lo[2], lo[3]), cvtpk(hi[0], hi[1]), cvtpk(hi[2], hi[3])};
    return r;
}
__device__ __forceinline__ void unpack8_add(u32x4 v, f32x4& lo, f32x4& hi) {
    unsigned a = v[0], b = v[1], c = v[2], d = v[3];
    lo[0] += __uint_as_float(a << 16); lo[1] += __uint_as_float(a & 0xffff0000u);
    lo[2] += __uint_as_float(b << 16); lo[3] += __uint_as_float(b & 0xffff0000u);
    hi[0] += __uint_as_float(c << 16); hi[1] += __uint_as_float(c & 0xffff0000u);
    hi[2] += __uint_as_float(d << 16); hi[3] += __uint_as_float(d & 0xffff0000u);
}
__device__ __forceinline__ float fmx(float a, float b) { return __builtin_elementwise_maximum(a, b); }
__device__ __forceinline__ void swap16(float& a, float& b) {
    auto r = __builtin_amdgcn_permlane16_swap(__float_as_uint(a), __float_as_uint(b), false, false);
    unsigned x = r[0], y = r[1]; a = __uint_as_float(x); b = __uint_as_float(y);
}
__device__ __forceinline__ void swap32(float& a, float& b) {
    auto r = __builtin_amdgcn_permlane32_swap(__float_as_uint(a), __float_as_uint(b), false, false);
    unsigned x = r[0], y = r[1]; a = __uint_as_float(x); b = __uint_as_float(y);
}
template <bool MAX> __device__ __forceinline__ float op2(float a, float b) { return MAX ? fmx(a, b) : a + b; }
template <bool MAX> __device__ __forceinline__ void allred4(float* v) {
    swap16(v[0], v[1]); float c = op2<MAX>(v[0], v[1]);
    swap16(v[2], v[3]); float d = op2<MAX>(v[2], v[3]);
    swap32(c, d); float e = op2<MAX>(c, d);
    if (!MAX) e = __builtin_amdgcn_rcpf(e);
    float e2 = e; swap32(e, e2);
    float f = e; swap16(e, f);
    float h = e2; swap16(e2, h);
    v[0] = e; v[1] = f; v[2] = e2; v[3] = h;
}
__device__ __forceinline__ unsigned ld_flag(GAS unsigned* f) {
    return __builtin_amdgcn_readfirstlane(__hip_atomic_load(f, __ATOMIC_RELAXED, __HIP_MEMORY_SCOPE_AGENT));
}
__device__ __forceinline__ void add_flag(GAS unsigned* f, int lane) {
    if (lane == 0) (void)__hip_atomic_fetch_add(f, 1u, __ATOMIC_RELAXED, __HIP_MEMORY_SCOPE_AGENT);
}
#define LD_FLAG_ASYNC(REG, f) asm volatile("global_load_dword " REG ", %0, off sc1" ::"v"(f) : "memory", REG)
#define RD_FLAG(REG, out) asm volatile("v_readfirstlane_b32 %0, " REG : "=s"(out)::"memory")
#define RD_VEC(REG, out) asm volatile("v_mov_b32 %0, " REG : "=v"(out)::"memory")
__device__ __forceinline__ void add_flag_async(GAS unsigned* f, int lane) {
    unsigned one = 1u;
    if (lane == 0) asm volatile("global_atomic_add %0, %1, off" ::"v"(f), "v"(one) : "memory");
}
__device__ __forceinline__ void wait_ge(GAS unsigned* f, unsigned target, GAS unsigned* hb, int lane) {
    unsigned spins = 0;
    while (ld_flag(f) < target) {
        ++spins;
        if ((spins & 31u) == 0u) {
            if (lane < 8) (void)__hip_atomic_fetch_add(hb + lane * FW, 1u, __ATOMIC_RELAXED, __HIP_MEMORY_SCOPE_AGENT);
            if (spins > SPIN_CAP) break;
        }
        __builtin_amdgcn_s_sleep(1);
    }
    asm volatile("" ::: "memory");
}
struct Frag { bf16x8 a[4], b[4]; };
#define SGB() do { _Pragma("unroll") for (int _i = 0; _i < 8; ++_i) { __builtin_amdgcn_sched_group_barrier(0x8, 1, 0); __builtin_amdgcn_sched_group_barrier(0x100, 1, 0); } __builtin_amdgcn_sched_group_barrier(0x8, 8, 0); } while (0)
__device__ __forceinline__ void nt_read(Frag& f, LAS char* pa, LAS char* pb) {
    f.a[0] = lds_b128(pa);
#pragma unroll
    for (int n = 0; n < 4; ++n) f.b[n] = lds_b128(pb + n * 2048);
#pragma unroll
    for (int m = 1; m < 4; ++m) f.a[m] = lds_b128(pa + m * 2048);
}
__device__ __forceinline__ void nt_mma(f32x4 (&acc)[4][4], const Frag& f) {
#pragma unroll
    for (int m = 0; m < 4; ++m)
#pragma unroll
        for (int n = 0; n < 4; ++n) acc[m][n] = mfma32(f.a[m], f.b[n], acc[m][n]);
}
template <bool LASTBAR, typename Hook>
__device__ __forceinline__ int nt_consume(f32x4 (&acc)[4][4], LAS char* smem, int slot, int nst, int w, int lane, Hook hook) {
    const int r = lane & 15, g = lane >> 4;
    const int c0 = ((g ^ (r >> 1)) << 4), c1 = c0 ^ 64;
    const int oa = ((w & 1) * 64 + r) * 128, ob = 16384 + ((w >> 1) * 64 + r) * 128;
    Frag f0, f1;
    LAS char* s = smem + slot * SLOT;
    nt_read(f0, s + oa + c0, s + ob + c0);
#pragma unroll 1
    for (int it = 0; it < nst - 1; ++it) {
        __builtin_amdgcn_sched_barrier(0);
        nt_read(f1, s + oa + c1, s + ob + c1);
        nt_mma(acc, f0);
        SGB();
        __builtin_amdgcn_sched_barrier(0);
        slot = (slot + 1 == 5) ? 0 : slot + 1;
        s = smem + slot * SLOT;
        hook(it);
        bar_lgkm();
        __builtin_amdgcn_sched_barrier(0);
        nt_read(f0, s + oa + c0, s + ob + c0);
        nt_mma(acc, f1);
        SGB();
        __builtin_amdgcn_sched_barrier(0);
    }
    nt_read(f1, s + oa + c1, s + ob + c1);
    nt_mma(acc, f0);
    SGB();
    __builtin_amdgcn_sched_barrier(0);
    slot = (slot + 1 == 5) ? 0 : slot + 1;
    if (LASTBAR) bar_lgkm();
    __builtin_amdgcn_sched_barrier(0);
    nt_mma(acc, f1);
    return slot;
}

#define PIN4(a, b, c, d) asm volatile("" : "+v"(a), "+v"(b), "+v"(c), "+v"(d))
__device__ __forceinline__ void t2_pin(f32x4 (&aq)[8], f32x4 (&ak)[9], f32x4 (&av)[9]) {
    asm volatile("" : "+v"(aq[0]), "+v"(aq[1]), "+v"(aq[2]), "+v"(aq[3]), "+v"(aq[4]), "+v"(aq[5]), "+v"(aq[6]), "+v"(aq[7]),
                      "+v"(ak[0]), "+v"(ak[1]), "+v"(ak[2]), "+v"(ak[3]), "+v"(ak[4]));
    asm volatile("" : "+v"(ak[5]), "+v"(ak[6]), "+v"(ak[7]), "+v"(ak[8]),
                      "+v"(av[0]), "+v"(av[1]), "+v"(av[2]), "+v"(av[3]), "+v"(av[4]), "+v"(av[5]), "+v"(av[6]), "+v"(av[7]), "+v"(av[8]));
}
struct Frag2 { bf16x8 x[9], w[3]; };
__device__ __forceinline__ void t2_read(Frag2& f, LAS char* px, LAS char* pw) {
    f.w[1] = lds_tr2(pw + 1024);
    f.x[0] = lds_b128(px);
    f.w[2] = lds_tr2(pw + 2048);
    f.w[0] = lds_tr2(pw);
#pragma unroll
    for (int rb = 1; rb < 9; ++rb) f.x[rb] = lds_b128(px + rb * 2048);
}
__device__ __forceinline__ void t2_mma(f32x4 (&aq)[8], f32x4 (&ak)[9], f32x4 (&av)[9], const Frag2& f) {
#pragma unroll
    for (int rb = 0; rb < 9; ++rb) {
        ak[rb] = mfma32(f.w[1], f.x[rb], ak[rb]);
        av[rb] = mfma32(f.x[rb], f.w[2], av[rb]);
        if (rb > 0) aq[rb - 1] = mfma32(f.w[0], f.x[rb], aq[rb - 1]);
    }
}
#define SGB2() do { _Pragma("unroll") for (int _i = 0; _i < 15; ++_i) { __builtin_amdgcn_sched_group_barrier(0x8, 1, 0); __builtin_amdgcn_sched_group_barrier(0x100, 1, 0); } __builtin_amdgcn_sched_group_barrier(0x8, 11, 0); } while (0)

__global__ __launch_bounds__(512, 2) void fused(const __bf16* __restrict__ xa, const __bf16* __restrict__ cre, const __bf16* __restrict__ cim,
                                                const __bf16* __restrict__ hre, const __bf16* __restrict__ him, const __bf16* __restrict__ xb,
                                                const __bf16* __restrict__ wqkv, const __bf16* __restrict__ wo,
                                                char* __restrict__ ws, __bf16* __restrict__ out) {
    __shared__ __attribute__((aligned(1024))) char smem_[163840];
    LAS char* smem = (LAS char*)smem_;
    unsigned long long tsa = 0, tsb = 0;
    const int tid = threadIdx.x, lane = tid & 63;
    const int wave = __builtin_amdgcn_readfirstlane(tid >> 6);
    const int bid = blockIdx.x, xcd = bid & 7, idx = bid >> 3;
    const int q = xcd & 3, th = xcd >> 2;
    const int ttl = idx >> 2, et = idx & 3, tt = th * 8 + ttl;
    const int t0 = tt * 128, e0 = et * 128;
    const bool typeA = (ttl & 1) == 0;
    const int pg = q;
    const int s1 = typeA ? ((q & 1) * 8 + th * 4 + (ttl >> 1)) * 128 : ((xcd >> 1) * 4 + (ttl >> 1)) * 128;
    const int hg = (xcd & 1) * 4 + et;
    const int s4 = (xcd * 4 + (idx >> 3)) * 64, n4 = (idx & 7) * 64;
    GAS unsigned* flags = (GAS unsigned*)(ws + WS_FLAGS);
    const int slot3 = typeA ? 3 : 0;
#define ROT1 0
#define ROT3 0
    const int rot1 = ROT1, rot3 = ROT3;

    if (wave >= 6) {
        const int lw = wave - 6;
        const int row = 8 * lw + (lane >> 3);
        const int ch = ((lane & 7) ^ (4 * lw + (lane >> 4))) * 16;
        const int cht = ((lane & 7) ^ (2 * (((lane >> 4) & 1) | (lw << 1)))) * 16;
        const bool master = (bid < 8 && lw == 0);
        GAS unsigned* myhb = flags + bid * FW;
        if (master && lane < 42) __hip_atomic_store(flags + (8 + bid * 42 + lane) * FW, 0u, __ATOMIC_RELAXED, __HIP_MEMORY_SCOPE_AGENT);
        rsrc_t rc = mk_rsrc((q >> 1) ? cim : cre, SEQ * SEQ * 2);
        const int vb = row * 4096 + ch;
        const int sb = t0 * 4096 + (q & 1) * 2048;
        auto issueC = [&](int st, int slot) {
            const int so = sb + ((st + rot3) & 15) * 128;
#pragma unroll
            for (int u = 0; u < 8; ++u) glds<NT>(rc, smem + slot * SLOT + 16384 + (lw + 2 * u) * 1024, vb, so + 16 * ((u & 4) | ((u + q) & 3)) * 4096);
        };
        int slot = 0;
        if (typeA) {
            rsrc_t rh = mk_rsrc((pg >> 1) ? him : hre, DM * DM * 2);
            const int vh = row * 1024 + ch;
#pragma unroll 1
            for (int st = 0; st < 8; ++st) {
                if (PB && (st == 1 || st == 2)) bar_only();
                if (st == 5) bar_vm<32>();
                if (st >= 5) { if (master) add_flag_async(myhb, lane); bar_vm<24>(); }
                const int so = e0 * 1024 + ((st + rot1) & 7) * 128;
#pragma unroll
                for (int u = 0; u < 8; ++u) glds<NT>(rh, smem + slot * SLOT + 16384 + (lw + 2 * u) * 1024, vh, so + 16 * u * 1024);
                slot = (slot + 1 == 5) ? 0 : slot + 1;
            }
        } else {
            rsrc_t rq = mk_rsrc(wqkv, DM * 3 * DM * 2);
            const int vq = row * 3072 + cht;
            int pos = 1;
#pragma unroll 1
            for (int st = 0; st < 8; ++st) {
                if (PB && (st == 1 || st == 2)) bar_only();
                if (st == 3) bar_vm<24>();
                if (st >= 3) { if (master && st < 6) add_flag_async(myhb, lane); bar_vm<12>(); }
                LAS char* sl = smem + T2_BASE + pos * T2_SLOT;
                const int so = ((st + rot1) & 7) * 64 * 3072 + hg * 128;
#pragma unroll
                for (int v = 0; v < 4; ++v)
#pragma unroll
                    for (int cg = 0; cg < 3; ++cg)
                        glds<NT>(rq, sl + 18432 + ((lw + 2 * v) * 3 + cg) * 1024, vq, so + 16 * v * 3072 + cg * 1024);
                pos = (pos + 1 == 3) ? 0 : pos + 1;
            }
        }
#pragma unroll 1
        for (int st = 0; st < 16; ++st) {
            if (st >= 5) {
                if (st == 5) { if (typeA) bar_only(); bar_vm<32>(); }
                bar_vm<24>();
            } else if (typeA) {
                if (st < 4) bar_vm<24>(); else bar_only();
            } else {
                if (st == 1) bar_vm<20>();
                if (st == 2) bar_vm<16>();
                if (st == 3) bar_only();
            }
            issueC(st, slot);
            slot = (slot + 1 == 5) ? 0 : slot + 1;
        }
        rsrc_t rw = mk_rsrc(wo, DM * DM * 2);
        const int vw = row * 1024 + cht;
#pragma unroll 1
        for (int st = 0; st < 4; ++st) {
            bar_vm<24>();
#pragma unroll
            for (int h = 0; h < 2; ++h)
#pragma unroll
                for (int u = 0; u < 4; ++u)
                    glds<NT>(rw, smem + slot * SLOT + h * 16384 + 8192 + (lw + 2 * u) * 1024, vw, ((st * 2 + h) * 64 + 16 * u) * 1024 + n4 * 2);
            slot = (slot + 1 == 5) ? 0 : slot + 1;
        }
        bar_vm<8>(); bar_vm<0>();
    } else if (wave >= 4) {
        const int lw = wave - 4;
        const int ch = ((lane & 7) ^ (4 * lw + (lane >> 4))) * 16;
        const int prow = 8 * (lane >> 5) + ((lane >> 3) & 3) + 16 * lw;
        const int row = 8 * lw + (lane >> 3);
        GAS unsigned* hbp = flags + (lane & 7) * FW;
        if (lw == 0) LD_FLAG_ASYNC("v254", hbp);
        if (typeA) {
            rsrc_t rx = mk_rsrc(xa, SEQ * DM * 2);
            const int va = prow * 1024 + ch;
            int sl = 0;
#pragma unroll 1
            for (int st = 0; st < 8; ++st) {
                if (PB && (st == 1 || st == 2)) bar_only();
                if (st == 5) bar_vm<32>();
                if (st >= 5) bar_vm<24>();
                if (st == 7 && lw == 0) LD_FLAG_ASYNC("v253", hbp);
                const int so = s1 * 1024 + ((st + rot1) & 7) * 128;
#pragma unroll
                for (int u = 0; u < 8; ++u) glds<NT>(rx, smem + sl * SLOT + (lw + 2 * u) * 1024, va, so + (32 * (u >> 1) + 4 * (u & 1)) * 1024);
                sl = (sl + 1 == 5) ? 0 : sl + 1;
            }
            bar_vm<24>(); bar_vm<16>(); bar_vm<8>();
        } else {
            rsrc_t rx = mk_rsrc(xb, SEQ * DM * 2);
            const int vx = row * 1024 + ch;
            const int sxm = s1 * 1024, sxh = (s1 >= 16 ? s1 - 16 : 0) * 1024;
            int pos = 1;
#pragma unroll 1
            for (int st = 0; st < 8; ++st) {
                if (PB && (st == 1 || st == 2)) bar_only();
                if (st == 3) bar_vm<18>();
                if (st >= 3) bar_vm<9>();
                if (st == 7 && lw == 0) LD_FLAG_ASYNC("v253", hbp);
                LAS char* sl = smem + T2_BASE + pos * T2_SLOT;
                const int ko = ((st + rot1) & 7) * 128;
                glds<NT>(rx, sl + lw * 1024, vx, sxh + ko);
#pragma unroll
                for (int u = 0; u < 8; ++u) glds<NT>(rx, sl + 2048 + (lw + 2 * u) * 1024, vx, sxm + 16 * u * 1024 + ko);
                pos = (pos + 1 == 3) ? 0 : pos + 1;
            }
            bar_vm<9>();
        }
        bar_vm<0>();
        if (lw == 0) {
            unsigned a, b, spins = 0;
            RD_VEC("v254", a); RD_VEC("v253", b);
            bool moved = (a != b) || ((lane & 7) == bid);
            while (!__all(moved)) {
                if (bid < 8) add_flag(flags + bid * FW, lane);
                __builtin_amdgcn_s_sleep(1);
                b = __hip_atomic_load(hbp, __ATOMIC_RELAXED, __HIP_MEMORY_SCOPE_AGENT);
                moved = moved || (a != b);
                if (++spins > SPIN_CAP) break;
            }
        }
        bar_only();
        rsrc_t ra = mk_rsrc(ws + WS_ABT, 1024 * SEQ * 2), ro = mk_rsrc(ws + WS_O, SEQ * DM * 2);
        const int va = prow * 64 + (ch & 48) + (ch >> 6) * 8192;
        const int sa = (((q >> 1) * 4 + et) * 64 + (q & 1) * 32) * 8192;
        const int vo = row * 32 + (ch & 16) + (ch >> 5) * 65536;
        if (typeA) {
            bar_only();
            if (lw == 0 && lane < 4) (void)__hip_atomic_fetch_add(flags + (F1_IDX + (pg * 4 + et) * 4 + lane) * FW, 1u, __ATOMIC_RELAXED, __HIP_MEMORY_SCOPE_AGENT);
        }
        wait_ge(flags + (F1_IDX + (q * 4 + et) * 4 + lw + 2 * th) * FW, 8u, flags, lane);
        if (wave == 4) STAMP(6);
        int slot = slot3;
        GAS unsigned* f2 = flags + (F2_IDX + (s4 >> 7)) * FW;
#pragma unroll 1
        for (int st = 0; st < 16; ++st) {
            if (st == 5) bar_vm<32>();
            if (st >= 5) bar_vm<24>();
            const int so = sa + ((st + rot3) & 15) * 16384;
#pragma unroll
            for (int u = 0; u < 8; ++u) glds<16>(ra, smem + slot * SLOT + (lw + 2 * u) * 1024, va, so + (32 * (u >> 1) + 4 * (u & 1)) * 64);
            slot = (slot + 1 == 5) ? 0 : slot + 1;
            if (st == 12) LD_FLAG_ASYNC("v252", f2);
        }
#pragma unroll 1
        for (int st = 0; st < 4; ++st) {
            bar_vm<24>();
            if (st == 0) {
                unsigned f2v;
                RD_FLAG("v252", f2v);
                if (f2v < 32u) wait_ge(f2, 32u, flags, lane);
            }
#pragma unroll
            for (int h = 0; h < 2; ++h)
#pragma unroll
                for (int u = 0; u < 4; ++u)
                    glds<16>(ro, smem + slot * SLOT + h * 16384 + (lw + 2 * u) * 1024, vo, (s4 + 16 * u) * 32 + (st * 2 + h) * 4 * 65536);
            slot = (slot + 1 == 5) ? 0 : slot + 1;
        }
        bar_vm<8>(); bar_vm<0>();
    } else {
        const int r = lane & 15, g = lane >> 4;
        if (wave == 0) STAMP(0);
        if (PB) { bar_only(); bar_only(); }
        if (typeA) {
            f32x4 acc[4][4] = {};
            bar_lgkm();
            if (wave == 0) STAMP(1);
            nt_consume<true>(acc, smem, 0, 8, wave, lane, [](int) {});
            if (wave == 0) STAMP(2);
            const unsigned sg = (pg >> 1) ? 0x80008000u : 0u;
            rsrc_t rs = mk_rsrc(ws + WS_ABT, 1024 * SEQ * 2);
            const int vs = r * 64 + g * 16;
            const int ss = (((pg >> 1) * 4 + et) * 64 + (s1 >> 5) + (wave & 1) * 2) * 8192 + (wave >> 1) * 4096;
#pragma unroll
            for (int n = 0; n < 4; ++n)
#pragma unroll
                for (int u = 0; u < 2; ++u)
                    __builtin_amdgcn_raw_buffer_store_b128(pack8(acc[2 * u][n], acc[2 * u + 1][n]) ^ sg, rs, vs + ss + n * 1024 + u * 8192, 0, 16);
            if (wave == 0) STAMP(7);
            bar_vm<0>();
            if (wave == 0) STAMP(3);
        } else {
            const int c0 = ((g ^ (r >> 1)) << 4);
            const int ox = r * 128;
            const int qq = (lane & 15) >> 2, p = lane & 3;
            const int ow = 18432 + g * 3072 + qq * 128 + (((2 * wave + (p >> 1)) ^ (2 * ((qq >> 1) | ((g & 1) << 1)))) << 4) + 8 * (p & 1);
            f32x4 aq[8] = {}, ak[9] = {}, av[9] = {};
            Frag2 f0, f1;
            int pos = 1;
            LAS char* sl = smem + T2_BASE + pos * T2_SLOT;
            bar_lgkm();
            if (wave == 0) STAMP(1);
            t2_read(f0, sl + ox + c0, sl + ow);
#pragma unroll 1
            for (int it = 0; it < 7; ++it) {
                __builtin_amdgcn_sched_barrier(0);
                t2_read(f1, sl + ox + (c0 ^ 64), sl + ow + 12288);
                t2_mma(aq, ak, av, f0);
                SGB2();
                __builtin_amdgcn_sched_barrier(0);
                t2_pin(aq, ak, av);
                pos = (pos + 1 == 3) ? 0 : pos + 1;
                sl = smem + T2_BASE + pos * T2_SLOT;
                bar_lgkm();
                __builtin_amdgcn_sched_barrier(0);
                t2_read(f0, sl + ox + c0, sl + ow);
                t2_mma(aq, ak, av, f1);
                SGB2();
                __builtin_amdgcn_sched_barrier(0);
            }
            t2_read(f1, sl + ox + (c0 ^ 64), sl + ow + 12288);
            t2_mma(aq, ak, av, f0);
            SGB2();
            __builtin_amdgcn_sched_barrier(0);
            t2_pin(aq, ak, av);
            bar_lgkm();
            __builtin_amdgcn_sched_barrier(0);
            t2_mma(aq, ak, av, f1);
            if (wave == 0) STAMP(4);
            const int j = r, par = j & 1;
            const bool uc0 = (4 * g + par) <= j, uc1 = (4 * g + par + 2) <= j;
            const unsigned s0v = par ? 0x01000c0cu : 0x0c0c0100u, s1v = par ? 0x03020c0cu : 0x0c0c0302u, zs = 0x0c0c0c0cu;
            const unsigned selc0 = uc0 ? s0v : zs, selp0 = uc0 ? zs : s0v, selc1 = uc1 ? s1v : zs, selp1 = uc1 ? zs : s1v;
            rsrc_t rs = mk_rsrc(ws + WS_O, SEQ * DM * 2);
            const int vs = j * 32 + g * 8;
            const int ss = (hg * 4 + wave) * 65536 + s1 * 32;
            const f32x4 z = {0.f, 0.f, 0.f, 0.f};
            const float C = 0.25f * 1.44269504088896f;
            u32x2 kb[9], vb[9];
            float l0[8], l1[8], m[8];
#pragma unroll
            for (int i = 0; i < 9; ++i) kb[i] = cvt4u(ak[i]);
#pragma unroll
            for (int b = 0; b < 8; ++b) {
                u32x2 qb = cvt4u(aq[b]);
                f32x4 sc = mfma16(kb[b + 1], qb, z), sp = mfma16(kb[b], qb, z);
                float c0v = par ? sc[1] : sc[0], c1v = par ? sc[3] : sc[2];
                float p0v = par ? sp[1] : sp[0], p1v = par ? sp[3] : sp[2];
                if (b == 0 && s1 == 0) { p0v = -4e9f; p1v = -4e9f; }
                unsigned pk = cvtpk(uc0 ? c0v : p0v, uc1 ? c1v : p1v);
                l0[b] = __uint_as_float(pk << 16); l1[b] = __uint_as_float(pk & 0xffff0000u);
                m[b] = fmx(l0[b], l1[b]);
            }
            allred4<true>(m); allred4<true>(m + 4);
#pragma unroll
            for (int i = 0; i < 9; ++i) vb[i] = cvt4u(av[i]);
#pragma unroll
            for (int b = 0; b < 8; ++b) {
                l0[b] = __builtin_amdgcn_exp2f((l0[b] - m[b]) * C); l1[b] = __builtin_amdgcn_exp2f((l1[b] - m[b]) * C);
                m[b] = l0[b] + l1[b];
            }
            allred4<false>(m); allred4<false>(m + 4);
            f32x4 ov[8];
#pragma unroll
            for (int b = 0; b < 8; ++b) {
                unsigned P = cvtpk(l0[b] * m[b], l1[b] * m[b]);
                u32x2 pc = {__builtin_amdgcn_perm(0u, P, selc0), __builtin_amdgcn_perm(0u, P, selc1)};
                u32x2 pp = {__builtin_amdgcn_perm(0u, P, selp0), __builtin_amdgcn_perm(0u, P, selp1)};
                ov[b] = mfma16(vb[b], pp, mfma16(vb[b + 1], pc, z));
            }
#pragma unroll
            for (int b = 0; b < 8; ++b) __builtin_amdgcn_raw_buffer_store_b64(cvt4u(ov[b]), rs, vs, ss + b * 512, 16);
            if (wave == 0) STAMP(5);
        }
        f32x4 acc[4][4] = {};
        bar_lgkm();
        if (wave == 0) STAMP(9);
        int slot = nt_consume<false>(acc, smem, slot3, 16, wave, lane, [&](int it) {
            if (it == 2 && !typeA) {
                drain_vm();
                add_flag_async(flags + (F2_IDX + (s1 >> 7)) * FW, lane);
            }
        });
        if (wave == 0) STAMP(10);
        const int tile = tt * 4 + et;
        rsrc_t rp = mk_rsrc(ws + WS_PART, 64 * 16 * 4 * 2048);
#pragma unroll
        for (int n = 1; n < 4; ++n) {
            const int base = (((tile * 4 + ((q + n) & 3)) * 4 + q) * 4 + wave) * 2048;
            __builtin_amdgcn_raw_buffer_store_b128(pack8(acc[0][n], acc[1][n]), rp, lane * 16 + base, 0, 16);
            __builtin_amdgcn_raw_buffer_store_b128(pack8(acc[2][n], acc[3][n]), rp, lane * 16 + base + 1024, 0, 16);
        }
        f32x4 own[4];
#pragma unroll
        for (int m = 0; m < 4; ++m) own[m] = acc[m][0];
        f32x4 a4[2][2] = {};
        {
            const int c0 = ((g ^ (r >> 1)) << 4);
            const int oo = ((wave >> 1) * 32 + r) * 128;
            const int qq = (lane & 15) >> 2, p = lane & 3;
            const int swz = 2 * ((qq >> 1) | ((g & 1) << 1));
            const int ow0 = 8192 + g * 1024 + qq * 128 + ((((wave & 1) * 4 + (p >> 1)) ^ swz) << 4) + 8 * (p & 1);
            const int ow1 = ow0 ^ 32;
            bar_only();
#pragma unroll
            for (int st = 0; st < 4; ++st) {
                if (st == 3) {
                    drain_vm();
                    if (lane < 4 && lane != q)
                        (void)__hip_atomic_fetch_add(flags + (F3_IDX + tile * 4 + lane) * FW, 1u, __ATOMIC_RELAXED, __HIP_MEMORY_SCOPE_AGENT);
                    bar_only();
                }
                LAS char* sl = smem + slot * SLOT;
#pragma unroll
                for (int h = 0; h < 2; ++h)
#pragma unroll
                    for (int kk = 0; kk < 2; ++kk) {
                        bf16x8 w0 = lds_tr2(sl + h * 16384 + ow0 + kk * 4096), w1 = lds_tr2(sl + h * 16384 + ow1 + kk * 4096);
                        bf16x8 o0 = lds_b128(sl + h * 16384 + oo + (c0 ^ (kk * 64))), o1 = lds_b128(sl + h * 16384 + oo + (c0 ^ (kk * 64)) + 2048);
                        a4[0][0] = mfma32(w0, o0, a4[0][0]);
                        a4[0][1] = mfma32(w0, o1, a4[0][1]);
                        a4[1][0] = mfma32(w1, o0, a4[1][0]);
                        a4[1][1] = mfma32(w1, o1, a4[1][1]);
                    }
                slot = (slot + 1 == 5) ? 0 : slot + 1;
            }
#pragma unroll
            for (int n = 0; n < 2; ++n) {
                u32x2 lo = cvt4u(a4[0][n]), hi = cvt4u(a4[1][n]);
                auto r0 = __builtin_amdgcn_permlane16_swap(lo[0], hi[0], false, false);
                auto r1 = __builtin_amdgcn_permlane16_swap(lo[1], hi[1], false, false);
                u32x4 v = {r0[0], r1[0], r0[1], r1[1]};
                *(u32x4*)(out + (size_t)(s4 + (wave >> 1) * 32 + n * 16 + r) * 1024 + 512 + n4 + (wave & 1) * 32 + (g & 1) * 16 + (g >> 1) * 8) = v;
            }
        }
        if (wave == 0) STAMP(11);
        wait_ge(flags + (F3_IDX + tile * 4 + q) * FW, 12u, flags, lane);
        if (wave == 0) STAMP(12);
        u32x4 pv[3][2];
#pragma unroll
        for (int k = 0; k < 3; ++k) {
            const int src = (q + 1 + k) & 3;
            const int base = (((tile * 4 + q) * 4 + src) * 4 + wave) * 2048;
            pv[k][0] = __builtin_amdgcn_raw_buffer_load_b128(rp, lane * 16, base, 16);
            pv[k][1] = __builtin_amdgcn_raw_buffer_load_b128(rp, lane * 16, base + 1024, 16);
        }
#pragma unroll
        for (int k = 0; k < 3; ++k) {
            unpack8_add(pv[k][0], own[0], own[1]);
            unpack8_add(pv[k][1], own[2], own[3]);
        }
        const int trow = t0 + (wave >> 1) * 64 + q * 16 + r;
#pragma unroll
        for (int u = 0; u < 2; ++u)
            *(u32x4*)(out + (size_t)trow * 1024 + e0 + (wave & 1) * 64 + 32 * u + 8 * g) = pack8(own[2 * u], own[2 * u + 1]);
        drain_vm();
        if (wave == 0) STAMP(13);
        if (wave == 0 && bid == DBID && MULT > 0) {
            unsigned long long d = (tsb - tsa) * MULT, t = __builtin_amdgcn_s_memrealtime();
            if (d > 20000) d = 20000;
            while (__builtin_amdgcn_s_memrealtime() - t < d) __builtin_amdgcn_s_sleep(2);
        }
    }
}

extern "C" void kernel_launch(void* const* d_in, const int* in_sizes, int n_in,
                              void* d_out, int out_size, void* d_ws, size_t ws_size,
                              hipStream_t stream) {
    fused<<<256, 512, 0, stream>>>((const __bf16*)d_in[0], (const __bf16*)d_in[1], (const __bf16*)d_in[2], (const __bf16*)d_in[3],
                                   (const __bf16*)d_in[4], (const __bf16*)d_in[5], (const __bf16*)d_in[6], (const __bf16*)d_in[7],
                                   (char*)d_ws, (__bf16*)d_out);
}
```

```cpp
#define SA 4
#define SB 5
#define MULT 4
#define DBID 33
#include <hip/hip_runtime.h>
#include <hip/hip_bf16.h>
#include <stdint.h>

#define LAS __attribute__((address_space(3)))
#define GAS __attribute__((address_space(1)))
typedef __attribute__((ext_vector_type(8))) __bf16 bf16x8;
typedef __attribute__((ext_vector_type(4))) __bf16 bf16x4;
typedef __attribute__((ext_vector_type(2))) __bf16 bf16x2;
typedef __attribute__((ext_vector_type(2))) float f32x2;
typedef __attribute__((ext_vector_type(4))) short s16x4;
typedef __attribute__((ext_vector_type(4))) float f32x4;
typedef __attribute__((ext_vector_type(4))) unsigned u32x4;
typedef __attribute__((ext_vector_type(2))) unsigned u32x2;
typedef __amdgpu_buffer_rsrc_t rsrc_t;

#define STAMP_N 16
#define STAMP_OFF (64u<<20)
#define STAMP(k) do{ if ((k)==SA) tsa=__builtin_amdgcn_s_memrealtime(); if ((k)==SB) tsb=__builtin_amdgcn_s_memrealtime(); }while(0)

constexpr int SEQ = 2048, DM = 512;
constexpr int SLOT = 32768;
constexpr int T2_SLOT = 43008;
constexpr int T2_BASE = 32768;

constexpr unsigned WS_FLAGS = 0;
constexpr unsigned WS_ABT   = 1u << 20;
constexpr unsigned WS_O     = WS_ABT + (4u << 20);
constexpr unsigned WS_PART  = WS_O + (2u << 20);
constexpr int FW = 32;
constexpr int F1_IDX = 8, F2_IDX = 72, F3_IDX = 88, NFLAGS = 88 + 256;
constexpr int NT = 0;
#define PB 1
constexpr unsigned SPIN_CAP = 1u << 17;

__device__ __forceinline__ rsrc_t mk_rsrc(const void* p, unsigned bytes) {
    return __builtin_amdgcn_make_buffer_rsrc((void*)p, 0, bytes, 0x00020000);
}
template <int AUX>
__device__ __forceinline__ void glds(rsrc_t r, LAS char* dst, int voff, int soff) {
    __builtin_amdgcn_raw_ptr_buffer_load_lds(r, (LAS void*)dst, 16, voff, soff, 0, AUX);
}
template <int N> __device__ __forceinline__ void bar_vm() {
    asm volatile("s_waitcnt vmcnt(%0)\n\ts_barrier" ::"n"(N) : "memory");
}
__device__ __forceinline__ void bar_only() { asm volatile("s_barrier" ::: "memory"); }
__device__ __forceinline__ void bar_lgkm() {
    asm volatile("s_waitcnt lgkmcnt(0)\n\ts_barrier" ::: "memory");
}
__device__ __forceinline__ void drain_vm() { asm volatile("s_waitcnt vmcnt(0)" ::: "memory"); }
__device__ __forceinline__ f32x4 mfma32(bf16x8 a, bf16x8 b, f32x4 c) {
    return __builtin_amdgcn_mfma_f32_16x16x32_bf16(a, b, c, 0, 0, 0);
}
__device__ __forceinline__ f32x4 mfma16(u32x2 a, u32x2 b, f32x4 c) {
    return __builtin_amdgcn_mfma_f32_16x16x16bf16_1k(__builtin_bit_cast(s16x4, a), __builtin_bit_cast(s16x4, b), c, 0, 0, 0);
}
__device__ __forceinline__ bf16x8 lds_b128(LAS char* p) { return *(LAS bf16x8*)p; }
__device__ __forceinline__ bf16x8 lds_tr2(LAS char* p) {
    bf16x4 t0 = __builtin_amdgcn_ds_read_tr16_b64_v4bf16((LAS bf16x4*)p);
    bf16x4 t1 = __builtin_amdgcn_ds_read_tr16_b64_v4bf16((LAS bf16x4*)(p + 512));
    return __builtin_shufflevector(t0, t1, 0, 1, 2, 3, 4, 5, 6, 7);
}
__device__ __forceinline__ unsigned cvtpk(float a, float b) {
    f32x2 v = {a, b};
    unsigned u = __builtin_bit_cast(unsigned, __builtin_convertvector(v, bf16x2));
    asm("" : "+v"(u));
    return u;
}
__device__ __forceinline__ u32x2 cvt4u(f32x4 v) { u32x2 r = {cvtpk(v[0], v[1]), cvtpk(v[2], v[3])}; return r; }
__device__ __forceinline__ u32x4 pack8(f32x4 lo, f32x4 hi) {
    u32x4 r = {cvtpk(lo[0], lo[1]), cvtpk(lo[2], lo[3]), cvtpk(hi[0], hi[1]), cvtpk(hi[2], hi[3])};
    return r;
}
__device__ __forceinline__ void unpack8_add(u32x4 v, f32x4& lo, f32x4& hi) {
    unsigned a = v[0], b = v[1], c = v[2], d = v[3];
    lo[0] += __uint_as_float(a << 16); lo[1] += __uint_as_float(a & 0xffff0000u);
    lo[2] += __uint_as_float(b << 16); lo[3] += __uint_as_float(b & 0xffff0000u);
    hi[0] += __uint_as_float(c << 16); hi[1] += __uint_as_float(c & 0xffff0000u);
    hi[2] += __uint_as_float(d << 16); hi[3] += __uint_as_float(d & 0xffff0000u);
}
__device__ __forceinline__ float fmx(float a, float b) { return __builtin_elementwise_maximum(a, b); }
__device__ __forceinline__ void swap16(float& a, float& b) {
    auto r = __builtin_amdgcn_permlane16_swap(__float_as_uint(a), __float_as_uint(b), false, false);
    unsigned x = r[0], y = r[1]; a = __uint_as_float(x); b = __uint_as_float(y);
}
__device__ __forceinline__ void swap32(float& a, float& b) {
    auto r = __builtin_amdgcn_permlane32_swap(__float_as_uint(a), __float_as_uint(b), false, false);
    unsigned x = r[0], y = r[1]; a = __uint_as_float(x); b = __uint_as_float(y);
}
template <bool MAX> __device__ __forceinline__ float op2(float a, float b) { return MAX ? fmx(a, b) : a + b; }
template <bool MAX> __device__ __forceinline__ void allred4(float* v) {
    swap16(v[0], v[1]); float c = op2<MAX>(v[0], v[1]);
    swap16(v[2], v[3]); float d = op2<MAX>(v[2], v[3]);
    swap32(c, d); float e = op2<MAX>(c, d);
    if (!MAX) e = __builtin_amdgcn_rcpf(e);
    float e2 = e; swap32(e, e2);
    float f = e; swap16(e, f);
    float h = e2; swap16(e2, h);
    v[0] = e; v[1] = f; v[2] = e2; v[3] = h;
}
__device__ __forceinline__ unsigned ld_flag(GAS unsigned* f) {
    return __builtin_amdgcn_readfirstlane(__hip_atomic_load(f, __ATOMIC_RELAXED, __HIP_MEMORY_SCOPE_AGENT));
}
__device__ __forceinline__ void add_flag(GAS unsigned* f, int lane) {
    if (lane == 0) (void)__hip_atomic_fetch_add(f, 1u, __ATOMIC_RELAXED, __HIP_MEMORY_SCOPE_AGENT);
}
#define LD_FLAG_ASYNC(REG, f) asm volatile("global_load_dword " REG ", %0, off sc1" ::"v"(f) : "memory", REG)
#define RD_FLAG(REG, out) asm volatile("v_readfirstlane_b32 %0, " REG : "=s"(out)::"memory")
#define RD_VEC(REG, out) asm volatile("v_mov_b32 %0, " REG : "=v"(out)::"memory")
__device__ __forceinline__ void add_flag_async(GAS unsigned* f, int lane) {
    unsigned one = 1u;
    if (lane == 0) asm volatile("global_atomic_add %0, %1, off" ::"v"(f), "v"(one) : "memory");
}
__device__ __forceinline__ void wait_ge(GAS unsigned* f, unsigned target, GAS unsigned* hb, int lane) {
    unsigned spins = 0;
    while (ld_flag(f) < target) {
        ++spins;
        if ((spins & 31u) == 0u) {
            if (lane < 8) (void)__hip_atomic_fetch_add(hb + lane * FW, 1u, __ATOMIC_RELAXED, __HIP_MEMORY_SCOPE_AGENT);
            if (spins > SPIN_CAP) break;
        }
        __builtin_amdgcn_s_sleep(1);
    }
    asm volatile("" ::: "memory");
}
struct Frag { bf16x8 a[4], b[4]; };
#define SGB() do { _Pragma("unroll") for (int _i = 0; _i < 8; ++_i) { __builtin_amdgcn_sched_group_barrier(0x8, 1, 0); __builtin_amdgcn_sched_group_barrier(0x100, 1, 0); } __builtin_amdgcn_sched_group_barrier(0x8, 8, 0); } while (0)
__device__ __forceinline__ void nt_read(Frag& f, LAS char* pa, LAS char* pb) {
    f.a[0] = lds_b128(pa);
#pragma unroll
    for (int n = 0; n < 4; ++n) f.b[n] = lds_b128(pb + n * 2048);
#pragma unroll
    for (int m = 1; m < 4; ++m) f.a[m] = lds_b128(pa + m * 2048);
}
__device__ __forceinline__ void nt_mma(f32x4 (&acc)[4][4], const Frag& f) {
#pragma unroll
    for (int m = 0; m < 4; ++m)
#pragma unroll
        for (int n = 0; n < 4; ++n) acc[m][n] = mfma32(f.a[m], f.b[n], acc[m][n]);
}
template <bool LASTBAR, typename Hook>
__device__ __forceinline__ int nt_consume(f32x4 (&acc)[4][4], LAS char* smem, int slot, int nst, int w, int lane, Hook hook) {
    const int r = lane & 15, g = lane >> 4;
    const int c0 = ((g ^ (r >> 1)) << 4), c1 = c0 ^ 64;
    const int oa = ((w & 1) * 64 + r) * 128, ob = 16384 + ((w >> 1) * 64 + r) * 128;
    Frag f0, f1;
    LAS char* s = smem + slot * SLOT;
    nt_read(f0, s + oa + c0, s + ob + c0);
#pragma unroll 1
    for (int it = 0; it < nst - 1; ++it) {
        __builtin_amdgcn_sched_barrier(0);
        nt_read(f1, s + oa + c1, s + ob + c1);
        nt_mma(acc, f0);
        SGB();
        __builtin_amdgcn_sched_barrier(0);
        slot = (slot + 1 == 5) ? 0 : slot + 1;
        s = smem + slot * SLOT;
        hook(it);
        bar_lgkm();
        __builtin_amdgcn_sched_barrier(0);
        nt_read(f0, s + oa + c0, s + ob + c0);
        nt_mma(acc, f1);
        SGB();
        __builtin_amdgcn_sched_barrier(0);
    }
    nt_read(f1, s + oa + c1, s + ob + c1);
    nt_mma(acc, f0);
    SGB();
    __builtin_amdgcn_sched_barrier(0);
    slot = (slot + 1 == 5) ? 0 : slot + 1;
    if (LASTBAR) bar_lgkm();
    __builtin_amdgcn_sched_barrier(0);
    nt_mma(acc, f1);
    return slot;
}

#define PIN4(a, b, c, d) asm volatile("" : "+v"(a), "+v"(b), "+v"(c), "+v"(d))
__device__ __forceinline__ void t2_pin(f32x4 (&aq)[8], f32x4 (&ak)[9], f32x4 (&av)[9]) {
    asm volatile("" : "+v"(aq[0]), "+v"(aq[1]), "+v"(aq[2]), "+v"(aq[3]), "+v"(aq[4]), "+v"(aq[5]), "+v"(aq[6]), "+v"(aq[7]),
                      "+v"(ak[0]), "+v"(ak[1]), "+v"(ak[2]), "+v"(ak[3]), "+v"(ak[4]));
    asm volatile("" : "+v"(ak[5]), "+v"(ak[6]), "+v"(ak[7]), "+v"(ak[8]),
                      "+v"(av[0]), "+v"(av[1]), "+v"(av[2]), "+v"(av[3]), "+v"(av[4]), "+v"(av[5]), "+v"(av[6]), "+v"(av[7]), "+v"(av[8]));
}
struct Frag2 { bf16x8 x[9], w[3]; };
__device__ __forceinline__ void t2_read(Frag2& f, LAS char* px, LAS char* pw) {
    f.w[1] = lds_tr2(pw + 1024);
    f.x[0] = lds_b128(px);
    f.w[2] = lds_tr2(pw + 2048);
    f.w[0] = lds_tr2(pw);
#pragma unroll
    for (int rb = 1; rb < 9; ++rb) f.x[rb] = lds_b128(px + rb * 2048);
}
__device__ __forceinline__ void t2_mma(f32x4 (&aq)[8], f32x4 (&ak)[9], f32x4 (&av)[9], const Frag2& f) {
#pragma unroll
    for (int rb = 0; rb < 9; ++rb) {
        ak[rb] = mfma32(f.w[1], f.x[rb], ak[rb]);
        av[rb] = mfma32(f.x[rb], f.w[2], av[rb]);
        if (rb > 0) aq[rb - 1] = mfma32(f.w[0], f.x[rb], aq[rb - 1]);
    }
}
#define SGB2() do { _Pragma("unroll") for (int _i = 0; _i < 15; ++_i) { __builtin_amdgcn_sched_group_barrier(0x8, 1, 0); __builtin_amdgcn_sched_group_barrier(0x100, 1, 0); } __builtin_amdgcn_sched_group_barrier(0x8, 11, 0); } while (0)

__global__ __launch_bounds__(512, 2) void fused(const __bf16* __restrict__ xa, const __bf16* __restrict__ cre, const __bf16* __restrict__ cim,
                                                const __bf16* __restrict__ hre, const __bf16* __restrict__ him, const __bf16* __restrict__ xb,
                                                const __bf16* __restrict__ wqkv, const __bf16* __restrict__ wo,
                                                char* __restrict__ ws, __bf16* __restrict__ out) {
    __shared__ __attribute__((aligned(1024))) char smem_[163840];
    LAS char* smem = (LAS char*)smem_;
    unsigned long long tsa = 0, tsb = 0;
    const int tid = threadIdx.x, lane = tid & 63;
    const int wave = __builtin_amdgcn_readfirstlane(tid >> 6);
    const int bid = blockIdx.x, xcd = bid & 7, idx = bid >> 3;
    const int q = xcd & 3, th = xcd >> 2;
    const int ttl = idx >> 2, et = idx & 3, tt = th * 8 + ttl;
    const int t0 = tt * 128, e0 = et * 128;
    const bool typeA = (ttl & 1) == 0;
    const int pg = q;
    const int s1 = typeA ? ((q & 1) * 8 + th * 4 + (ttl >> 1)) * 128 : ((xcd >> 1) * 4 + (ttl >> 1)) * 128;
    const int hg = (xcd & 1) * 4 + et;
    const int s4 = (xcd * 4 + (idx >> 3)) * 64, n4 = (idx & 7) * 64;
    GAS unsigned* flags = (GAS unsigned*)(ws + WS_FLAGS);
    const int slot3 = typeA ? 3 : 0;
#define ROT1 0
#define ROT3 0
    const int rot1 = ROT1, rot3 = ROT3;

    if (wave >= 6) {
        const int lw = wave - 6;
        const int row = 8 * lw + (lane >> 3);
        const int ch = ((lane & 7) ^ (4 * lw + (lane >> 4))) * 16;
        const int cht = ((lane & 7) ^ (2 * (((lane >> 4) & 1) | (lw << 1)))) * 16;
        const bool master = (bid < 8 && lw == 0);
        GAS unsigned* myhb = flags + bid * FW;
        if (master && lane < 42) __hip_atomic_store(flags + (8 + bid * 42 + lane) * FW, 0u, __ATOMIC_RELAXED, __HIP_MEMORY_SCOPE_AGENT);
        rsrc_t rc = mk_rsrc((q >> 1) ? cim : cre, SEQ * SEQ * 2);
        const int vb = row * 4096 + ch;
        const int sb = t0 * 4096 + (q & 1) * 2048;
        auto issueC = [&](int st, int slot) {
            const int so = sb + ((st + rot3) & 15) * 128;
#pragma unroll
            for (int u = 0; u < 8; ++u) glds<NT>(rc, smem + slot * SLOT + 16384 + (lw + 2 * u) * 1024, vb, so + 16 * ((u & 4) | ((u + q) & 3)) * 4096);
        };
        int slot = 0;
        if (typeA) {
            rsrc_t rh = mk_rsrc((pg >> 1) ? him : hre, DM * DM * 2);
            const int vh = row * 1024 + ch;
#pragma unroll 1
            for (int st = 0; st < 8; ++st) {
                if (PB && (st == 1 || st == 2)) bar_only();
                if (st == 5) bar_vm<32>();
                if (st >= 5) { if (master) add_flag_async(myhb, lane); bar_vm<24>(); }
                const int so = e0 * 1024 + ((st + rot1) & 7) * 128;
#pragma unroll
                for (int u = 0; u < 8; ++u) glds<NT>(rh, smem + slot * SLOT + 16384 + (lw + 2 * u) * 1024, vh, so + 16 * u * 1024);
                slot = (slot + 1 == 5) ? 0 : slot + 1;
            }
        } else {
            rsrc_t rq = mk_rsrc(wqkv, DM * 3 * DM * 2);
            const int vq = row * 3072 + cht;
            int pos = 1;
#pragma unroll 1
            for (int st = 0; st < 8; ++st) {
                if (PB && (st == 1 || st == 2)) bar_only();
                if (st == 3) bar_vm<24>();
                if (st >= 3) { if (master && st < 6) add_flag_async(myhb, lane); bar_vm<12>(); }
                LAS char* sl = smem + T2_BASE + pos * T2_SLOT;
                const int so = ((st + rot1) & 7) * 64 * 3072 + hg * 128;
#pragma unroll
                for (int v = 0; v < 4; ++v)
#pragma unroll
                    for (int cg = 0; cg < 3; ++cg)
                        glds<NT>(rq, sl + 18432 + ((lw + 2 * v) * 3 + cg) * 1024, vq, so + 16 * v * 3072 + cg * 1024);
                pos = (pos + 1 == 3) ? 0 : pos + 1;
            }
        }
#pragma unroll 1
        for (int st = 0; st < 16; ++st) {
            if (st >= 5) {
                if (st == 5) { if (typeA) bar_only(); bar_vm<32>(); }
                bar_vm<24>();
            } else if (typeA) {
                if (st < 4) bar_vm<24>(); else bar_only();
            } else {
                if (st == 1) bar_vm<20>();
                if (st == 2) bar_vm<16>();
                if (st == 3) bar_only();
            }
            issueC(st, slot);
            slot = (slot + 1 == 5) ? 0 : slot + 1;
        }
        rsrc_t rw = mk_rsrc(wo, DM * DM * 2);
        const int vw = row * 1024 + cht;
#pragma unroll 1
        for (int st = 0; st < 4; ++st) {
            bar_vm<24>();
#pragma unroll
            for (int h = 0; h < 2; ++h)
#pragma unroll
                for (int u = 0; u < 4; ++u)
                    glds<NT>(rw, smem + slot * SLOT + h * 16384 + 8192 + (lw + 2 * u) * 1024, vw, ((st * 2 + h) * 64 + 16 * u) * 1024 + n4 * 2);
            slot = (slot + 1 == 5) ? 0 : slot + 1;
        }
        bar_vm<8>(); bar_vm<0>();
    } else if (wave >= 4) {
        const int lw = wave - 4;
        const int ch = ((lane & 7) ^ (4 * lw + (lane >> 4))) * 16;
        const int prow = 8 * (lane >> 5) + ((lane >> 3) & 3) + 16 * lw;
        const int row = 8 * lw + (lane >> 3);
        GAS unsigned* hbp = flags + (lane & 7) * FW;
        if (lw == 0) LD_FLAG_ASYNC("v254", hbp);
        if (typeA) {
            rsrc_t rx = mk_rsrc(xa, SEQ * DM * 2);
            const int va = prow * 1024 + ch;
            int sl = 0;
#pragma unroll 1
            for (int st = 0; st < 8; ++st) {
                if (PB && (st == 1 || st == 2)) bar_only();
                if (st == 5) bar_vm<32>();
                if (st >= 5) bar_vm<24>();
                if (st == 7 && lw == 0) LD_FLAG_ASYNC("v253", hbp);
                const int so = s1 * 1024 + ((st + rot1) & 7) * 128;
#pragma unroll
                for (int u = 0; u < 8; ++u) glds<NT>(rx, smem + sl * SLOT + (lw + 2 * u) * 1024, va, so + (32 * (u >> 1) + 4 * (u & 1)) * 1024);
                sl = (sl + 1 == 5) ? 0 : sl + 1;
            }
            bar_vm<24>(); bar_vm<16>(); bar_vm<8>();
        } else {
            rsrc_t rx = mk_rsrc(xb, SEQ * DM * 2);
            const int vx = row * 1024 + ch;
            const int sxm = s1 * 1024, sxh = (s1 >= 16 ? s1 - 16 : 0) * 1024;
            int pos = 1;
#pragma unroll 1
            for (int st = 0; st < 8; ++st) {
                if (PB && (st == 1 || st == 2)) bar_only();
                if (st == 3) bar_vm<18>();
                if (st >= 3) bar_vm<9>();
                if (st == 7 && lw == 0) LD_FLAG_ASYNC("v253", hbp);
                LAS char* sl = smem + T2_BASE + pos * T2_SLOT;
                const int ko = ((st + rot1) & 7) * 128;
                glds<NT>(rx, sl + lw * 1024, vx, sxh + ko);
#pragma unroll
                for (int u = 0; u < 8; ++u) glds<NT>(rx, sl + 2048 + (lw + 2 * u) * 1024, vx, sxm + 16 * u * 1024 + ko);
                pos = (pos + 1 == 3) ? 0 : pos + 1;
            }
            bar_vm<9>();
        }
        bar_vm<0>();
        if (lw == 0) {
            unsigned a, b, spins = 0;
            RD_VEC("v254", a); RD_VEC("v253", b);
            bool moved = (a != b) || ((lane & 7) == bid);
            while (!__all(moved)) {
                if (bid < 8) add_flag(flags + bid * FW, lane);
                __builtin_amdgcn_s_sleep(1);
                b = __hip_atomic_load(hbp, __ATOMIC_RELAXED, __HIP_MEMORY_SCOPE_AGENT);
                moved = moved || (a != b);
                if (++spins > SPIN_CAP) break;
            }
        }
        bar_only();
        rsrc_t ra = mk_rsrc(ws + WS_ABT, 1024 * SEQ * 2), ro = mk_rsrc(ws + WS_O, SEQ * DM * 2);
        const int va = prow * 64 + (ch & 48) + (ch >> 6) * 8192;
        const int sa = (((q >> 1) * 4 + et) * 64 + (q & 1) * 32) * 8192;
        const int vo = row * 32 + (ch & 16) + (ch >> 5) * 65536;
        if (typeA) {
            bar_only();
            if (lw == 0 && lane < 4) (void)__hip_atomic_fetch_add(flags + (F1_IDX + (pg * 4 + et) * 4 + lane) * FW, 1u, __ATOMIC_RELAXED, __HIP_MEMORY_SCOPE_AGENT);
        }
        wait_ge(flags + (F1_IDX + (q * 4 + et) * 4 + lw + 2 * th) * FW, 8u, flags, lane);
        if (wave == 4) STAMP(6);
        int slot = slot3;
        GAS unsigned* f2 = flags + (F2_IDX + (s4 >> 7)) * FW;
#pragma unroll 1
        for (int st = 0; st < 16; ++st) {
            if (st == 5) bar_vm<32>();
            if (st >= 5) bar_vm<24>();
            const int so = sa + ((st + rot3) & 15) * 16384;
#pragma unroll
            for (int u = 0; u < 8; ++u) glds<16>(ra, smem + slot * SLOT + (lw + 2 * u) * 1024, va, so + (32 * (u >> 1) + 4 * (u & 1)) * 64);
            slot = (slot + 1 == 5) ? 0 : slot + 1;
            if (st == 12) LD_FLAG_ASYNC("v252", f2);
        }
#pragma unroll 1
        for (int st = 0; st < 4; ++st) {
            bar_vm<24>();
            if (st == 0) {
                unsigned f2v;
                RD_FLAG("v252", f2v);
                if (f2v < 32u) wait_ge(f2, 32u, flags, lane);
            }
#pragma unroll
            for (int h = 0; h < 2; ++h)
#pragma unroll
                for (int u = 0; u < 4; ++u)
                    glds<16>(ro, smem + slot * SLOT + h * 16384 + (lw + 2 * u) * 1024, vo, (s4 + 16 * u) * 32 + (st * 2 + h) * 4 * 65536);
            slot = (slot + 1 == 5) ? 0 : slot + 1;
        }
        bar_vm<8>(); bar_vm<0>();
    } else {
        const int r = lane & 15, g = lane >> 4;
        if (wave == 0) STAMP(0);
        if (PB) { bar_only(); bar_only(); }
        if (typeA) {
            f32x4 acc[4][4] = {};
            bar_lgkm();
            if (wave == 0) STAMP(1);
            nt_consume<true>(acc, smem, 0, 8, wave, lane, [](int) {});
            if (wave == 0) STAMP(2);
            const unsigned sg = (pg >> 1) ? 0x80008000u : 0u;
            rsrc_t rs = mk_rsrc(ws + WS_ABT, 1024 * SEQ * 2);
            const int vs = r * 64 + g * 16;
            const int ss = (((pg >> 1) * 4 + et) * 64 + (s1 >> 5) + (wave & 1) * 2) * 8192 + (wave >> 1) * 4096;
#pragma unroll
            for (int n = 0; n < 4; ++n)
#pragma unroll
                for (int u = 0; u < 2; ++u)
                    __builtin_amdgcn_raw_buffer_store_b128(pack8(acc[2 * u][n], acc[2 * u + 1][n]) ^ sg, rs, vs + ss + n * 1024 + u * 8192, 0, 16);
            if (wave == 0) STAMP(7);
            bar_vm<0>();
            if (wave == 0) STAMP(3);
        } else {
            const int c0 = ((g ^ (r >> 1)) << 4);
            const int ox = r * 128;
            const int qq = (lane & 15) >> 2, p = lane & 3;
            const int ow = 18432 + g * 3072 + qq * 128 + (((2 * wave + (p >> 1)) ^ (2 * ((qq >> 1) | ((g & 1) << 1)))) << 4) + 8 * (p & 1);
            f32x4 aq[8] = {}, ak[9] = {}, av[9] = {};
            Frag2 f0, f1;
            int pos = 1;
            LAS char* sl = smem + T2_BASE + pos * T2_SLOT;
            bar_lgkm();
            if (wave == 0) STAMP(1);
            t2_read(f0, sl + ox + c0, sl + ow);
#pragma unroll 1
            for (int it = 0; it < 7; ++it) {
                __builtin_amdgcn_sched_barrier(0);
                t2_read(f1, sl + ox + (c0 ^ 64), sl + ow + 12288);
                t2_mma(aq, ak, av, f0);
                SGB2();
                __builtin_amdgcn_sched_barrier(0);
                t2_pin(aq, ak, av);
                pos = (pos + 1 == 3) ? 0 : pos + 1;
                sl = smem + T2_BASE + pos * T2_SLOT;
                bar_lgkm();
                __builtin_amdgcn_sched_barrier(0);
                t2_read(f0, sl + ox + c0, sl + ow);
                t2_mma(aq, ak, av, f1);
                SGB2();
                __builtin_amdgcn_sched_barrier(0);
            }
            t2_read(f1, sl + ox + (c0 ^ 64), sl + ow + 12288);
            t2_mma(aq, ak, av, f0);
            SGB2();
            __builtin_amdgcn_sched_barrier(0);
            t2_pin(aq, ak, av);
            bar_lgkm();
            __builtin_amdgcn_sched_barrier(0);
            t2_mma(aq, ak, av, f1);
            if (wave == 0) STAMP(4);
            const int j = r, par = j & 1;
            const bool uc0 = (4 * g + par) <= j, uc1 = (4 * g + par + 2) <= j;
            const unsigned s0v = par ? 0x01000c0cu : 0x0c0c0100u, s1v = par ? 0x03020c0cu : 0x0c0c0302u, zs = 0x0c0c0c0cu;
            const unsigned selc0 = uc0 ? s0v : zs, selp0 = uc0 ? zs : s0v, selc1 = uc1 ? s1v : zs, selp1 = uc1 ? zs : s1v;
            rsrc_t rs = mk_rsrc(ws + WS_O, SEQ * DM * 2);
            const int vs = j * 32 + g * 8;
            const int ss = (hg * 4 + wave) * 65536 + s1 * 32;
            const f32x4 z = {0.f, 0.f, 0.f, 0.f};
            const float C = 0.25f * 1.44269504088896f;
            u32x2 kb[9], vb[9];
            float l0[8], l1[8], m[8];
#pragma unroll
            for (int i = 0; i < 9; ++i) kb[i] = cvt4u(ak[i]);
#pragma unroll
            for (int b = 0; b < 8; ++b) {
                u32x2 qb = cvt4u(aq[b]);
                f32x4 sc = mfma16(kb[b + 1], qb, z), sp = mfma16(kb[b], qb, z);
                float c0v = par ? sc[1] : sc[0], c1v = par ? sc[3] : sc[2];
                float p0v = par ? sp[1] : sp[0], p1v = par ? sp[3] : sp[2];
                if (b == 0 && s1 == 0) { p0v = -4e9f; p1v = -4e9f; }
                unsigned pk = cvtpk(uc0 ? c0v : p0v, uc1 ? c1v : p1v);
                l0[b] = __uint_as_float(pk << 16); l1[b] = __uint_as_float(pk & 0xffff0000u);
                m[b] = fmx(l0[b], l1[b]);
            }
            allred4<true>(m); allred4<true>(m + 4);
#pragma unroll
            for (int i = 0; i < 9; ++i) vb[i] = cvt4u(av[i]);
#pragma unroll
            for (int b = 0; b < 8; ++b) {
                l0[b] = __builtin_amdgcn_exp2f((l0[b] - m[b]) * C); l1[b] = __builtin_amdgcn_exp2f((l1[b] - m[b]) * C);
                m[b] = l0[b] + l1[b];
            }
            allred4<false>(m); allred4<false>(m + 4);
            f32x4 ov[8];
#pragma unroll
            for (int b = 0; b < 8; ++b) {
                unsigned P = cvtpk(l0[b] * m[b], l1[b] * m[b]);
                u32x2 pc = {__builtin_amdgcn_perm(0u, P, selc0), __builtin_amdgcn_perm(0u, P, selc1)};
                u32x2 pp = {__builtin_amdgcn_perm(0u, P, selp0), __builtin_amdgcn_perm(0u, P, selp1)};
                ov[b] = mfma16(vb[b], pp, mfma16(vb[b + 1], pc, z));
            }
#pragma unroll
            for (int b = 0; b < 8; ++b) __builtin_amdgcn_raw_buffer_store_b64(cvt4u(ov[b]), rs, vs, ss + b * 512, 16);
            if (wave == 0) STAMP(5);
        }
        f32x4 acc[4][4] = {};
        bar_lgkm();
        if (wave == 0) STAMP(9);
        int slot = nt_consume<false>(acc, smem, slot3, 16, wave, lane, [&](int it) {
            if (it == 2 && !typeA) {
                drain_vm();
                add_flag_async(flags + (F2_IDX + (s1 >> 7)) * FW, lane);
            }
        });
        if (wave == 0) STAMP(10);
        const int tile = tt * 4 + et;
        rsrc_t rp = mk_rsrc(ws + WS_PART, 64 * 16 * 4 * 2048);
#pragma unroll
        for (int n = 1; n < 4; ++n) {
            const int base = (((tile * 4 + ((q + n) & 3)) * 4 + q) * 4 + wave) * 2048;
            __builtin_amdgcn_raw_buffer_store_b128(pack8(acc[0][n], acc[1][n]), rp, lane * 16 + base, 0, 16);
            __builtin_amdgcn_raw_buffer_store_b128(pack8(acc[2][n], acc[3][n]), rp, lane * 16 + base + 1024, 0, 16);
        }
        f32x4 own[4];
#pragma unroll
        for (int m = 0; m < 4; ++m) own[m] = acc[m][0];
        f32x4 a4[2][2] = {};
        {
            const int c0 = ((g ^ (r >> 1)) << 4);
            const int oo = ((wave >> 1) * 32 + r) * 128;
            const int qq = (lane & 15) >> 2, p = lane & 3;
            const int swz = 2 * ((qq >> 1) | ((g & 1) << 1));
            const int ow0 = 8192 + g * 1024 + qq * 128 + ((((wave & 1) * 4 + (p >> 1)) ^ swz) << 4) + 8 * (p & 1);
            const int ow1 = ow0 ^ 32;
            bar_only();
#pragma unroll
            for (int st = 0; st < 4; ++st) {
                if (st == 3) {
                    drain_vm();
                    if (lane < 4 && lane != q)
                        (void)__hip_atomic_fetch_add(flags + (F3_IDX + tile * 4 + lane) * FW, 1u, __ATOMIC_RELAXED, __HIP_MEMORY_SCOPE_AGENT);
                    bar_only();
                }
                LAS char* sl = smem + slot * SLOT;
#pragma unroll
                for (int h = 0; h < 2; ++h)
#pragma unroll
                    for (int kk = 0; kk < 2; ++kk) {
                        bf16x8 w0 = lds_tr2(sl + h * 16384 + ow0 + kk * 4096), w1 = lds_tr2(sl + h * 16384 + ow1 + kk * 4096);
                        bf16x8 o0 = lds_b128(sl + h * 16384 + oo + (c0 ^ (kk * 64))), o1 = lds_b128(sl + h * 16384 + oo + (c0 ^ (kk * 64)) + 2048);
                        a4[0][0] = mfma32(w0, o0, a4[0][0]);
                        a4[0][1] = mfma32(w0, o1, a4[0][1]);
                        a4[1][0] = mfma32(w1, o0, a4[1][0]);
                        a4[1][1] = mfma32(w1, o1, a4[1][1]);
                    }
                slot = (slot + 1 == 5) ? 0 : slot + 1;
            }
#pragma unroll
            for (int n = 0; n < 2; ++n) {
                u32x2 lo = cvt4u(a4[0][n]), hi = cvt4u(a4[1][n]);
                auto r0 = __builtin_amdgcn_permlane16_swap(lo[0], hi[0], false, false);
                auto r1 = __builtin_amdgcn_permlane16_swap(lo[1], hi[1], false, false);
                u32x4 v = {r0[0], r1[0], r0[1], r1[1]};
                *(u32x4*)(out + (size_t)(s4 + (wave >> 1) * 32 + n * 16 + r) * 1024 + 512 + n4 + (wave & 1) * 32 + (g & 1) * 16 + (g >> 1) * 8) = v;
            }
        }
        if (wave == 0) STAMP(11);
        wait_ge(flags + (F3_IDX + tile * 4 + q) * FW, 12u, flags, lane);
        if (wave == 0) STAMP(12);
        u32x4 pv[3][2];
#pragma unroll
        for (int k = 0; k < 3; ++k) {
            const int src = (q + 1 + k) & 3;
            const int base = (((tile * 4 + q) * 4 + src) * 4 + wave) * 2048;
            pv[k][0] = __builtin_amdgcn_raw_buffer_load_b128(rp, lane * 16, base, 16);
            pv[k][1] = __builtin_amdgcn_raw_buffer_load_b128(rp, lane * 16, base + 1024, 16);
        }
#pragma unroll
        for (int k = 0; k < 3; ++k) {
            unpack8_add(pv[k][0], own[0], own[1]);
            unpack8_add(pv[k][1], own[2], own[3]);
        }
        const int trow = t0 + (wave >> 1) * 64 + q * 16 + r;
#pragma unroll
        for (int u = 0; u < 2; ++u)
            *(u32x4*)(out + (size_t)trow * 1024 + e0 + (wave & 1) * 64 + 32 * u + 8 * g) = pack8(own[2 * u], own[2 * u + 1]);
        drain_vm();
        if (wave == 0) STAMP(13);
        if (wave == 0 && bid == DBID && MULT > 0) {
            unsigned long long d = (tsb - tsa) * MULT, t = __builtin_amdgcn_s_memrealtime();
            if (d > 20000) d = 20000;
            while (__builtin_amdgcn_s_memrealtime() - t < d) __builtin_amdgcn_s_sleep(2);
        }
    }
}

extern "C" void kernel_launch(void* const* d_in, const int* in_sizes, int n_in,
                              void* d_out, int out_size, void* d_ws, size_t ws_size,
                              hipStream_t stream) {
    fused<<<256, 512, 0, stream>>>((const __bf16*)d_in[0], (const __bf16*)d_in[1], (const __bf16*)d_in[2], (const __bf16*)d_in[3],
                                   (const __bf16*)d_in[4], (const __bf16*)d_in[5], (const __bf16*)d_in[6], (const __bf16*)d_in[7],
                                   (char*)d_ws, (__bf16*)d_out);
}
```

```cpp
#define ROT3 (4*et)
#define SA 10
#define SB 13
#define MULT 2
#define DBID 9
#include <hip/hip_runtime.h>
#include <hip/hip_bf16.h>
#include <stdint.h>

#define LAS __attribute__((address_space(3)))
#define GAS __attribute__((address_space(1)))
typedef __attribute__((ext_vector_type(8))) __bf16 bf16x8;
typedef __attribute__((ext_vector_type(4))) __bf16 bf16x4;
typedef __attribute__((ext_vector_type(2))) __bf16 bf16x2;
typedef __attribute__((ext_vector_type(2))) float f32x2;
typedef __attribute__((ext_vector_type(4))) short s16x4;
typedef __attribute__((ext_vector_type(4))) float f32x4;
typedef __attribute__((ext_vector_type(4))) unsigned u32x4;
typedef __attribute__((ext_vector_type(2))) unsigned u32x2;
typedef __amdgpu_buffer_rsrc_t rsrc_t;

#define STAMP_N 16
#define STAMP_OFF (64u<<20)
#define STAMP(k) do{ if ((k)==SA) tsa=__builtin_amdgcn_s_memrealtime(); if ((k)==SB) tsb=__builtin_amdgcn_s_memrealtime(); }while(0)

constexpr int SEQ = 2048, DM = 512;
constexpr int SLOT = 32768;
constexpr int T2_SLOT = 43008;
constexpr int T2_BASE = 32768;

constexpr unsigned WS_FLAGS = 0;
constexpr unsigned WS_ABT   = 1u << 20;
constexpr unsigned WS_O     = WS_ABT + (4u << 20);
constexpr unsigned WS_PART  = WS_O + (2u << 20);
constexpr int FW = 32;
constexpr int F1_IDX = 8, F2_IDX = 72, F3_IDX = 88, NFLAGS = 88 + 256;
constexpr int NT = 0;
#define PB 1
constexpr unsigned SPIN_CAP = 1u << 17;

__device__ __forceinline__ rsrc_t mk_rsrc(const void* p, unsigned bytes) {
    return __builtin_amdgcn_make_buffer_rsrc((void*)p, 0, bytes, 0x00020000);
}
template <int AUX>
__device__ __forceinline__ void glds(rsrc_t r, LAS char* dst, int voff, int soff) {
    __builtin_amdgcn_raw_ptr_buffer_load_lds(r, (LAS void*)dst, 16, voff, soff, 0, AUX);
}
template <int N> __device__ __forceinline__ void bar_vm() {
    asm volatile("s_waitcnt vmcnt(%0)\n\ts_barrier" ::"n"(N) : "memory");
}
__device__ __forceinline__ void bar_only() { asm volatile("s_barrier" ::: "memory"); }
__device__ __forceinline__ void bar_lgkm() {
    asm volatile("s_waitcnt lgkmcnt(0)\n\ts_barrier" ::: "memory");
}
__device__ __forceinline__ void drain_vm() { asm volatile("s_waitcnt vmcnt(0)" ::: "memory"); }
__device__ __forceinline__ f32x4 mfma32(bf16x8 a, bf16x8 b, f32x4 c) {
    return __builtin_amdgcn_mfma_f32_16x16x32_bf16(a, b, c, 0, 0, 0);
}
__device__ __forceinline__ f32x4 mfma16(u32x2 a, u32x2 b, f32x4 c) {
    return __builtin_amdgcn_mfma_f32_16x16x16bf16_1k(__builtin_bit_cast(s16x4, a), __builtin_bit_cast(s16x4, b), c, 0, 0, 0);
}
__device__ __forceinline__ bf16x8 lds_b128(LAS char* p) { return *(LAS bf16x8*)p; }
__device__ __forceinline__ bf16x8 lds_tr2(LAS char* p) {
    bf16x4 t0 = __builtin_amdgcn_ds_read_tr16_b64_v4bf16((LAS bf16x4*)p);
    bf16x4 t1 = __builtin_amdgcn_ds_read_tr16_b64_v4bf16((LAS bf16x4*)(p + 512));
    return __builtin_shufflevector(t0, t1, 0, 1, 2, 3, 4, 5, 6, 7);
}
__device__ __forceinline__ unsigned cvtpk(float a, float b) {
    f32x2 v = {a, b};
    unsigned u = __builtin_bit_cast(unsigned, __builtin_convertvector(v, bf16x2));
    asm("" : "+v"(u));
    return u;
}
__device__ __forceinline__ u32x2 cvt4u(f32x4 v) { u32x2 r = {cvtpk(v[0], v[1]), cvtpk(v[2], v[3])}; return r; }
__device__ __forceinline__ u32x4 pack8(f32x4 lo, f32x4 hi) {
    u32x4 r = {cvtpk(lo[0], lo[1]), cvtpk(lo[2], lo[3]), cvtpk(hi[0], hi[1]), cvtpk(hi[2], hi[3])};
    return r;
}
__device__ __forceinline__ void unpack8_add(u32x4 v, f32x4& lo, f32x4& hi) {
    unsigned a = v[0], b = v[1], c = v[2], d = v[3];
    lo[0] += __uint_as_float(a << 16); lo[1] += __uint_as_float(a & 0xffff0000u);
    lo[2] += __uint_as_float(b << 16); lo[3] += __uint_as_float(b & 0xffff0000u);
    hi[0] += __uint_as_float(c << 16); hi[1] += __uint_as_float(c & 0xffff0000u);
    hi[2] += __uint_as_float(d << 16); hi[3] += __uint_as_float(d & 0xffff0000u);
}
__device__ __forceinline__ float fmx(float a, float b) { return __builtin_elementwise_maximum(a, b); }
__device__ __forceinline__ void swap16(float& a, float& b) {
    auto r = __builtin_amdgcn_permlane16_swap(__float_as_uint(a), __float_as_uint(b), false, false);
    unsigned x = r[0], y = r[1]; a = __uint_as_float(x); b = __uint_as_float(y);
}
__device__ __forceinline__ void swap32(float& a, float& b) {
    auto r = __builtin_amdgcn_permlane32_swap(__float_as_uint(a), __float_as_uint(b), false, false);
    unsigned x = r[0], y = r[1]; a = __uint_as_float(x); b = __uint_as_float(y);
}
template <bool MAX> __device__ __forceinline__ float op2(float a, float b) { return MAX ? fmx(a, b) : a + b; }
template <bool MAX> __device__ __forceinline__ void allred4(float* v) {
    swap16(v[0], v[1]); float c = op2<MAX>(v[0], v[1]);
    swap16(v[2], v[3]); float d = op2<MAX>(v[2], v[3]);
    swap32(c, d); float e = op2<MAX>(c, d);
    if (!MAX) e = __builtin_amdgcn_rcpf(e);
    float e2 = e; swap32(e, e2);
    float f = e; swap16(e, f);
    float h = e2; swap16(e2, h);
    v[0] = e; v[1] = f; v[2] = e2; v[3] = h;
}
__device__ __forceinline__ unsigned ld_flag(GAS unsigned* f) {
    return __builtin_amdgcn_readfirstlane(__hip_atomic_load(f, __ATOMIC_RELAXED, __HIP_MEMORY_SCOPE_AGENT));
}
__device__ __forceinline__ void add_flag(GAS unsigned* f, int lane) {
    if (lane == 0) (void)__hip_atomic_fetch_add(f, 1u, __ATOMIC_RELAXED, __HIP_MEMORY_SCOPE_AGENT);
}
#define LD_FLAG_ASYNC(REG, f) asm volatile("global_load_dword " REG ", %0, off sc1" ::"v"(f) : "memory", REG)
#define RD_FLAG(REG, out) asm volatile("v_readfirstlane_b32 %0, " REG : "=s"(out)::"memory")
#define RD_VEC(REG, out) asm volatile("v_mov_b32 %0, " REG : "=v"(out)::"memory")
__device__ __forceinline__ void add_flag_async(GAS unsigned* f, int lane) {
    unsigned one = 1u;
    if (lane == 0) asm volatile("global_atomic_add %0, %1, off" ::"v"(f), "v"(one) : "memory");
}
__device__ __forceinline__ void wait_ge(GAS unsigned* f, unsigned target, GAS unsigned* hb, int lane) {
    unsigned spins = 0;
    while (ld_flag(f) < target) {
        ++spins;
        if ((spins & 31u) == 0u) {
            if (lane < 8) (void)__hip_atomic_fetch_add(hb + lane * FW, 1u, __ATOMIC_RELAXED, __HIP_MEMORY_SCOPE_AGENT);
            if (spins > SPIN_CAP) break;
        }
        __builtin_amdgcn_s_sleep(1);
    }
    asm volatile("" ::: "memory");
}
struct Frag { bf16x8 a[4], b[4]; };
#define SGB() do { _Pragma("unroll") for (int _i = 0; _i < 8; ++_i) { __builtin_amdgcn_sched_group_barrier(0x8, 1, 0); __builtin_amdgcn_sched_group_barrier(0x100, 1, 0); } __builtin_amdgcn_sched_group_barrier(0x8, 8, 0); } while (0)
__device__ __forceinline__ void nt_read(Frag& f, LAS char* pa, LAS char* pb) {
    f.a[0] = lds_b128(pa);
#pragma unroll
    for (int n = 0; n < 4; ++n) f.b[n] = lds_b128(pb + n * 2048);
#pragma unroll
    for (int m = 1; m < 4; ++m) f.a[m] = lds_b128(pa + m * 2048);
}
__device__ __forceinline__ void nt_mma(f32x4 (&acc)[4][4], const Frag& f) {
#pragma unroll
    for (int m = 0; m < 4; ++m)
#pragma unroll
        for (int n = 0; n < 4; ++n) acc[m][n] = mfma32(f.a[m], f.b[n], acc[m][n]);
}
template <bool LASTBAR, typename Hook>
__device__ __forceinline__ int nt_consume(f32x4 (&acc)[4][4], LAS char* smem, int slot, int nst, int w, int lane, Hook hook) {
    const int r = lane & 15, g = lane >> 4;
    const int c0 = ((g ^ (r >> 1)) << 4), c1 = c0 ^ 64;
    const int oa = ((w & 1) * 64 + r) * 128, ob = 16384 + ((w >> 1) * 64 + r) * 128;
    Frag f0, f1;
    LAS char* s = smem + slot * SLOT;
    nt_read(f0, s + oa + c0, s + ob + c0);
#pragma unroll 1
    for (int it = 0; it < nst - 1; ++it) {
        __builtin_amdgcn_sched_barrier(0);
        nt_read(f1, s + oa + c1, s + ob + c1);
        nt_mma(acc, f0);
        SGB();
        __builtin_amdgcn_sched_barrier(0);
        slot = (slot + 1 == 5) ? 0 : slot + 1;
        s = smem + slot * SLOT;
        hook(it);
        bar_lgkm();
        __builtin_amdgcn_sched_barrier(0);
        nt_read(f0, s + oa + c0, s + ob + c0);
        nt_mma(acc, f1);
        SGB();
        __builtin_amdgcn_sched_barrier(0);
    }
    nt_read(f1, s + oa + c1, s + ob + c1);
    nt_mma(acc, f0);
    SGB();
    __builtin_amdgcn_sched_barrier(0);
    slot = (slot + 1 == 5) ? 0 : slot + 1;
    if (LASTBAR) bar_lgkm();
    __builtin_amdgcn_sched_barrier(0);
    nt_mma(acc, f1);
    return slot;
}

#define PIN4(a, b, c, d) asm volatile("" : "+v"(a), "+v"(b), "+v"(c), "+v"(d))
__device__ __forceinline__ void t2_pin(f32x4 (&aq)[8], f32x4 (&ak)[9], f32x4 (&av)[9]) {
    asm volatile("" : "+v"(aq[0]), "+v"(aq[1]), "+v"(aq[2]), "+v"(aq[3]), "+v"(aq[4]), "+v"(aq[5]), "+v"(aq[6]), "+v"(aq[7]),
                      "+v"(ak[0]), "+v"(ak[1]), "+v"(ak[2]), "+v"(ak[3]), "+v"(ak[4]));
    asm volatile("" : "+v"(ak[5]), "+v"(ak[6]), "+v"(ak[7]), "+v"(ak[8]),
                      "+v"(av[0]), "+v"(av[1]), "+v"(av[2]), "+v"(av[3]), "+v"(av[4]), "+v"(av[5]), "+v"(av[6]), "+v"(av[7]), "+v"(av[8]));
}
struct Frag2 { bf16x8 x[9], w[3]; };
__device__ __forceinline__ void t2_read(Frag2& f, LAS char* px, LAS char* pw) {
    f.w[1] = lds_tr2(pw + 1024);
    f.x[0] = lds_b128(px);
    f.w[2] = lds_tr2(pw + 2048);
    f.w[0] = lds_tr2(pw);
#pragma unroll
    for (int rb = 1; rb < 9; ++rb) f.x[rb] = lds_b128(px + rb * 2048);
}
__device__ __forceinline__ void t2_mma(f32x4 (&aq)[8], f32x4 (&ak)[9], f32x4 (&av)[9], const Frag2& f) {
#pragma unroll
    for (int rb = 0; rb < 9; ++rb) {
        ak[rb] = mfma32(f.w[1], f.x[rb], ak[rb]);
        av[rb] = mfma32(f.x[rb], f.w[2], av[rb]);
        if (rb > 0) aq[rb - 1] = mfma32(f.w[0], f.x[rb], aq[rb - 1]);
    }
}
#define SGB2() do { _Pragma("unroll") for (int _i = 0; _i < 15; ++_i) { __builtin_amdgcn_sched_group_barrier(0x8, 1, 0); __builtin_amdgcn_sched_group_barrier(0x100, 1, 0); } __builtin_amdgcn_sched_group_barrier(0x8, 11, 0); } while (0)

__global__ __launch_bounds__(512, 2) void fused(const __bf16* __restrict__ xa, const __bf16* __restrict__ cre, const __bf16* __restrict__ cim,
                                                const __bf16* __restrict__ hre, const __bf16* __restrict__ him, const __bf16* __restrict__ xb,
                                                const __bf16* __restrict__ wqkv, const __bf16* __restrict__ wo,
                                                char* __restrict__ ws, __bf16* __restrict__ out) {
    __shared__ __attribute__((aligned(1024))) char smem_[163840];
    LAS char* smem = (LAS char*)smem_;
    unsigned long long tsa = 0, tsb = 0;
    const int tid = threadIdx.x, lane = tid & 63;
    const int wave = __builtin_amdgcn_readfirstlane(tid >> 6);
    const int bid = blockIdx.x, xcd = bid & 7, idx = bid >> 3;
    const int q = xcd & 3, th = xcd >> 2;
    const int ttl = idx >> 2, et = idx & 3, tt = th * 8 + ttl;
    const int t0 = tt * 128, e0 = et * 128;
    const bool typeA = (ttl & 1) == 0;
    const int pg = q;
    const int s1 = typeA ? ((q & 1) * 8 + th * 4 + (ttl >> 1)) * 128 : ((xcd >> 1) * 4 + (ttl >> 1)) * 128;
    const int hg = (xcd & 1) * 4 + et;
    const int s4 = (xcd * 4 + (idx >> 3)) * 64, n4 = (idx & 7) * 64;
    GAS unsigned* flags = (GAS unsigned*)(ws + WS_FLAGS);
    const int slot3 = typeA ? 3 : 0;
#define ROT1 0
    const int rot1 = ROT1, rot3 = ROT3;

    if (wave >= 6) {
        const int lw = wave - 6;
        const int row = 8 * lw + (lane >> 3);
        const int ch = ((lane & 7) ^ (4 * lw + (lane >> 4))) * 16;
        const int cht = ((lane & 7) ^ (2 * (((lane >> 4) & 1) | (lw << 1)))) * 16;
        const bool master = (bid < 8 && lw == 0);
        GAS unsigned* myhb = flags + bid * FW;
        if (master && lane < 42) __hip_atomic_store(flags + (8 + bid * 42 + lane) * FW, 0u, __ATOMIC_RELAXED, __HIP_MEMORY_SCOPE_AGENT);
        rsrc_t rc = mk_rsrc((q >> 1) ? cim : cre, SEQ * SEQ * 2);
        const int vb = row * 4096 + ch;
        const int sb = t0 * 4096 + (q & 1) * 2048;
        auto issueC = [&](int st, int slot) {
            const int so = sb + ((st + rot3) & 15) * 128;
#pragma unroll
            for (int u = 0; u < 8; ++u) glds<NT>(rc, smem + slot * SLOT + 16384 + (lw + 2 * u) * 1024, vb, so + 16 * ((u & 4) | ((u + q) & 3)) * 4096);
        };
        int slot = 0;
        if (typeA) {
            rsrc_t rh = mk_rsrc((pg >> 1) ? him : hre, DM * DM * 2);
            const int vh = row * 1024 + ch;
#pragma unroll 1
            for (int st = 0; st < 8; ++st) {
                if (PB && (st == 1 || st == 2)) bar_only();
                if (st == 5) bar_vm<32>();
                if (st >= 5) { if (master) add_flag_async(myhb, lane); bar_vm<24>(); }
                const int so = e0 * 1024 + ((st + rot1) & 7) * 128;
#pragma unroll
                for (int u = 0; u < 8; ++u) glds<NT>(rh, smem + slot * SLOT + 16384 + (lw + 2 * u) * 1024, vh, so + 16 * u * 1024);
                slot = (slot + 1 == 5) ? 0 : slot + 1;
            }
        } else {
            rsrc_t rq = mk_rsrc(wqkv, DM * 3 * DM * 2);
            const int vq = row * 3072 + cht;
            int pos = 1;
#pragma unroll 1
            for (int st = 0; st < 8; ++st) {
                if (PB && (st == 1 || st == 2)) bar_only();
                if (st == 3) bar_vm<24>();
                if (st >= 3) { if (master && st < 6) add_flag_async(myhb, lane); bar_vm<12>(); }
                LAS char* sl = smem + T2_BASE + pos * T2_SLOT;
                const int so = ((st + rot1) & 7) * 64 * 3072 + hg * 128;
#pragma unroll
                for (int v = 0; v < 4; ++v)
#pragma unroll
                    for (int cg = 0; cg < 3; ++cg)
                        glds<NT>(rq, sl + 18432 + ((lw + 2 * v) * 3 + cg) * 1024, vq, so + 16 * v * 3072 + cg * 1024);
                pos = (pos + 1 == 3) ? 0 : pos + 1;
            }
        }
#pragma unroll 1
        for (int st = 0; st < 16; ++st) {
            if (st >= 5) {
                if (st == 5) { if (typeA) bar_only(); bar_vm<32>(); }
                bar_vm<24>();
            } else if (typeA) {
                if (st < 4) bar_vm<24>(); else bar_only();
            } else {
                if (st == 1) bar_vm<20>();
                if (st == 2) bar_vm<16>();
                if (st == 3) bar_only();
            }
            issueC(st, slot);
            slot = (slot + 1 == 5) ? 0 : slot + 1;
        }
        rsrc_t rw = mk_rsrc(wo, DM * DM * 2);
        const int vw = row * 1024 + cht;
#pragma unroll 1
        for (int st = 0; st < 4; ++st) {
            bar_vm<24>();
#pragma unroll
            for (int h = 0; h < 2; ++h)
#pragma unroll
                for (int u = 0; u < 4; ++u)
                    glds<NT>(rw, smem + slot * SLOT + h * 16384 + 8192 + (lw + 2 * u) * 1024, vw, ((st * 2 + h) * 64 + 16 * u) * 1024 + n4 * 2);
            slot = (slot + 1 == 5) ? 0 : slot + 1;
        }
        bar_vm<8>(); bar_vm<0>();
    } else if (wave >= 4) {
        const int lw = wave - 4;
        const int ch = ((lane & 7) ^ (4 * lw + (lane >> 4))) * 16;
        const int prow = 8 * (lane >> 5) + ((lane >> 3) & 3) + 16 * lw;
        const int row = 8 * lw + (lane >> 3);
        GAS unsigned* hbp = flags + (lane & 7) * FW;
        if (lw == 0) LD_FLAG_ASYNC("v254", hbp);
        if (typeA) {
            rsrc_t rx = mk_rsrc(xa, SEQ * DM * 2);
            const int va = prow * 1024 + ch;
            int sl = 0;
#pragma unroll 1
            for (int st = 0; st < 8; ++st) {
                if (PB && (st == 1 || st == 2)) bar_only();
                if (st == 5) bar_vm<32>();
                if (st >= 5) bar_vm<24>();
                if (st == 7 && lw == 0) LD_FLAG_ASYNC("v253", hbp);
                const int so = s1 * 1024 + ((st + rot1) & 7) * 128;
#pragma unroll
                for (int u = 0; u < 8; ++u) glds<NT>(rx, smem + sl * SLOT + (lw + 2 * u) * 1024, va, so + (32 * (u >> 1) + 4 * (u & 1)) * 1024);
                sl = (sl + 1 == 5) ? 0 : sl + 1;
            }
            bar_vm<24>(); bar_vm<16>(); bar_vm<8>();
        } else {
            rsrc_t rx = mk_rsrc(xb, SEQ * DM * 2);
            const int vx = row * 1024 + ch;
            const int sxm = s1 * 1024, sxh = (s1 >= 16 ? s1 - 16 : 0) * 1024;
            int pos = 1;
#pragma unroll 1
            for (int st = 0; st < 8; ++st) {
                if (PB && (st == 1 || st == 2)) bar_only();
                if (st == 3) bar_vm<18>();
                if (st >= 3) bar_vm<9>();
                if (st == 7 && lw == 0) LD_FLAG_ASYNC("v253", hbp);
                LAS char* sl = smem + T2_BASE + pos * T2_SLOT;
                const int ko = ((st + rot1) & 7) * 128;
                glds<NT>(rx, sl + lw * 1024, vx, sxh + ko);
#pragma unroll
                for (int u = 0; u < 8; ++u) glds<NT>(rx, sl + 2048 + (lw + 2 * u) * 1024, vx, sxm + 16 * u * 1024 + ko);
                pos = (pos + 1 == 3) ? 0 : pos + 1;
            }
            bar_vm<9>();
        }
        bar_vm<0>();
        if (lw == 0) {
            unsigned a, b, spins = 0;
            RD_VEC("v254", a); RD_VEC("v253", b);
            bool moved = (a != b) || ((lane & 7) == bid);
            while (!__all(moved)) {
                if (bid < 8) add_flag(flags + bid * FW, lane);
                __builtin_amdgcn_s_sleep(1);
                b = __hip_atomic_load(hbp, __ATOMIC_RELAXED, __HIP_MEMORY_SCOPE_AGENT);
                moved = moved || (a != b);
                if (++spins > SPIN_CAP) break;
            }
        }
        bar_only();
        rsrc_t ra = mk_rsrc(ws + WS_ABT, 1024 * SEQ * 2), ro = mk_rsrc(ws + WS_O, SEQ * DM * 2);
        const int va = prow * 64 + (ch & 48) + (ch >> 6) * 8192;
        const int sa = (((q >> 1) * 4 + et) * 64 + (q & 1) * 32) * 8192;
        const int vo = row * 32 + (ch & 16) + (ch >> 5) * 65536;
        if (typeA) {
            bar_only();
            if (lw == 0 && lane < 4) (void)__hip_atomic_fetch_add(flags + (F1_IDX + (pg * 4 + et) * 4 + lane) * FW, 1u, __ATOMIC_RELAXED, __HIP_MEMORY_SCOPE_AGENT);
        }
        wait_ge(flags + (F1_IDX + (q * 4 + et) * 4 + lw + 2 * th) * FW, 8u, flags, lane);
        if (wave == 4) STAMP(6);
        int slot = slot3;
        GAS unsigned* f2 = flags + (F2_IDX + (s4 >> 7)) * FW;
#pragma unroll 1
        for (int st = 0; st < 16; ++st) {
            if (st == 5) bar_vm<32>();
            if (st >= 5) bar_vm<24>();
            const int so = sa + ((st + rot3) & 15) * 16384;
#pragma unroll
            for (int u = 0; u < 8; ++u) glds<16>(ra, smem + slot * SLOT + (lw + 2 * u) * 1024, va, so + (32 * (u >> 1) + 4 * (u & 1)) * 64);
            slot = (slot + 1 == 5) ? 0 : slot + 1;
            if (st == 12) LD_FLAG_ASYNC("v252", f2);
        }
#pragma unroll 1
        for (int st = 0; st < 4; ++st) {
            bar_vm<24>();
            if (st == 0) {
                unsigned f2v;
                RD_FLAG("v252", f2v);
                if (f2v < 32u) wait_ge(f2, 32u, flags, lane);
            }
#pragma unroll
            for (int h = 0; h < 2; ++h)
#pragma unroll
                for (int u = 0; u < 4; ++u)
                    glds<16>(ro, smem + slot * SLOT + h * 16384 + (lw + 2 * u) * 1024, vo, (s4 + 16 * u) * 32 + (st * 2 + h) * 4 * 65536);
            slot = (slot + 1 == 5) ? 0 : slot + 1;
        }
        bar_vm<8>(); bar_vm<0>();
    } else {
        const int r = lane & 15, g = lane >> 4;
        if (wave == 0) STAMP(0);
        if (PB) { bar_only(); bar_only(); }
        if (typeA) {
            f32x4 acc[4][4] = {};
            bar_lgkm();
            if (wave == 0) STAMP(1);
            nt_consume<true>(acc, smem, 0, 8, wave, lane, [](int) {});
            if (wave == 0) STAMP(2);
            const unsigned sg = (pg >> 1) ? 0x80008000u : 0u;
            rsrc_t rs = mk_rsrc(ws + WS_ABT, 1024 * SEQ * 2);
            const int vs = r * 64 + g * 16;
            const int ss = (((pg >> 1) * 4 + et) * 64 + (s1 >> 5) + (wave & 1) * 2) * 8192 + (wave >> 1) * 4096;
#pragma unroll
            for (int n = 0; n < 4; ++n)
#pragma unroll
                for (int u = 0; u < 2; ++u)
                    __builtin_amdgcn_raw_buffer_store_b128(pack8(acc[2 * u][n], acc[2 * u + 1][n]) ^ sg, rs, vs + ss + n * 1024 + u * 8192, 0, 16);
            if (wave == 0) STAMP(7);
            bar_vm<0>();
            if (wave == 0) STAMP(3);
        } else {
            const int c0 = ((g ^ (r >> 1)) << 4);
            const int ox = r * 128;
            const int qq = (lane & 15) >> 2, p = lane & 3;
            const int ow = 18432 + g * 3072 + qq * 128 + (((2 * wave + (p >> 1)) ^ (2 * ((qq >> 1) | ((g & 1) << 1)))) << 4) + 8 * (p & 1);
            f32x4 aq[8] = {}, ak[9] = {}, av[9] = {};
            Frag2 f0, f1;
            int pos = 1;
            LAS char* sl = smem + T2_BASE + pos * T2_SLOT;
            bar_lgkm();
            if (wave == 0) STAMP(1);
            t2_read(f0, sl + ox + c0, sl + ow);
#pragma unroll 1
            for (int it = 0; it < 7; ++it) {
                __builtin_amdgcn_sched_barrier(0);
                t2_read(f1, sl + ox + (c0 ^ 64), sl + ow + 12288);
                t2_mma(aq, ak, av, f0);
                SGB2();
                __builtin_amdgcn_sched_barrier(0);
                t2_pin(aq, ak, av);
                pos = (pos + 1 == 3) ? 0 : pos + 1;
                sl = smem + T2_BASE + pos * T2_SLOT;
                bar_lgkm();
                __builtin_amdgcn_sched_barrier(0);
                t2_read(f0, sl + ox + c0, sl + ow);
                t2_mma(aq, ak, av, f1);
                SGB2();
                __builtin_amdgcn_sched_barrier(0);
            }
            t2_read(f1, sl + ox + (c0 ^ 64), sl + ow + 12288);
            t2_mma(aq, ak, av, f0);
            SGB2();
            __builtin_amdgcn_sched_barrier(0);
            t2_pin(aq, ak, av);
            bar_lgkm();
            __builtin_amdgcn_sched_barrier(0);
            t2_mma(aq, ak, av, f1);
            if (wave == 0) STAMP(4);
            const int j = r, par = j & 1;
            const bool uc0 = (4 * g + par) <= j, uc1 = (4 * g + par + 2) <= j;
            const unsigned s0v = par ? 0x01000c0cu : 0x0c0c0100u, s1v = par ? 0x03020c0cu : 0x0c0c0302u, zs = 0x0c0c0c0cu;
            const unsigned selc0 = uc0 ? s0v : zs, selp0 = uc0 ? zs : s0v, selc1 = uc1 ? s1v : zs, selp1 = uc1 ? zs : s1v;
            rsrc_t rs = mk_rsrc(ws + WS_O, SEQ * DM * 2);
            const int vs = j * 32 + g * 8;
            const int ss = (hg * 4 + wave) * 65536 + s1 * 32;
            const f32x4 z = {0.f, 0.f, 0.f, 0.f};
            const float C = 0.25f * 1.44269504088896f;
            u32x2 kb[9], vb[9];
            float l0[8], l1[8], m[8];
#pragma unroll
            for (int i = 0; i < 9; ++i) kb[i] = cvt4u(ak[i]);
#pragma unroll
            for (int b = 0; b < 8; ++b) {
                u32x2 qb = cvt4u(aq[b]);
                f32x4 sc = mfma16(kb[b + 1], qb, z), sp = mfma16(kb[b], qb, z);
                float c0v = par ? sc[1] : sc[0], c1v = par ? sc[3] : sc[2];
                float p0v = par ? sp[1] : sp[0], p1v = par ? sp[3] : sp[2];
                if (b == 0 && s1 == 0) { p0v = -4e9f; p1v = -4e9f; }
                unsigned pk = cvtpk(uc0 ? c0v : p0v, uc1 ? c1v : p1v);
                l0[b] = __uint_as_float(pk << 16); l1[b] = __uint_as_float(pk & 0xffff0000u);
                m[b] = fmx(l0[b], l1[b]);
            }
            allred4<true>(m); allred4<true>(m + 4);
#pragma unroll
            for (int i = 0; i < 9; ++i) vb[i] = cvt4u(av[i]);
#pragma unroll
            for (int b = 0; b < 8; ++b) {
                l0[b] = __builtin_amdgcn_exp2f((l0[b] - m[b]) * C); l1[b] = __builtin_amdgcn_exp2f((l1[b] - m[b]) * C);
                m[b] = l0[b] + l1[b];
            }
            allred4<false>(m); allred4<false>(m + 4);
            f32x4 ov[8];
#pragma unroll
            for (int b = 0; b < 8; ++b) {
                unsigned P = cvtpk(l0[b] * m[b], l1[b] * m[b]);
                u32x2 pc = {__builtin_amdgcn_perm(0u, P, selc0), __builtin_amdgcn_perm(0u, P, selc1)};
                u32x2 pp = {__builtin_amdgcn_perm(0u, P, selp0), __builtin_amdgcn_perm(0u, P, selp1)};
                ov[b] = mfma16(vb[b], pp, mfma16(vb[b + 1], pc, z));
            }
#pragma unroll
            for (int b = 0; b < 8; ++b) __builtin_amdgcn_raw_buffer_store_b64(cvt4u(ov[b]), rs, vs, ss + b * 512, 16);
            if (wave == 0) STAMP(5);
        }
        f32x4 acc[4][4] = {};
        bar_lgkm();
        if (wave == 0) STAMP(9);
        int slot = nt_consume<false>(acc, smem, slot3, 16, wave, lane, [&](int it) {
            if (it == 10 && wave == 0) STAMP(15);
            if (it == 2 && !typeA) {
                drain_vm();
                add_flag_async(flags + (F2_IDX + (s1 >> 7)) * FW, lane);
            }
        });
        if (wave == 0) STAMP(10);
        const int tile = tt * 4 + et;
        rsrc_t rp = mk_rsrc(ws + WS_PART, 64 * 16 * 4 * 2048);
#pragma unroll
        for (int n = 1; n < 4; ++n) {
            const int base = (((tile * 4 + ((q + n) & 3)) * 4 + q) * 4 + wave) * 2048;
            __builtin_amdgcn_raw_buffer_store_b128(pack8(acc[0][n], acc[1][n]), rp, lane * 16 + base, 0, 16);
            __builtin_amdgcn_raw_buffer_store_b128(pack8(acc[2][n], acc[3][n]), rp, lane * 16 + base + 1024, 0, 16);
        }
        f32x4 own[4];
#pragma unroll
        for (int m = 0; m < 4; ++m) own[m] = acc[m][0];
        f32x4 a4[2][2] = {};
        {
            const int c0 = ((g ^ (r >> 1)) << 4);
            const int oo = ((wave >> 1) * 32 + r) * 128;
            const int qq = (lane & 15) >> 2, p = lane & 3;
            const int swz = 2 * ((qq >> 1) | ((g & 1) << 1));
            const int ow0 = 8192 + g * 1024 + qq * 128 + ((((wave & 1) * 4 + (p >> 1)) ^ swz) << 4) + 8 * (p & 1);
            const int ow1 = ow0 ^ 32;
            bar_only();
#pragma unroll
            for (int st = 0; st < 4; ++st) {
                if (st == 3) {
                    drain_vm();
                    if (lane < 4 && lane != q)
                        (void)__hip_atomic_fetch_add(flags + (F3_IDX + tile * 4 + lane) * FW, 1u, __ATOMIC_RELAXED, __HIP_MEMORY_SCOPE_AGENT);
                    bar_only();
                }
                LAS char* sl = smem + slot * SLOT;
#pragma unroll
                for (int h = 0; h < 2; ++h)
#pragma unroll
                    for (int kk = 0; kk < 2; ++kk) {
                        bf16x8 w0 = lds_tr2(sl + h * 16384 + ow0 + kk * 4096), w1 = lds_tr2(sl + h * 16384 + ow1 + kk * 4096);
                        bf16x8 o0 = lds_b128(sl + h * 16384 + oo + (c0 ^ (kk * 64))), o1 = lds_b128(sl + h * 16384 + oo + (c0 ^ (kk * 64)) + 2048);
                        a4[0][0] = mfma32(w0, o0, a4[0][0]);
                        a4[0][1] = mfma32(w0, o1, a4[0][1]);
                        a4[1][0] = mfma32(w1, o0, a4[1][0]);
                        a4[1][1] = mfma32(w1, o1, a4[1][1]);
                    }
                slot = (slot + 1 == 5) ? 0 : slot + 1;
            }
#pragma unroll
            for (int n = 0; n < 2; ++n) {
                u32x2 lo = cvt4u(a4[0][n]), hi = cvt4u(a4[1][n]);
                auto r0 = __builtin_amdgcn_permlane16_swap(lo[0], hi[0], false, false);
                auto r1 = __builtin_amdgcn_permlane16_swap(lo[1], hi[1], false, false);
                u32x4 v = {r0[0], r1[0], r0[1], r1[1]};
                *(u32x4*)(out + (size_t)(s4 + (wave >> 1) * 32 + n * 16 + r) * 1024 + 512 + n4 + (wave & 1) * 32 + (g & 1) * 16 + (g >> 1) * 8) = v;
            }
        }
        if (wave == 0) STAMP(11);
        wait_ge(flags + (F3_IDX + tile * 4 + q) * FW, 12u, flags, lane);
        if (wave == 0) STAMP(12);
        u32x4 pv[3][2];
#pragma unroll
        for (int k = 0; k < 3; ++k) {
            const int src = (q + 1 + k) & 3;
            const int base = (((tile * 4 + q) * 4 + src) * 4 + wave) * 2048;
            pv[k][0] = __builtin_amdgcn_raw_buffer_load_b128(rp, lane * 16, base, 16);
            pv[k][1] = __builtin_amdgcn_raw_buffer_load_b128(rp, lane * 16, base + 1024, 16);
        }
#pragma unroll
        for (int k = 0; k < 3; ++k) {
            unpack8_add(pv[k][0], own[0], own[1]);
            unpack8_add(pv[k][1], own[2], own[3]);
        }
        const int trow = t0 + (wave >> 1) * 64 + q * 16 + r;
#pragma unroll
        for (int u = 0; u < 2; ++u)
            *(u32x4*)(out + (size_t)trow * 1024 + e0 + (wave & 1) * 64 + 32 * u + 8 * g) = pack8(own[2 * u], own[2 * u + 1]);
        drain_vm();
        if (wave == 0) STAMP(13);
        if (wave == 0 && bid == DBID && MULT > 0) {
            unsigned long long d = (tsb - tsa) * MULT, t = __builtin_amdgcn_s_memrealtime();
            if (d > 20000) d = 20000;
            while (__builtin_amdgcn_s_memrealtime() - t < d) __builtin_amdgcn_s_sleep(2);
        }
    }
}

extern "C" void kernel_launch(void* const* d_in, const int* in_sizes, int n_in,
                              void* d_out, int out_size, void* d_ws, size_t ws_size,
                              hipStream_t stream) {
    fused<<<256, 512, 0, stream>>>((const __bf16*)d_in[0], (const __bf16*)d_in[1], (const __bf16*)d_in[2], (const __bf16*)d_in[3],
                                   (const __bf16*)d_in[4], (const __bf16*)d_in[5], (const __bf16*)d_in[6], (const __bf16*)d_in[7],
                                   (char*)d_ws, (__bf16*)d_out);
}
```

```cpp
#define ROT3 (4*et)
#define COND (bid==9)
#include <hip/hip_runtime.h>
#include <hip/hip_bf16.h>
#include <stdint.h>

#define LAS __attribute__((address_space(3)))
#define GAS __attribute__((address_space(1)))
typedef __attribute__((ext_vector_type(8))) __bf16 bf16x8;
typedef __attribute__((ext_vector_type(4))) __bf16 bf16x4;
typedef __attribute__((ext_vector_type(2))) __bf16 bf16x2;
typedef __attribute__((ext_vector_type(2))) float f32x2;
typedef __attribute__((ext_vector_type(4))) short s16x4;
typedef __attribute__((ext_vector_type(4))) float f32x4;
typedef __attribute__((ext_vector_type(4))) unsigned u32x4;
typedef __attribute__((ext_vector_type(2))) unsigned u32x2;
typedef __amdgpu_buffer_rsrc_t rsrc_t;

#define STAMP_N 16
#define STAMP_OFF (64u<<20)
#define STAMP(k) do{}while(0)

constexpr int SEQ = 2048, DM = 512;
constexpr int SLOT = 32768;
constexpr int T2_SLOT = 43008;
constexpr int T2_BASE = 32768;

constexpr unsigned WS_FLAGS = 0;
constexpr unsigned WS_ABT   = 1u << 20;
constexpr unsigned WS_O     = WS_ABT + (4u << 20);
constexpr unsigned WS_PART  = WS_O + (2u << 20);
constexpr int FW = 32;
constexpr int F1_IDX = 8, F2_IDX = 72, F3_IDX = 88, NFLAGS = 88 + 256;
constexpr int NT = 0;
#define PB 1
constexpr unsigned SPIN_CAP = 1u << 17;

__device__ __forceinline__ rsrc_t mk_rsrc(const void* p, unsigned bytes) {
    return __builtin_amdgcn_make_buffer_rsrc((void*)p, 0, bytes, 0x00020000);
}
template <int AUX>
__device__ __forceinline__ void glds(rsrc_t r, LAS char* dst, int voff, int soff) {
    __builtin_amdgcn_raw_ptr_buffer_load_lds(r, (LAS void*)dst, 16, voff, soff, 0, AUX);
}
template <int N> __device__ __forceinline__ void bar_vm() {
    asm volatile("s_waitcnt vmcnt(%0)\n\ts_barrier" ::"n"(N) : "memory");
}
__device__ __forceinline__ void bar_only() { asm volatile("s_barrier" ::: "memory"); }
__device__ __forceinline__ void bar_lgkm() {
    asm volatile("s_waitcnt lgkmcnt(0)\n\ts_barrier" ::: "memory");
}
__device__ __forceinline__ void drain_vm() { asm volatile("s_waitcnt vmcnt(0)" ::: "memory"); }
__device__ __forceinline__ f32x4 mfma32(bf16x8 a, bf16x8 b, f32x4 c) {
    return __builtin_amdgcn_mfma_f32_16x16x32_bf16(a, b, c, 0, 0, 0);
}
__device__ __forceinline__ f32x4 mfma16(u32x2 a, u32x2 b, f32x4 c) {
    return __builtin_amdgcn_mfma_f32_16x16x16bf16_1k(__builtin_bit_cast(s16x4, a), __builtin_bit_cast(s16x4, b), c, 0, 0, 0);
}
__device__ __forceinline__ bf16x8 lds_b128(LAS char* p) { return *(LAS bf16x8*)p; }
__device__ __forceinline__ bf16x8 lds_tr2(LAS char* p) {
    bf16x4 t0 = __builtin_amdgcn_ds_read_tr16_b64_v4bf16((LAS bf16x4*)p);
    bf16x4 t1 = __builtin_amdgcn_ds_read_tr16_b64_v4bf16((LAS bf16x4*)(p + 512));
    return __builtin_shufflevector(t0, t1, 0, 1, 2, 3, 4, 5, 6, 7);
}
__device__ __forceinline__ unsigned cvtpk(float a, float b) {
    f32x2 v = {a, b};
    unsigned u = __builtin_bit_cast(unsigned, __builtin_convertvector(v, bf16x2));
    asm("" : "+v"(u));
    return u;
}
__device__ __forceinline__ u32x2 cvt4u(f32x4 v) { u32x2 r = {cvtpk(v[0], v[1]), cvtpk(v[2], v[3])}; return r; }
__device__ __forceinline__ u32x4 pack8(f32x4 lo, f32x4 hi) {
    u32x4 r = {cvtpk(lo[0], lo[1]), cvtpk(lo[2], lo[3]), cvtpk(hi[0], hi[1]), cvtpk(hi[2], hi[3])};
    return r;
}
__device__ __forceinline__ void unpack8_add(u32x4 v, f32x4& lo, f32x4& hi) {
    unsigned a = v[0], b = v[1], c = v[2], d = v[3];
    lo[0] += __uint_as_float(a << 16); lo[1] += __uint_as_float(a & 0xffff0000u);
    lo[2] += __uint_as_float(b << 16); lo[3] += __uint_as_float(b & 0xffff0000u);
    hi[0] += __uint_as_float(c << 16); hi[1] += __uint_as_float(c & 0xffff0000u);
    hi[2] += __uint_as_float(d << 16); hi[3] += __uint_as_float(d & 0xffff0000u);
}
__device__ __forceinline__ float fmx(float a, float b) { return __builtin_elementwise_maximum(a, b); }
__device__ __forceinline__ void swap16(float& a, float& b) {
    auto r = __builtin_amdgcn_permlane16_swap(__float_as_uint(a), __float_as_uint(b), false, false);
    unsigned x = r[0], y = r[1]; a = __uint_as_float(x); b = __uint_as_float(y);
}
__device__ __forceinline__ void swap32(float& a, float& b) {
    auto r = __builtin_amdgcn_permlane32_swap(__float_as_uint(a), __float_as_uint(b), false, false);
    unsigned x = r[0], y = r[1]; a = __uint_as_float(x); b = __uint_as_float(y);
}
template <bool MAX> __device__ __forceinline__ float op2(float a, float b) { return MAX ? fmx(a, b) : a + b; }
template <bool MAX> __device__ __forceinline__ void allred4(float* v) {
    swap16(v[0], v[1]); float c = op2<MAX>(v[0], v[1]);
    swap16(v[2], v[3]); float d = op2<MAX>(v[2], v[3]);
    swap32(c, d); float e = op2<MAX>(c, d);
    if (!MAX) e = __builtin_amdgcn_rcpf(e);
    float e2 = e; swap32(e, e2);
    float f = e; swap16(e, f);
    float h = e2; swap16(e2, h);
    v[0] = e; v[1] = f; v[2] = e2; v[3] = h;
}
__device__ __forceinline__ unsigned ld_flag(GAS unsigned* f) {
    return __builtin_amdgcn_readfirstlane(__hip_atomic_load(f, __ATOMIC_RELAXED, __HIP_MEMORY_SCOPE_AGENT));
}
__device__ __forceinline__ void add_flag(GAS unsigned* f, int lane) {
    if (lane == 0) (void)__hip_atomic_fetch_add(f, 1u, __ATOMIC_RELAXED, __HIP_MEMORY_SCOPE_AGENT);
}
#define LD_FLAG_ASYNC(REG, f) asm volatile("global_load_dword " REG ", %0, off sc1" ::"v"(f) : "memory", REG)
#define RD_FLAG(REG, out) asm volatile("v_readfirstlane_b32 %0, " REG : "=s"(out)::"memory")
#define RD_VEC(REG, out) asm volatile("v_mov_b32 %0, " REG : "=v"(out)::"memory")
__device__ __forceinline__ void add_flag_async(GAS unsigned* f, int lane) {
    unsigned one = 1u;
    if (lane == 0) asm volatile("global_atomic_add %0, %1, off" ::"v"(f), "v"(one) : "memory");
}
__device__ __forceinline__ void wait_ge(GAS unsigned* f, unsigned target, GAS unsigned* hb, int lane) {
    unsigned spins = 0;
    while (ld_flag(f) < target) {
        ++spins;
        if ((spins & 31u) == 0u) {
            if (lane < 8) (void)__hip_atomic_fetch_add(hb + lane * FW, 1u, __ATOMIC_RELAXED, __HIP_MEMORY_SCOPE_AGENT);
            if (spins > SPIN_CAP) break;
        }
        __builtin_amdgcn_s_sleep(1);
    }
    asm volatile("" ::: "memory");
}
struct Frag { bf16x8 a[4], b[4]; };
#define SGB() do { _Pragma("unroll") for (int _i = 0; _i < 8; ++_i) { __builtin_amdgcn_sched_group_barrier(0x8, 1, 0); __builtin_amdgcn_sched_group_barrier(0x100, 1, 0); } __builtin_amdgcn_sched_group_barrier(0x8, 8, 0); } while (0)
__device__ __forceinline__ void nt_read(Frag& f, LAS char* pa, LAS char* pb) {
    f.a[0] = lds_b128(pa);
#pragma unroll
    for (int n = 0; n < 4; ++n) f.b[n] = lds_b128(pb + n * 2048);
#pragma unroll
    for (int m = 1; m < 4; ++m) f.a[m] = lds_b128(pa + m * 2048);
}
__device__ __forceinline__ void nt_mma(f32x4 (&acc)[4][4], const Frag& f) {
#pragma unroll
    for (int m = 0; m < 4; ++m)
#pragma unroll
        for (int n = 0; n < 4; ++n) acc[m][n] = mfma32(f.a[m], f.b[n], acc[m][n]);
}
template <bool LASTBAR, typename Hook>
__device__ __forceinline__ int nt_consume(f32x4 (&acc)[4][4], LAS char* smem, int slot, int nst, int w, int lane, Hook hook) {
    const int r = lane & 15, g = lane >> 4;
    const int c0 = ((g ^ (r >> 1)) << 4), c1 = c0 ^ 64;
    const int oa = ((w & 1) * 64 + r) * 128, ob = 16384 + ((w >> 1) * 64 + r) * 128;
    Frag f0, f1;
    LAS char* s = smem + slot * SLOT;
    nt_read(f0, s + oa + c0, s + ob + c0);
#pragma unroll 1
    for (int it = 0; it < nst - 1; ++it) {
        __builtin_amdgcn_sched_barrier(0);
        nt_read(f1, s + oa + c1, s + ob + c1);
        nt_mma(acc, f0);
        SGB();
        __builtin_amdgcn_sched_barrier(0);
        slot = (slot + 1 == 5) ? 0 : slot + 1;
        s = smem + slot * SLOT;
        hook(it);
        bar_lgkm();
        __builtin_amdgcn_sched_barrier(0);
        nt_read(f0, s + oa + c0, s + ob + c0);
        nt_mma(acc, f1);
        SGB();
        __builtin_amdgcn_sched_barrier(0);
    }
    nt_read(f1, s + oa + c1, s + ob + c1);
    nt_mma(acc, f0);
    SGB();
    __builtin_amdgcn_sched_barrier(0);
    slot = (slot + 1 == 5) ? 0 : slot + 1;
    if (LASTBAR) bar_lgkm();
    __builtin_amdgcn_sched_barrier(0);
    nt_mma(acc, f1);
    return slot;
}

#define PIN4(a, b, c, d) asm volatile("" : "+v"(a), "+v"(b), "+v"(c), "+v"(d))
__device__ __forceinline__ void t2_pin(f32x4 (&aq)[8], f32x4 (&ak)[9], f32x4 (&av)[9]) {
    asm volatile("" : "+v"(aq[0]), "+v"(aq[1]), "+v"(aq[2]), "+v"(aq[3]), "+v"(aq[4]), "+v"(aq[5]), "+v"(aq[6]), "+v"(aq[7]),
                      "+v"(ak[0]), "+v"(ak[1]), "+v"(ak[2]), "+v"(ak[3]), "+v"(ak[4]));
    asm volatile("" : "+v"(ak[5]), "+v"(ak[6]), "+v"(ak[7]), "+v"(ak[8]),
                      "+v"(av[0]), "+v"(av[1]), "+v"(av[2]), "+v"(av[3]), "+v"(av[4]), "+v"(av[5]), "+v"(av[6]), "+v"(av[7]), "+v"(av[8]));
}
struct Frag2 { bf16x8 x[9], w[3]; };
__device__ __forceinline__ void t2_read(Frag2& f, LAS char* px, LAS char* pw) {
    f.w[1] = lds_tr2(pw + 1024);
    f.x[0] = lds_b128(px);
    f.w[2] = lds_tr2(pw + 2048);
    f.w[0] = lds_tr2(pw);
#pragma unroll
    for (int rb = 1; rb < 9; ++rb) f.x[rb] = lds_b128(px + rb * 2048);
}
__device__ __forceinline__ void t2_mma(f32x4 (&aq)[8], f32x4 (&ak)[9], f32x4 (&av)[9], const Frag2& f) {
#pragma unroll
    for (int rb = 0; rb < 9; ++rb) {
        ak[rb] = mfma32(f.w[1], f.x[rb], ak[rb]);
        av[rb] = mfma32(f.x[rb], f.w[2], av[rb]);
        if (rb > 0) aq[rb - 1] = mfma32(f.w[0], f.x[rb], aq[rb - 1]);
    }
}
#define SGB2() do { _Pragma("unroll") for (int _i = 0; _i < 15; ++_i) { __builtin_amdgcn_sched_group_barrier(0x8, 1, 0); __builtin_amdgcn_sched_group_barrier(0x100, 1, 0); } __builtin_amdgcn_sched_group_barrier(0x8, 11, 0); } while (0)

__global__ __launch_bounds__(512, 2) void fused(const __bf16* __restrict__ xa, const __bf16* __restrict__ cre, const __bf16* __restrict__ cim,
                                                const __bf16* __restrict__ hre, const __bf16* __restrict__ him, const __bf16* __restrict__ xb,
                                                const __bf16* __restrict__ wqkv, const __bf16* __restrict__ wo,
                                                char* __restrict__ ws, __bf16* __restrict__ out) {
    __shared__ __attribute__((aligned(1024))) char smem_[163840];
    LAS char* smem = (LAS char*)smem_;
    const int tid = threadIdx.x, lane = tid & 63;
    const int wave = __builtin_amdgcn_readfirstlane(tid >> 6);
    const int bid = blockIdx.x, xcd = bid & 7, idx = bid >> 3;
    const int q = xcd & 3, th = xcd >> 2;
    const int ttl = idx >> 2, et = idx & 3, tt = th * 8 + ttl;
    const int t0 = tt * 128, e0 = et * 128;
    const bool typeA = (ttl & 1) == 0;
    const int pg = q;
    const int s1 = typeA ? ((q & 1) * 8 + th * 4 + (ttl >> 1)) * 128 : ((xcd >> 1) * 4 + (ttl >> 1)) * 128;
    const int hg = (xcd & 1) * 4 + et;
    const int s4 = (xcd * 4 + (idx >> 3)) * 64, n4 = (idx & 7) * 64;
    GAS unsigned* flags = (GAS unsigned*)(ws + WS_FLAGS);
    const int slot3 = typeA ? 3 : 0;
#define ROT1 0
    const int rot1 = ROT1, rot3 = ROT3;

    if (wave >= 6) {
        const int lw = wave - 6;
        const int row = 8 * lw + (lane >> 3);
        const int ch = ((lane & 7) ^ (4 * lw + (lane >> 4))) * 16;
        const int cht = ((lane & 7) ^ (2 * (((lane >> 4) & 1) | (lw << 1)))) * 16;
        const bool master = (bid < 8 && lw == 0);
        GAS unsigned* myhb = flags + bid * FW;
        if (master && lane < 42) __hip_atomic_store(flags + (8 + bid * 42 + lane) * FW, 0u, __ATOMIC_RELAXED, __HIP_MEMORY_SCOPE_AGENT);
        rsrc_t rc = mk_rsrc((q >> 1) ? cim : cre, SEQ * SEQ * 2);
        const int vb = row * 4096 + ch;
        const int sb = t0 * 4096 + (q & 1) * 2048;
        auto issueC = [&](int st, int slot) {
            const int so = sb + ((st + rot3) & 15) * 128;
#pragma unroll
            for (int u = 0; u < 8; ++u) glds<NT>(rc, smem + slot * SLOT + 16384 + (lw + 2 * u) * 1024, vb, so + 16 * ((u & 4) | ((u + q) & 3)) * 4096);
        };
        int slot = 0;
        if (typeA) {
            rsrc_t rh = mk_rsrc((pg >> 1) ? him : hre, DM * DM * 2);
            const int vh = row * 1024 + ch;
#pragma unroll 1
            for (int st = 0; st < 8; ++st) {
                if (PB && (st == 1 || st == 2)) bar_only();
                if (st == 5) bar_vm<32>();
                if (st >= 5) { if (master) add_flag_async(myhb, lane); bar_vm<24>(); }
                const int so = e0 * 1024 + ((st + rot1) & 7) * 128;
#pragma unroll
                for (int u = 0; u < 8; ++u) glds<NT>(rh, smem + slot * SLOT + 16384 + (lw + 2 * u) * 1024, vh, so + 16 * u * 1024);
                slot = (slot + 1 == 5) ? 0 : slot + 1;
            }
        } else {
            rsrc_t rq = mk_rsrc(wqkv, DM * 3 * DM * 2);
            const int vq = row * 3072 + cht;
            int pos = 1;
#pragma unroll 1
            for (int st = 0; st < 8; ++st) {
                if (PB && (st == 1 || st == 2)) bar_only();
                if (st == 3) bar_vm<24>();
                if (st >= 3) { if (master && st < 6) add_flag_async(myhb, lane); bar_vm<12>(); }
                LAS char* sl = smem + T2_BASE + pos * T2_SLOT;
                const int so = ((st + rot1) & 7) * 64 * 3072 + hg * 128;
#pragma unroll
                for (int v = 0; v < 4; ++v)
#pragma unroll
                    for (int cg = 0; cg < 3; ++cg)
                        glds<NT>(rq, sl + 18432 + ((lw + 2 * v) * 3 + cg) * 1024, vq, so + 16 * v * 3072 + cg * 1024);
                pos = (pos + 1 == 3) ? 0 : pos + 1;
            }
        }
#pragma unroll 1
        for (int st = 0; st < 16; ++st) {
            if (st >= 5) {
                if (st == 5) { if (typeA) bar_only(); bar_vm<32>(); }
                bar_vm<24>();
            } else if (typeA) {
                if (st < 4) bar_vm<24>(); else bar_only();
            } else {
                if (st == 1) bar_vm<20>();
                if (st == 2) bar_vm<16>();
                if (st == 3) bar_only();
            }
            issueC(st, slot);
            slot = (slot + 1 == 5) ? 0 : slot + 1;
        }
        rsrc_t rw = mk_rsrc(wo, DM * DM * 2);
        const int vw = row * 1024 + cht;
#pragma unroll 1
        for (int st = 0; st < 4; ++st) {
            bar_vm<24>();
#pragma unroll
            for (int h = 0; h < 2; ++h)
#pragma unroll
                for (int u = 0; u < 4; ++u)
                    glds<NT>(rw, smem + slot * SLOT + h * 16384 + 8192 + (lw + 2 * u) * 1024, vw, ((st * 2 + h) * 64 + 16 * u) * 1024 + n4 * 2);
            slot = (slot + 1 == 5) ? 0 : slot + 1;
        }
        bar_vm<8>(); bar_vm<0>();
    } else if (wave >= 4) {
        const int lw = wave - 4;
        const int ch = ((lane & 7) ^ (4 * lw + (lane >> 4))) * 16;
        const int prow = 8 * (lane >> 5) + ((lane >> 3) & 3) + 16 * lw;
        const int row = 8 * lw + (lane >> 3);
        GAS unsigned* hbp = flags + (lane & 7) * FW;
        if (lw == 0) LD_FLAG_ASYNC("v254", hbp);
        if (typeA) {
            rsrc_t rx = mk_rsrc(xa, SEQ * DM * 2);
            const int va = prow * 1024 + ch;
            int sl = 0;
#pragma unroll 1
            for (int st = 0; st < 8; ++st) {
                if (PB && (st == 1 || st == 2)) bar_only();
                if (st == 5) bar_vm<32>();
                if (st >= 5) bar_vm<24>();
                if (st == 7 && lw == 0) LD_FLAG_ASYNC("v253", hbp);
                const int so = s1 * 1024 + ((st + rot1) & 7) * 128;
#pragma unroll
                for (int u = 0; u < 8; ++u) glds<NT>(rx, smem + sl * SLOT + (lw + 2 * u) * 1024, va, so + (32 * (u >> 1) + 4 * (u & 1)) * 1024);
                sl = (sl + 1 == 5) ? 0 : sl + 1;
            }
            bar_vm<24>(); bar_vm<16>(); bar_vm<8>();
        } else {
            rsrc_t rx = mk_rsrc(xb, SEQ * DM * 2);
            const int vx = row * 1024 + ch;
            const int sxm = s1 * 1024, sxh = (s1 >= 16 ? s1 - 16 : 0) * 1024;
            int pos = 1;
#pragma unroll 1
            for (int st = 0; st < 8; ++st) {
                if (PB && (st == 1 || st == 2)) bar_only();
                if (st == 3) bar_vm<18>();
                if (st >= 3) bar_vm<9>();
                if (st == 7 && lw == 0) LD_FLAG_ASYNC("v253", hbp);
                LAS char* sl = smem + T2_BASE + pos * T2_SLOT;
                const int ko = ((st + rot1) & 7) * 128;
                glds<NT>(rx, sl + lw * 1024, vx, sxh + ko);
#pragma unroll
                for (int u = 0; u < 8; ++u) glds<NT>(rx, sl + 2048 + (lw + 2 * u) * 1024, vx, sxm + 16 * u * 1024 + ko);
                pos = (pos + 1 == 3) ? 0 : pos + 1;
            }
            bar_vm<9>();
        }
        bar_vm<0>();
        if (lw == 0) {
            unsigned a, b, spins = 0;
            RD_VEC("v254", a); RD_VEC("v253", b);
            bool moved = (a != b) || ((lane & 7) == bid);
            while (!__all(moved)) {
                if (bid < 8) add_flag(flags + bid * FW, lane);
                __builtin_amdgcn_s_sleep(1);
                b = __hip_atomic_load(hbp, __ATOMIC_RELAXED, __HIP_MEMORY_SCOPE_AGENT);
                moved = moved || (a != b);
                if (++spins > SPIN_CAP) break;
            }
        }
        bar_only();
        rsrc_t ra = mk_rsrc(ws + WS_ABT, 1024 * SEQ * 2), ro = mk_rsrc(ws + WS_O, SEQ * DM * 2);
        const int va = prow * 64 + (ch & 48) + (ch >> 6) * 8192;
        const int sa = (((q >> 1) * 4 + et) * 64 + (q & 1) * 32) * 8192;
        const int vo = row * 32 + (ch & 16) + (ch >> 5) * 65536;
        if (typeA) {
            bar_only();
            if (lw == 0 && lane < 4) (void)__hip_atomic_fetch_add(flags + (F1_IDX + (pg * 4 + et) * 4 + lane) * FW, 1u, __ATOMIC_RELAXED, __HIP_MEMORY_SCOPE_AGENT);
        }
        wait_ge(flags + (F1_IDX + (q * 4 + et) * 4 + lw + 2 * th) * FW, 8u, flags, lane);
        if (wave == 4) STAMP(6);
        int slot = slot3;
        GAS unsigned* f2 = flags + (F2_IDX + (s4 >> 7)) * FW;
#pragma unroll 1
        for (int st = 0; st < 16; ++st) {
            if (st == 5) bar_vm<32>();
            if (st >= 5) bar_vm<24>();
            const int so = sa + ((st + rot3) & 15) * 16384;
#pragma unroll
            for (int u = 0; u < 8; ++u) glds<16>(ra, smem + slot * SLOT + (lw + 2 * u) * 1024, va, so + (32 * (u >> 1) + 4 * (u & 1)) * 64);
            slot = (slot + 1 == 5) ? 0 : slot + 1;
            if (st == 12) LD_FLAG_ASYNC("v252", f2);
        }
#pragma unroll 1
        for (int st = 0; st < 4; ++st) {
            bar_vm<24>();
            if (st == 0) {
                unsigned f2v;
                RD_FLAG("v252", f2v);
                if (f2v < 32u) wait_ge(f2, 32u, flags, lane);
            }
#pragma unroll
            for (int h = 0; h < 2; ++h)
#pragma unroll
                for (int u = 0; u < 4; ++u)
                    glds<16>(ro, smem + slot * SLOT + h * 16384 + (lw + 2 * u) * 1024, vo, (s4 + 16 * u) * 32 + (st * 2 + h) * 4 * 65536);
            slot = (slot + 1 == 5) ? 0 : slot + 1;
        }
        bar_vm<8>(); bar_vm<0>();
    } else {
        const int r = lane & 15, g = lane >> 4;
        if (wave == 0) STAMP(0);
        if (PB) { bar_only(); bar_only(); }
        if (typeA) {
            f32x4 acc[4][4] = {};
            bar_lgkm();
            if (wave == 0) STAMP(1);
            nt_consume<true>(acc, smem, 0, 8, wave, lane, [](int) {});
            if (wave == 0) STAMP(2);
            const unsigned sg = (pg >> 1) ? 0x80008000u : 0u;
            rsrc_t rs = mk_rsrc(ws + WS_ABT, 1024 * SEQ * 2);
            const int vs = r * 64 + g * 16;
            const int ss = (((pg >> 1) * 4 + et) * 64 + (s1 >> 5) + (wave & 1) * 2) * 8192 + (wave >> 1) * 4096;
#pragma unroll
            for (int n = 0; n < 4; ++n)
#pragma unroll
                for (int u = 0; u < 2; ++u)
                    __builtin_amdgcn_raw_buffer_store_b128(pack8(acc[2 * u][n], acc[2 * u + 1][n]) ^ sg, rs, vs + ss + n * 1024 + u * 8192, 0, 16);
            if (wave == 0) STAMP(7);
            bar_vm<0>();
            if (wave == 0) STAMP(3);
        } else {
            const int c0 = ((g ^ (r >> 1)) << 4);
            const int ox = r * 128;
            const int qq = (lane & 15) >> 2, p = lane & 3;
            const int ow = 18432 + g * 3072 + qq * 128 + (((2 * wave + (p >> 1)) ^ (2 * ((qq >> 1) | ((g & 1) << 1)))) << 4) + 8 * (p & 1);
            f32x4 aq[8] = {}, ak[9] = {}, av[9] = {};
            Frag2 f0, f1;
            int pos = 1;
            LAS char* sl = smem + T2_BASE + pos * T2_SLOT;
            bar_lgkm();
            if (wave == 0) STAMP(1);
            t2_read(f0, sl + ox + c0, sl + ow);
#pragma unroll 1
            for (int it = 0; it < 7; ++it) {
                __builtin_amdgcn_sched_barrier(0);
                t2_read(f1, sl + ox + (c0 ^ 64), sl + ow + 12288);
                t2_mma(aq, ak, av, f0);
                SGB2();
                __builtin_amdgcn_sched_barrier(0);
                t2_pin(aq, ak, av);
                pos = (pos + 1 == 3) ? 0 : pos + 1;
                sl = smem + T2_BASE + pos * T2_SLOT;
                bar_lgkm();
                __builtin_amdgcn_sched_barrier(0);
                t2_read(f0, sl + ox + c0, sl + ow);
                t2_mma(aq, ak, av, f1);
                SGB2();
                __builtin_amdgcn_sched_barrier(0);
            }
            t2_read(f1, sl + ox + (c0 ^ 64), sl + ow + 12288);
            t2_mma(aq, ak, av, f0);
            SGB2();
            __builtin_amdgcn_sched_barrier(0);
            t2_pin(aq, ak, av);
            bar_lgkm();
            __builtin_amdgcn_sched_barrier(0);
            t2_mma(aq, ak, av, f1);
            if (wave == 0) STAMP(4);
            const int j = r, par = j & 1;
            const bool uc0 = (4 * g + par) <= j, uc1 = (4 * g + par + 2) <= j;
            const unsigned s0v = par ? 0x01000c0cu : 0x0c0c0100u, s1v = par ? 0x03020c0cu : 0x0c0c0302u, zs = 0x0c0c0c0cu;
            const unsigned selc0 = uc0 ? s0v : zs, selp0 = uc0 ? zs : s0v, selc1 = uc1 ? s1v : zs, selp1 = uc1 ? zs : s1v;
            rsrc_t rs = mk_rsrc(ws + WS_O, SEQ * DM * 2);
            const int vs = j * 32 + g * 8;
            const int ss = (hg * 4 + wave) * 65536 + s1 * 32;
            const f32x4 z = {0.f, 0.f, 0.f, 0.f};
            const float C = 0.25f * 1.44269504088896f;
            u32x2 kb[9], vb[9];
            float l0[8], l1[8], m[8];
#pragma unroll
            for (int i = 0; i < 9; ++i) kb[i] = cvt4u(ak[i]);
#pragma unroll
            for (int b = 0; b < 8; ++b) {
                u32x2 qb = cvt4u(aq[b]);
                f32x4 sc = mfma16(kb[b + 1], qb, z), sp = mfma16(kb[b], qb, z);
                float c0v = par ? sc[1] : sc[0], c1v = par ? sc[3] : sc[2];
                float p0v = par ? sp[1] : sp[0], p1v = par ? sp[3] : sp[2];
                if (b == 0 && s1 == 0) { p0v = -4e9f; p1v = -4e9f; }
                unsigned pk = cvtpk(uc0 ? c0v : p0v, uc1 ? c1v : p1v);
                l0[b] = __uint_as_float(pk << 16); l1[b] = __uint_as_float(pk & 0xffff0000u);
                m[b] = fmx(l0[b], l1[b]);
            }
            allred4<true>(m); allred4<true>(m + 4);
#pragma unroll
            for (int i = 0; i < 9; ++i) vb[i] = cvt4u(av[i]);
#pragma unroll
            for (int b = 0; b < 8; ++b) {
                l0[b] = __builtin_amdgcn_exp2f((l0[b] - m[b]) * C); l1[b] = __builtin_amdgcn_exp2f((l1[b] - m[b]) * C);
                m[b] = l0[b] + l1[b];
            }
            allred4<false>(m); allred4<false>(m + 4);
            f32x4 ov[8];
#pragma unroll
            for (int b = 0; b < 8; ++b) {
                unsigned P = cvtpk(l0[b] * m[b], l1[b] * m[b]);
                u32x2 pc = {__builtin_amdgcn_perm(0u, P, selc0), __builtin_amdgcn_perm(0u, P, selc1)};
                u32x2 pp = {__builtin_amdgcn_perm(0u, P, selp0), __builtin_amdgcn_perm(0u, P, selp1)};
                ov[b] = mfma16(vb[b], pp, mfma16(vb[b + 1], pc, z));
            }
#pragma unroll
            for (int b = 0; b < 8; ++b) __builtin_amdgcn_raw_buffer_store_b64(cvt4u(ov[b]), rs, vs, ss + b * 512, 16);
            if (wave == 0) STAMP(5);
        }
        f32x4 acc[4][4] = {};
        bar_lgkm();
        if (wave == 0) STAMP(9);
        int slot = nt_consume<false>(acc, smem, slot3, 16, wave, lane, [&](int it) {
            if (it == 2 && !typeA) {
                drain_vm();
                add_flag_async(flags + (F2_IDX + (s1 >> 7)) * FW, lane);
            }
        });
        if (wave == 0) STAMP(10);
        const int tile = tt * 4 + et;
        rsrc_t rp = mk_rsrc(ws + WS_PART, 64 * 16 * 4 * 2048);
#pragma unroll
        for (int n = 1; n < 4; ++n) {
            const int base = (((tile * 4 + ((q + n) & 3)) * 4 + q) * 4 + wave) * 2048;
            __builtin_amdgcn_raw_buffer_store_b128(pack8(acc[0][n], acc[1][n]), rp, lane * 16 + base, 0, 16);
            __builtin_amdgcn_raw_buffer_store_b128(pack8(acc[2][n], acc[3][n]), rp, lane * 16 + base + 1024, 0, 16);
        }
        f32x4 own[4];
#pragma unroll
        for (int m = 0; m < 4; ++m) own[m] = acc[m][0];
        f32x4 a4[2][2] = {};
        {
            const int c0 = ((g ^ (r >> 1)) << 4);
            const int oo = ((wave >> 1) * 32 + r) * 128;
            const int qq = (lane & 15) >> 2, p = lane & 3;
            const int swz = 2 * ((qq >> 1) | ((g & 1) << 1));
            const int ow0 = 8192 + g * 1024 + qq * 128 + ((((wave & 1) * 4 + (p >> 1)) ^ swz) << 4) + 8 * (p & 1);
            const int ow1 = ow0 ^ 32;
            bar_only();
#pragma unroll
            for (int st = 0; st < 4; ++st) {
                if (st == 3) {
                    drain_vm();
                    if (lane < 4 && lane != q)
                        (void)__hip_atomic_fetch_add(flags + (F3_IDX + tile * 4 + lane) * FW, 1u, __ATOMIC_RELAXED, __HIP_MEMORY_SCOPE_AGENT);
                    bar_only();
                }
                LAS char* sl = smem + slot * SLOT;
#pragma unroll
                for (int h = 0; h < 2; ++h)
#pragma unroll
                    for (int kk = 0; kk < 2; ++kk) {
                        bf16x8 w0 = lds_tr2(sl + h * 16384 + ow0 + kk * 4096), w1 = lds_tr2(sl + h * 16384 + ow1 + kk * 4096);
                        bf16x8 o0 = lds_b128(sl + h * 16384 + oo + (c0 ^ (kk * 64))), o1 = lds_b128(sl + h * 16384 + oo + (c0 ^ (kk * 64)) + 2048);
                        a4[0][0] = mfma32(w0, o0, a4[0][0]);
                        a4[0][1] = mfma32(w0, o1, a4[0][1]);
                        a4[1][0] = mfma32(w1, o0, a4[1][0]);
                        a4[1][1] = mfma32(w1, o1, a4[1][1]);
                    }
                slot = (slot + 1 == 5) ? 0 : slot + 1;
            }
#pragma unroll
            for (int n = 0; n < 2; ++n) {
                u32x2 lo = cvt4u(a4[0][n]), hi = cvt4u(a4[1][n]);
                auto r0 = __builtin_amdgcn_permlane16_swap(lo[0], hi[0], false, false);
                auto r1 = __builtin_amdgcn_permlane16_swap(lo[1], hi[1], false, false);
                u32x4 v = {r0[0], r1[0], r0[1], r1[1]};
                *(u32x4*)(out + (size_t)(s4 + (wave >> 1) * 32 + n * 16 + r) * 1024 + 512 + n4 + (wave & 1) * 32 + (g & 1) * 16 + (g >> 1) * 8) = v;
            }
        }
        if (wave == 0) STAMP(11);
        wait_ge(flags + (F3_IDX + tile * 4 + q) * FW, 12u, flags, lane);
        if (wave == 0) STAMP(12);
        u32x4 pv[3][2];
#pragma unroll
        for (int k = 0; k < 3; ++k) {
            const int src = (q + 1 + k) & 3;
            const int base = (((tile * 4 + q) * 4 + src) * 4 + wave) * 2048;
            pv[k][0] = __builtin_amdgcn_raw_buffer_load_b128(rp, lane * 16, base, 16);
            pv[k][1] = __builtin_amdgcn_raw_buffer_load_b128(rp, lane * 16, base + 1024, 16);
        }
#pragma unroll
        for (int k = 0; k < 3; ++k) {
            unpack8_add(pv[k][0], own[0], own[1]);
            unpack8_add(pv[k][1], own[2], own[3]);
        }
        const int trow = t0 + (wave >> 1) * 64 + q * 16 + r;
#pragma unroll
        for (int u = 0; u < 2; ++u)
            *(u32x4*)(out + (size_t)trow * 1024 + e0 + (wave & 1) * 64 + 32 * u + 8 * g) = pack8(own[2 * u], own[2 * u + 1]);
        if (COND) {
            drain_vm();
            unsigned long long t = __builtin_amdgcn_s_memrealtime();
            while (__builtin_amdgcn_s_memrealtime() - t < 1000) __builtin_amdgcn_s_sleep(2);
        }
    }
}

extern "C" void kernel_launch(void* const* d_in, const int* in_sizes, int n_in,
                              void* d_out, int out_size, void* d_ws, size_t ws_size,
                              hipStream_t stream) {
    fused<<<256, 512, 0, stream>>>((const __bf16*)d_in[0], (const __bf16*)d_in[1], (const __bf16*)d_in[2], (const __bf16*)d_in[3],
                                   (const __bf16*)d_in[4], (const __bf16*)d_in[5], (const __bf16*)d_in[6], (const __bf16*)d_in[7],
                                   (char*)d_ws, (__bf16*)d_out);
}
```

```cpp
#include <hip/hip_runtime.h>
#include <hip/hip_bf16.h>
#include <stdint.h>

#define LAS __attribute__((address_space(3)))
#define GAS __attribute__((address_space(1)))
typedef __attribute__((ext_vector_type(8))) __bf16 bf16x8;
typedef __attribute__((ext_vector_type(4))) __bf16 bf16x4;
typedef __attribute__((ext_vector_type(2))) __bf16 bf16x2;
typedef __attribute__((ext_vector_type(2))) float f32x2;
typedef __attribute__((ext_vector_type(4))) short s16x4;
typedef __attribute__((ext_vector_type(4))) float f32x4;
typedef __attribute__((ext_vector_type(4))) unsigned u32x4;
typedef __attribute__((ext_vector_type(2))) unsigned u32x2;
typedef __amdgpu_buffer_rsrc_t rsrc_t;

#define NOSTAMPS 1
#define STAMP_N 16
#define STAMP_OFF (64u<<20)
#define STAMP(k) do{}while(0)

constexpr int SEQ = 2048, DM = 512;
constexpr int SLOT = 32768;
constexpr int T2_SLOT = 43008;
constexpr int T2_BASE = 32768;

constexpr unsigned WS_FLAGS = 0;
constexpr unsigned WS_ABT   = 1u << 20;
constexpr unsigned WS_O     = WS_ABT + (4u << 20);
constexpr unsigned WS_PART  = WS_O + (2u << 20);
constexpr int FW = 32;
constexpr int F1_IDX = 8, F2_IDX = 72, F3_IDX = 88, NFLAGS = 88 + 256;
constexpr int NT = 0;
constexpr unsigned SPIN_CAP = 1u << 17;

__device__ __forceinline__ rsrc_t mk_rsrc(const void* p, unsigned bytes) {
    return __builtin_amdgcn_make_buffer_rsrc((void*)p, 0, bytes, 0x00020000);
}
template <int AUX>
__device__ __forceinline__ void glds(rsrc_t r, LAS char* dst, int voff, int soff) {
    __builtin_amdgcn_raw_ptr_buffer_load_lds(r, (LAS void*)dst, 16, voff, soff, 0, AUX);
}
template <int N> __device__ __forceinline__ void bar_vm() {
    asm volatile("s_waitcnt vmcnt(%0)\n\ts_barrier" ::"n"(N) : "memory");
}
__device__ __forceinline__ void bar_only() { asm volatile("s_barrier" ::: "memory"); }
__device__ __forceinline__ void bar_lgkm() {
    asm volatile("s_waitcnt lgkmcnt(0)\n\ts_barrier" ::: "memory");
}
__device__ __forceinline__ void drain_vm() { asm volatile("s_waitcnt vmcnt(0)" ::: "memory"); }
__device__ __forceinline__ f32x4 mfma32(bf16x8 a, bf16x8 b, f32x4 c) {
    return __builtin_amdgcn_mfma_f32_16x16x32_bf16(a, b, c, 0, 0, 0);
}
__device__ __forceinline__ f32x4 mfma16(u32x2 a, u32x2 b, f32x4 c) {
    return __builtin_amdgcn_mfma_f32_16x16x16bf16_1k(__builtin_bit_cast(s16x4, a), __builtin_bit_cast(s16x4, b), c, 0, 0, 0);
}
__device__ __forceinline__ bf16x8 lds_b128(LAS char* p) { return *(LAS bf16x8*)p; }
__device__ __forceinline__ bf16x8 lds_tr2(LAS char* p) {
    bf16x4 t0 = __builtin_amdgcn_ds_read_tr16_b64_v4bf16((LAS bf16x4*)p);
    bf16x4 t1 = __builtin_amdgcn_ds_read_tr16_b64_v4bf16((LAS bf16x4*)(p + 512));
    return __builtin_shufflevector(t0, t1, 0, 1, 2, 3, 4, 5, 6, 7);
}
__device__ __forceinline__ unsigned cvtpk(float a, float b) {
    f32x2 v = {a, b};
    unsigned u = __builtin_bit_cast(unsigned, __builtin_convertvector(v, bf16x2));
    asm("" : "+v"(u));
    return u;
}
__device__ __forceinline__ u32x2 cvt4u(f32x4 v) { u32x2 r = {cvtpk(v[0], v[1]), cvtpk(v[2], v[3])}; return r; }
__device__ __forceinline__ u32x4 pack8(f32x4 lo, f32x4 hi) {
    u32x4 r = {cvtpk(lo[0], lo[1]), cvtpk(lo[2], lo[3]), cvtpk(hi[0], hi[1]), cvtpk(hi[2], hi[3])};
    return r;
}
__device__ __forceinline__ void unpack8_add(u32x4 v, f32x4& lo, f32x4& hi) {
    unsigned a = v[0], b = v[1], c = v[2], d = v[3];
    lo[0] += __uint_as_float(a << 16); lo[1] += __uint_as_float(a & 0xffff0000u);
    lo[2] += __uint_as_float(b << 16); lo[3] += __uint_as_float(b & 0xffff0000u);
    hi[0] += __uint_as_float(c << 16); hi[1] += __uint_as_float(c & 0xffff0000u);
    hi[2] += __uint_as_float(d << 16); hi[3] += __uint_as_float(d & 0xffff0000u);
}
__device__ __forceinline__ float fmx(float a, float b) { return __builtin_elementwise_maximum(a, b); }
__device__ __forceinline__ void swap16(float& a, float& b) {
    auto r = __builtin_amdgcn_permlane16_swap(__float_as_uint(a), __float_as_uint(b), false, false);
    unsigned x = r[0], y = r[1]; a = __uint_as_float(x); b = __uint_as_float(y);
}
__device__ __forceinline__ void swap32(float& a, float& b) {
    auto r = __builtin_amdgcn_permlane32_swap(__float_as_uint(a), __float_as_uint(b), false, false);
    unsigned x = r[0], y = r[1]; a = __uint_as_float(x); b = __uint_as_float(y);
}
template <bool MAX> __device__ __forceinline__ float op2(float a, float b) { return MAX ? fmx(a, b) : a + b; }
template <bool MAX> __device__ __forceinline__ void allred4(float* v) {
    swap16(v[0], v[1]); float c = op2<MAX>(v[0], v[1]);
    swap16(v[2], v[3]); float d = op2<MAX>(v[2], v[3]);
    swap32(c, d); float e = op2<MAX>(c, d);
    if (!MAX) e = __builtin_amdgcn_rcpf(e);
    float e2 = e; swap32(e, e2);
    float f = e; swap16(e, f);
    float h = e2; swap16(e2, h);
    v[0] = e; v[1] = f; v[2] = e2; v[3] = h;
}
__device__ __forceinline__ unsigned ld_flag(GAS unsigned* f) {
    return __builtin_amdgcn_readfirstlane(__hip_atomic_load(f, __ATOMIC_RELAXED, __HIP_MEMORY_SCOPE_AGENT));
}
__device__ __forceinline__ void add_flag(GAS unsigned* f, int lane) {
    if (lane == 0) (void)__hip_atomic_fetch_add(f, 1u, __ATOMIC_RELAXED, __HIP_MEMORY_SCOPE_AGENT);
}
#define LD_FLAG_ASYNC(REG, f) asm volatile("global_load_dword " REG ", %0, off sc1" ::"v"(f) : "memory", REG)
#define RD_FLAG(REG, out) asm volatile("v_readfirstlane_b32 %0, " REG : "=s"(out)::"memory")
#define RD_VEC(REG, out) asm volatile("v_mov_b32 %0, " REG : "=v"(out)::"memory")
__device__ __forceinline__ void add_flag_async(GAS unsigned* f, int lane) {
    unsigned one = 1u;
    if (lane == 0) asm volatile("global_atomic_add %0, %1, off" ::"v"(f), "v"(one) : "memory");
}
__device__ __forceinline__ void wait_ge(GAS unsigned* f, unsigned target, GAS unsigned* hb, int lane) {
    unsigned spins = 0;
    while (ld_flag(f) < target) {
        ++spins;
        if ((spins & 31u) == 0u) {
            if (lane < 8) (void)__hip_atomic_fetch_add(hb + lane * FW, 1u, __ATOMIC_RELAXED, __HIP_MEMORY_SCOPE_AGENT);
            if (spins > SPIN_CAP) break;
        }
        __builtin_amdgcn_s_sleep(1);
    }
    asm volatile("" ::: "memory");
}
struct Frag { bf16x8 a[4], b[4]; };
#define SGB() do { _Pragma("unroll") for (int _i = 0; _i < 8; ++_i) { __builtin_amdgcn_sched_group_barrier(0x8, 1, 0); __builtin_amdgcn_sched_group_barrier(0x100, 1, 0); } __builtin_amdgcn_sched_group_barrier(0x8, 8, 0); } while (0)
__device__ __forceinline__ void nt_read(Frag& f, LAS char* pa, LAS char* pb) {
    f.a[0] = lds_b128(pa);
#pragma unroll
    for (int n = 0; n < 4; ++n) f.b[n] = lds_b128(pb + n * 2048);
#pragma unroll
    for (int m = 1; m < 4; ++m) f.a[m] = lds_b128(pa + m * 2048);
}
__device__ __forceinline__ void nt_mma(f32x4 (&acc)[4][4], const Frag& f) {
#pragma unroll
    for (int m = 0; m < 4; ++m)
#pragma unroll
        for (int n = 0; n < 4; ++n) acc[m][n] = mfma32(f.a[m], f.b[n], acc[m][n]);
}
template <bool LASTBAR, typename Hook>
__device__ __forceinline__ int nt_consume(f32x4 (&acc)[4][4], LAS char* smem, int slot, int nst, int w, int lane, Hook hook) {
    const int r = lane & 15, g = lane >> 4;
    const int c0 = ((g ^ (r >> 1)) << 4), c1 = c0 ^ 64;
    const int oa = ((w & 1) * 64 + r) * 128, ob = 16384 + ((w >> 1) * 64 + r) * 128;
    Frag f0, f1;
    LAS char* s = smem + slot * SLOT;
    nt_read(f0, s + oa + c0, s + ob + c0);
#pragma unroll 1
    for (int it = 0; it < nst - 1; ++it) {
        __builtin_amdgcn_sched_barrier(0);
        nt_read(f1, s + oa + c1, s + ob + c1);
        nt_mma(acc, f0);
        SGB();
        __builtin_amdgcn_sched_barrier(0);
        slot = (slot + 1 == 5) ? 0 : slot + 1;
        s = smem + slot * SLOT;
        hook(it);
        bar_lgkm();
        __builtin_amdgcn_sched_barrier(0);
        nt_read(f0, s + oa + c0, s + ob + c0);
        nt_mma(acc, f1);
        SGB();
        __builtin_amdgcn_sched_barrier(0);
    }
    nt_read(f1, s + oa + c1, s + ob + c1);
    nt_mma(acc, f0);
    SGB();
    __builtin_amdgcn_sched_barrier(0);
    slot = (slot + 1 == 5) ? 0 : slot + 1;
    if (LASTBAR) bar_lgkm();
    __builtin_amdgcn_sched_barrier(0);
    nt_mma(acc, f1);
    return slot;
}

#define PIN4(a, b, c, d) asm volatile("" : "+v"(a), "+v"(b), "+v"(c), "+v"(d))
__device__ __forceinline__ void t2_pin(f32x4 (&aq)[8], f32x4 (&ak)[9], f32x4 (&av)[9]) {
    asm volatile("" : "+v"(aq[0]), "+v"(aq[1]), "+v"(aq[2]), "+v"(aq[3]), "+v"(aq[4]), "+v"(aq[5]), "+v"(aq[6]), "+v"(aq[7]),
                      "+v"(ak[0]), "+v"(ak[1]), "+v"(ak[2]), "+v"(ak[3]), "+v"(ak[4]));
    asm volatile("" : "+v"(ak[5]), "+v"(ak[6]), "+v"(ak[7]), "+v"(ak[8]),
                      "+v"(av[0]), "+v"(av[1]), "+v"(av[2]), "+v"(av[3]), "+v"(av[4]), "+v"(av[5]), "+v"(av[6]), "+v"(av[7]), "+v"(av[8]));
}
struct Frag2 { bf16x8 x[9], w[3]; };
__device__ __forceinline__ void t2_read(Frag2& f, LAS char* px, LAS char* pw) {
    f.w[1] = lds_tr2(pw + 1024);
    f.x[0] = lds_b128(px);
    f.w[2] = lds_tr2(pw + 2048);
    f.w[0] = lds_tr2(pw);
#pragma unroll
    for (int rb = 1; rb < 9; ++rb) f.x[rb] = lds_b128(px + rb * 2048);
}
__device__ __forceinline__ void t2_mma(f32x4 (&aq)[8], f32x4 (&ak)[9], f32x4 (&av)[9], const Frag2& f) {
#pragma unroll
    for (int rb = 0; rb < 9; ++rb) {
        ak[rb] = mfma32(f.w[1], f.x[rb], ak[rb]);
        av[rb] = mfma32(f.x[rb], f.w[2], av[rb]);
        if (rb > 0) aq[rb - 1] = mfma32(f.w[0], f.x[rb], aq[rb - 1]);
    }
}
#define SGB2() do { _Pragma("unroll") for (int _i = 0; _i < 15; ++_i) { __builtin_amdgcn_sched_group_barrier(0x8, 1, 0); __builtin_amdgcn_sched_group_barrier(0x100, 1, 0); } __builtin_amdgcn_sched_group_barrier(0x8, 11, 0); } while (0)

struct Frag4 { bf16x8 w[4][2], o[4][2]; };
#define SGB4() do { _Pragma("unroll") for (int _i = 0; _i < 12; ++_i) { __builtin_amdgcn_sched_group_barrier(0x100, 2, 0); __builtin_amdgcn_sched_group_barrier(0x8, 1, 0); } __builtin_amdgcn_sched_group_barrier(0x8, 4, 0); } while (0)
__device__ __forceinline__ void pf(rsrc_t r, int voff, int soff) {
    asm volatile("s_nop 4\n\tbuffer_load_dword v255, %0, %1, %2 offen" ::"v"(voff), "s"(r), "s"(__builtin_amdgcn_readfirstlane(soff)) : "memory", "v255");
}

__global__ __launch_bounds__(512, 2) void fused(const __bf16* __restrict__ xa, const __bf16* __restrict__ cre, const __bf16* __restrict__ cim,
                                                const __bf16* __restrict__ hre, const __bf16* __restrict__ him, const __bf16* __restrict__ xb,
                                                const __bf16* __restrict__ wqkv, const __bf16* __restrict__ wo,
                                                char* __restrict__ ws, __bf16* __restrict__ out) {
    __shared__ __attribute__((aligned(1024))) char smem_[163840];
    LAS char* smem = (LAS char*)smem_;
    asm volatile("" ::"s"(xa), "s"(cre), "s"(cim), "s"(hre), "s"(him), "s"(xb), "s"(wqkv), "s"(wo), "s"(ws), "s"(out));
    const int tid = threadIdx.x, lane = tid & 63;
    const int wave = __builtin_amdgcn_readfirstlane(tid >> 6);
    const int bid = blockIdx.x, xcd = bid & 7, idx = bid >> 3;
    const int q = xcd & 3, th = xcd >> 2;
    const int ttl = idx >> 2, et = idx & 3, tt = th * 8 + ttl;
    const int t0 = tt * 128, e0 = et * 128;
    const bool typeA = (ttl & 1) == 0;
    const int pg = q;
    const int s1 = typeA ? ((q & 1) * 8 + th * 4 + (ttl >> 1)) * 128 : ((xcd >> 1) * 4 + (ttl >> 1)) * 128;
    const int hg = (xcd & 1) * 4 + et;
    const int s4 = (xcd * 4 + (idx >> 3)) * 64, n4 = (idx & 7) * 64;
    GAS unsigned* flags = (GAS unsigned*)(ws + WS_FLAGS);
    const int slot3 = typeA ? 3 : 0;
#define ROT1 (typeA ? 0 : (ttl & 6))
#define ROT3 (4 * et)
    const int rot1 = ROT1, rot3 = ROT3;

    if (__builtin_expect(wave >= 6, 1)) {
        const int lw = wave - 6;
        const int row = 8 * lw + (lane >> 3);
        const int ch = ((lane & 7) ^ (4 * lw + (lane >> 4))) * 16;
        const int cht = ((lane & 7) ^ (2 * (((lane >> 4) & 1) | (lw << 1)))) * 16;
        const bool master = (bid < 8 && lw == 0);
        GAS unsigned* myhb = flags + bid * FW;
        if (master && lane < 42) __hip_atomic_store(flags + (8 + bid * 42 + lane) * FW, 0u, __ATOMIC_RELAXED, __HIP_MEMORY_SCOPE_AGENT);
        rsrc_t rc = mk_rsrc((q >> 1) ? cim : cre, SEQ * SEQ * 2);
        const int vb = row * 4096 + ch;
        const int sb = t0 * 4096 + (q & 1) * 2048;
        auto issueC = [&](int st, int slot) {
            const int so = sb + ((st + rot3) & 15) * 128;
#pragma unroll
            for (int u = 0; u < 8; ++u) glds<NT>(rc, smem + slot * SLOT + 16384 + (lw + 2 * u) * 1024, vb, so + 16 * ((u & 4) | ((u + q) & 3)) * 4096);
        };
        rsrc_t rw = mk_rsrc(wo, DM * DM * 2);
        int slot = 0;
        if (typeA) {
            rsrc_t rh = mk_rsrc((pg >> 1) ? him : hre, DM * DM * 2);
            const int vh = row * 1024 + ch;
#pragma unroll 1
            for (int st = 0; st < 8; ++st) {
                if (st == 5) bar_vm<32>();
                if (st >= 5) { if (master) add_flag_async(myhb, lane); bar_vm<24>(); }
                const int so = e0 * 1024 + ((st + rot1) & 7) * 128;
#pragma unroll
                for (int u = 0; u < 8; ++u) glds<NT>(rh, smem + slot * SLOT + 16384 + (lw + 2 * u) * 1024, vh, so + 16 * u * 1024);
                slot = (slot + 1 == 5) ? 0 : slot + 1;
            }
        } else {
            rsrc_t rq = mk_rsrc(wqkv, DM * 3 * DM * 2);
            const int vq = row * 3072 + cht;
            int pos = 1;
#pragma unroll 1
            for (int st = 0; st < 8; ++st) {
                if (st == 3) bar_vm<24>();
                if (st >= 3) { if (master && st < 6) add_flag_async(myhb, lane); bar_vm<12>(); }
                LAS char* sl = smem + T2_BASE + pos * T2_SLOT;
                const int so = ((st + rot1) & 7) * 64 * 3072 + hg * 128;
#pragma unroll
                for (int v = 0; v < 4; ++v)
#pragma unroll
                    for (int cg = 0; cg < 3; ++cg)
                        glds<NT>(rq, sl + 18432 + ((lw + 2 * v) * 3 + cg) * 1024, vq, so + 16 * v * 3072 + cg * 1024);
                pos = (pos + 1 == 3) ? 0 : pos + 1;
            }
        }
#pragma unroll 1
        for (int st = 0; st < 16; ++st) {
            if (st >= 5) {
                if (st == 5) { if (typeA) bar_only(); bar_vm<32>(); }
                bar_vm<24>();
            } else if (typeA) {
                if (st < 4) bar_vm<24>(); else bar_only();
            } else {
                if (st == 1) bar_vm<20>();
                if (st == 2) bar_vm<16>();
                if (st == 3) bar_only();
            }
            issueC(st, slot);
            slot = (slot + 1 == 5) ? 0 : slot + 1;
            if (st == 7) pf(rw, lane * 128, idx * 16384 + lw * 8192);
        }
        const int vw = row * 1024 + cht;
#pragma unroll 1
        for (int st = 0; st < 4; ++st) {
            bar_vm<24>();
#pragma unroll
            for (int h = 0; h < 2; ++h)
#pragma unroll
                for (int u = 0; u < 4; ++u)
                    glds<NT>(rw, smem + slot * SLOT + h * 16384 + 8192 + (lw + 2 * u) * 1024, vw, ((st * 2 + h) * 64 + 16 * u) * 1024 + n4 * 2);
            slot = (slot + 1 == 5) ? 0 : slot + 1;
        }
        bar_vm<8>(); bar_vm<0>();
    } else if (__builtin_expect(wave >= 4, 1)) {
        const int lw = wave - 4;
        const int ch = ((lane & 7) ^ (4 * lw + (lane >> 4))) * 16;
        const int prow = 8 * (lane >> 5) + ((lane >> 3) & 3) + 16 * lw;
        const int row = 8 * lw + (lane >> 3);
        GAS unsigned* hbp = flags + (lane & 7) * FW;
        if (typeA) {
            rsrc_t rx = mk_rsrc(xa, SEQ * DM * 2);
            const int va = prow * 1024 + ch;
            int sl = 0;
#pragma unroll 1
            for (int st = 0; st < 8; ++st) {
                if (st == 5) bar_vm<32>();
                if (st >= 5) bar_vm<24>();
                if (st == 1 && lw == 0) LD_FLAG_ASYNC("v254", hbp);
                if (st == 7 && lw == 0) LD_FLAG_ASYNC("v253", hbp);
                const int so = s1 * 1024 + ((st + rot1) & 7) * 128;
#pragma unroll
                for (int u = 0; u < 8; ++u) glds<NT>(rx, smem + sl * SLOT + (lw + 2 * u) * 1024, va, so + (32 * (u >> 1) + 4 * (u & 1)) * 1024);
                sl = (sl + 1 == 5) ? 0 : sl + 1;
            }
            bar_vm<24>(); bar_vm<16>(); bar_vm<8>();
        } else {
            rsrc_t rx = mk_rsrc(xb, SEQ * DM * 2);
            const int vx = row * 1024 + ch;
            const int sxm = s1 * 1024, sxh = (s1 >= 16 ? s1 - 16 : 0) * 1024;
            int pos = 1;
#pragma unroll 1
            for (int st = 0; st < 8; ++st) {
                if (st == 3) bar_vm<18>();
                if (st >= 3) bar_vm<9>();
                if (st == 1 && lw == 0) LD_FLAG_ASYNC("v254", hbp);
                if (st == 7 && lw == 0) LD_FLAG_ASYNC("v253", hbp);
                LAS char* sl = smem + T2_BASE + pos * T2_SLOT;
                const int ko = ((st + rot1) & 7) * 128;
                glds<NT>(rx, sl + lw * 1024, vx, sxh + ko);
#pragma unroll
                for (int u = 0; u < 8; ++u) glds<NT>(rx, sl + 2048 + (lw + 2 * u) * 1024, vx, sxm + 16 * u * 1024 + ko);
                pos = (pos + 1 == 3) ? 0 : pos + 1;
            }
            bar_vm<9>();
        }
        bar_vm<0>();
        if (lw == 0) {
            unsigned a, b, spins = 0;
            RD_VEC("v254", a); RD_VEC("v253", b);
            bool moved = (a != b) || ((lane & 7) == bid);
            while (!__all(moved)) {
                if (bid < 8) add_flag(flags + bid * FW, lane);
                __builtin_amdgcn_s_sleep(1);
                b = __hip_atomic_load(hbp, __ATOMIC_RELAXED, __HIP_MEMORY_SCOPE_AGENT);
                moved = moved || (a != b);
                if (++spins > SPIN_CAP) break;
            }
        }
        bar_only();
        rsrc_t ra = mk_rsrc(ws + WS_ABT, 1024 * SEQ * 2), ro = mk_rsrc(ws + WS_O, SEQ * DM * 2);
        const int va = prow * 64 + (ch & 48) + (ch >> 6) * 8192;
        const int sa = (((q >> 1) * 4 + et) * 64 + (q & 1) * 32) * 8192;
        const int vo = row * 32 + (ch & 16) + (ch >> 5) * 65536;
        if (typeA) {
            bar_only();
            if (lw == 0 && lane < 4) (void)__hip_atomic_fetch_add(flags + (F1_IDX + (pg * 4 + et) * 4 + lane) * FW, 1u, __ATOMIC_RELAXED, __HIP_MEMORY_SCOPE_AGENT);
        }
        wait_ge(flags + (F1_IDX + (q * 4 + et) * 4 + lw + 2 * th) * FW, 8u, flags, lane);
        if (wave == 4) STAMP(6);
        int slot = slot3;
        GAS unsigned* f2 = flags + (F2_IDX + (s4 >> 7)) * FW;
#pragma unroll 1
        for (int st = 0; st < 16; ++st) {
            if (st == 5) bar_vm<32>();
            if (st >= 5) bar_vm<24>();
            const int so = sa + ((st + rot3) & 15) * 16384;
#pragma unroll
            for (int u = 0; u < 8; ++u) glds<16>(ra, smem + slot * SLOT + (lw + 2 * u) * 1024, va, so + (32 * (u >> 1) + 4 * (u & 1)) * 64);
            slot = (slot + 1 == 5) ? 0 : slot + 1;
            if (st == 12) LD_FLAG_ASYNC("v252", f2);
        }
#pragma unroll 1
        for (int st = 0; st < 4; ++st) {
            bar_vm<24>();
            if (st == 0) {
                unsigned f2v;
                RD_FLAG("v252", f2v);
                if (f2v < 32u) wait_ge(f2, 32u, flags, lane);
            }
#pragma unroll
            for (int h = 0; h < 2; ++h)
#pragma unroll
                for (int u = 0; u < 4; ++u)
                    glds<16>(ro, smem + slot * SLOT + h * 16384 + (lw + 2 * u) * 1024, vo, (s4 + 16 * u) * 32 + (st * 2 + h) * 4 * 65536);
            slot = (slot + 1 == 5) ? 0 : slot + 1;
        }
        bar_vm<8>(); bar_vm<0>();
    } else {
        const int r = lane & 15, g = lane >> 4;
        if (wave == 0) STAMP(0);
        const int tile = tt * 4 + et;
        rsrc_t rp = mk_rsrc(ws + WS_PART, 64 * 16 * 4 * 2048);
        const int inb = (tile * 4 + q) * 32768 + wave * 2048 + lane * 16;
        if (typeA) {
            f32x4 acc[4][4] = {};
            bar_lgkm();
            if (wave == 0) STAMP(1);
            nt_consume<true>(acc, smem, 0, 8, wave, lane, [](int) {});
            if (wave == 0) STAMP(2);
            const unsigned sg = (pg >> 1) ? 0x80008000u : 0u;
            rsrc_t rs = mk_rsrc(ws + WS_ABT, 1024 * SEQ * 2);
            const int vs = r * 64 + g * 16;
            const int ss = (((pg >> 1) * 4 + et) * 64 + (s1 >> 5) + (wave & 1) * 2) * 8192 + (wave >> 1) * 4096;
#pragma unroll
            for (int n = 0; n < 4; ++n)
#pragma unroll
                for (int u = 0; u < 2; ++u)
                    __builtin_amdgcn_raw_buffer_store_b128(pack8(acc[2 * u][n], acc[2 * u + 1][n]) ^ sg, rs, vs + ss + n * 1024 + u * 8192, 0, 16);
            if (wave == 0) STAMP(7);
            bar_vm<0>();
            if (wave == 0) STAMP(3);
        } else {
            const int c0 = ((g ^ (r >> 1)) << 4);
            const int ox = r * 128;
            const int qq = (lane & 15) >> 2, p = lane & 3;
            const int ow = 18432 + g * 3072 + qq * 128 + (((2 * wave + (p >> 1)) ^ (2 * ((qq >> 1) | ((g & 1) << 1)))) << 4) + 8 * (p & 1);
            f32x4 aq[8] = {}, ak[9] = {}, av[9] = {};
            Frag2 f0, f1;
            int pos = 1;
            LAS char* sl = smem + T2_BASE + pos * T2_SLOT;
            bar_lgkm();
            if (wave == 0) STAMP(1);
            t2_read(f0, sl + ox + c0, sl + ow);
#pragma unroll 1
            for (int it = 0; it < 7; ++it) {
                __builtin_amdgcn_sched_barrier(0);
                t2_read(f1, sl + ox + (c0 ^ 64), sl + ow + 12288);
                t2_mma(aq, ak, av, f0);
                SGB2();
                __builtin_amdgcn_sched_barrier(0);
                t2_pin(aq, ak, av);
                pos = (pos + 1 == 3) ? 0 : pos + 1;
                sl = smem + T2_BASE + pos * T2_SLOT;
                bar_lgkm();
                __builtin_amdgcn_sched_barrier(0);
                t2_read(f0, sl + ox + c0, sl + ow);
                t2_mma(aq, ak, av, f1);
                SGB2();
                __builtin_amdgcn_sched_barrier(0);
            }
            t2_read(f1, sl + ox + (c0 ^ 64), sl + ow + 12288);
            t2_mma(aq, ak, av, f0);
            SGB2();
            __builtin_amdgcn_sched_barrier(0);
            t2_pin(aq, ak, av);
            bar_lgkm();
            __builtin_amdgcn_sched_barrier(0);
            t2_mma(aq, ak, av, f1);
            if (wave == 0) STAMP(4);
            const int j = r, par = j & 1;
            const bool uc0 = (4 * g + par) <= j, uc1 = (4 * g + par + 2) <= j;
            const unsigned s0v = par ? 0x01000c0cu : 0x0c0c0100u, s1v = par ? 0x03020c0cu : 0x0c0c0302u, zs = 0x0c0c0c0cu;
            const unsigned selc0 = uc0 ? s0v : zs, selp0 = uc0 ? zs : s0v, selc1 = uc1 ? s1v : zs, selp1 = uc1 ? zs : s1v;
            rsrc_t rs = mk_rsrc(ws + WS_O, SEQ * DM * 2);
            const int vs = j * 32 + g * 8;
            const int ss = (hg * 4 + wave) * 65536 + s1 * 32;
            const f32x4 z = {0.f, 0.f, 0.f, 0.f};
            const float C = 0.25f * 1.44269504088896f;
            u32x2 kb[9], vb[9];
            float l0[8], l1[8], m[8];
#pragma unroll
            for (int i = 0; i < 9; ++i) kb[i] = cvt4u(ak[i]);
#pragma unroll
            for (int b = 0; b < 8; ++b) {
                u32x2 qb = cvt4u(aq[b]);
                f32x4 sc = mfma16(kb[b + 1], qb, z), sp = mfma16(kb[b], qb, z);
                float c0v = par ? sc[1] : sc[0], c1v = par ? sc[3] : sc[2];
                float p0v = par ? sp[1] : sp[0], p1v = par ? sp[3] : sp[2];
                if (b == 0 && s1 == 0) { p0v = -4e9f; p1v = -4e9f; }
                unsigned pk = cvtpk(uc0 ? c0v : p0v, uc1 ? c1v : p1v);
                l0[b] = __uint_as_float(pk << 16); l1[b] = __uint_as_float(pk & 0xffff0000u);
                m[b] = fmx(l0[b], l1[b]);
            }
            allred4<true>(m); allred4<true>(m + 4);
#pragma unroll
            for (int i = 0; i < 9; ++i) vb[i] = cvt4u(av[i]);
#pragma unroll
            for (int b = 0; b < 8; ++b) {
                l0[b] = __builtin_amdgcn_exp2f((l0[b] - m[b]) * C); l1[b] = __builtin_amdgcn_exp2f((l1[b] - m[b]) * C);
                m[b] = l0[b] + l1[b];
            }
            allred4<false>(m); allred4<false>(m + 4);
            f32x4 ov[8];
#pragma unroll
            for (int b = 0; b < 8; ++b) {
                unsigned P = cvtpk(l0[b] * m[b], l1[b] * m[b]);
                u32x2 pc = {__builtin_amdgcn_perm(0u, P, selc0), __builtin_amdgcn_perm(0u, P, selc1)};
                u32x2 pp = {__builtin_amdgcn_perm(0u, P, selp0), __builtin_amdgcn_perm(0u, P, selp1)};
                ov[b] = mfma16(vb[b], pp, mfma16(vb[b + 1], pc, z));
            }
#pragma unroll
            for (int b = 0; b < 8; ++b) __builtin_amdgcn_raw_buffer_store_b64(cvt4u(ov[b]), rs, vs, ss + b * 512, 16);
            if (wave == 0) STAMP(5);
        }
        f32x4 acc[4][4] = {};
        bar_lgkm();
        if (wave == 0) STAMP(9);
        int slot = nt_consume<false>(acc, smem, slot3, 16, wave, lane, [&](int it) {
            if (it == 2 && !typeA) {
                drain_vm();
                add_flag_async(flags + (F2_IDX + (s1 >> 7)) * FW, lane);
            }
        });
        if (wave == 0) STAMP(10);
        u32x4 sv[3][2];
#pragma unroll
        for (int n = 1; n < 4; ++n) { sv[n - 1][0] = pack8(acc[0][n], acc[1][n]); sv[n - 1][1] = pack8(acc[2][n], acc[3][n]); }
        auto send = [&](int n) {
            const int base = (((tile * 4 + ((q + n) & 3)) * 4 + q) * 4 + wave) * 2048 + lane * 16;
            __builtin_amdgcn_raw_buffer_store_b128(sv[n - 1][0], rp, base, 0, 16);
            __builtin_amdgcn_raw_buffer_store_b128(sv[n - 1][1], rp, base + 1024, 0, 16);
        };
#define ILV 1
        send(1);
        if (!ILV) { send(2); send(3); }
        f32x4 own[4];
#pragma unroll
        for (int m = 0; m < 4; ++m) own[m] = acc[m][0];
        f32x4 a4[2][2] = {};
        {
            const int c0 = ((g ^ (r >> 1)) << 4);
            const int oo = ((wave >> 1) * 32 + r) * 128;
            const int qq = (lane & 15) >> 2, p = lane & 3;
            const int swz = 2 * ((qq >> 1) | ((g & 1) << 1));
            const int ow0 = 8192 + g * 1024 + qq * 128 + ((((wave & 1) * 4 + (p >> 1)) ^ swz) << 4) + 8 * (p & 1);
            const int ow1 = ow0 ^ 32;
            Frag4 h0, h1;
            auto rd = [&](Frag4& f) {
                LAS char* sl = smem + slot * SLOT;
#pragma unroll
                for (int i = 0; i < 4; ++i) {
                    const int ho = (i >> 1) * 16384, kk = i & 1;
                    f.w[i][0] = lds_tr2(sl + ho + ow0 + kk * 4096);
                    f.o[i][0] = lds_b128(sl + ho + oo + (c0 ^ (kk * 64)));
                    f.o[i][1] = lds_b128(sl + ho + oo + (c0 ^ (kk * 64)) + 2048);
                    f.w[i][1] = lds_tr2(sl + ho + ow1 + kk * 4096);
                }
                slot = (slot + 1 == 5) ? 0 : slot + 1;
            };
            auto mm = [&](const Frag4& f) {
#pragma unroll
                for (int i = 0; i < 4; ++i) {
                    a4[0][0] = mfma32(f.w[i][0], f.o[i][0], a4[0][0]);
                    a4[0][1] = mfma32(f.w[i][0], f.o[i][1], a4[0][1]);
                    a4[1][0] = mfma32(f.w[i][1], f.o[i][0], a4[1][0]);
                    a4[1][1] = mfma32(f.w[i][1], f.o[i][1], a4[1][1]);
                }
                asm volatile("" : "+v"(a4[0][0]), "+v"(a4[0][1]), "+v"(a4[1][0]), "+v"(a4[1][1]));
            };
            asm volatile("" : "+v"(own[0]), "+v"(own[1]), "+v"(own[2]), "+v"(own[3]));
            bar_only();
            __builtin_amdgcn_sched_barrier(0);
            rd(h0);
            __builtin_amdgcn_sched_barrier(0);
            if (ILV) send(2);
            __builtin_amdgcn_sched_barrier(0);
            rd(h1); mm(h0); SGB4();
            __builtin_amdgcn_sched_barrier(0);
            if (ILV) send(3);
            __builtin_amdgcn_sched_barrier(0);
            rd(h0); mm(h1); SGB4();
            __builtin_amdgcn_sched_barrier(0);
            bar_only();
            __builtin_amdgcn_sched_barrier(0);
            rd(h1); mm(h0); SGB4();
            __builtin_amdgcn_sched_barrier(0);
            mm(h1);
            drain_vm();
            if (lane < 4 && lane != q)
                (void)__hip_atomic_fetch_add(flags + (F3_IDX + tile * 4 + lane) * FW, 1u, __ATOMIC_RELAXED, __HIP_MEMORY_SCOPE_AGENT);
#pragma unroll
            for (int n = 0; n < 2; ++n) {
                u32x2 lo = cvt4u(a4[0][n]), hi = cvt4u(a4[1][n]);
                auto r0 = __builtin_amdgcn_permlane16_swap(lo[0], hi[0], false, false);
                auto r1 = __builtin_amdgcn_permlane16_swap(lo[1], hi[1], false, false);
                u32x4 v = {r0[0], r1[0], r0[1], r1[1]};
                *(u32x4*)(out + (size_t)(s4 + (wave >> 1) * 32 + n * 16 + r) * 1024 + 512 + n4 + (wave & 1) * 32 + (g & 1) * 16 + (g >> 1) * 8) = v;
            }
        }
        if (wave == 0) STAMP(11);
        wait_ge(flags + (F3_IDX + tile * 4 + q) * FW, 12u, flags, lane);
        if (wave == 0) STAMP(12);
        u32x4 pv[3][2];
#pragma unroll
        for (int k = 0; k < 3; ++k) {
            const int o = inb + ((q + 1 + k) & 3) * 8192;
            pv[k][0] = __builtin_amdgcn_raw_buffer_load_b128(rp, o, 0, 16);
            pv[k][1] = __builtin_amdgcn_raw_buffer_load_b128(rp, o + 1024, 0, 16);
        }
#pragma unroll
        for (int k = 0; k < 3; ++k) {
            unpack8_add(pv[k][0], own[0], own[1]);
            unpack8_add(pv[k][1], own[2], own[3]);
        }
        const int trow = t0 + (wave >> 1) * 64 + q * 16 + r;
#pragma unroll
        for (int u = 0; u < 2; ++u)
            *(u32x4*)(out + (size_t)trow * 1024 + e0 + (wave & 1) * 64 + 32 * u + 8 * g) = pack8(own[2 * u], own[2 * u + 1]);
    }
}

extern "C" void kernel_launch(void* const* d_in, const int* in_sizes, int n_in,
                              void* d_out, int out_size, void* d_ws, size_t ws_size,
                              hipStream_t stream) {
    fused<<<256, 512, 0, stream>>>((const __bf16*)d_in[0], (const __bf16*)d_in[1], (const __bf16*)d_in[2], (const __bf16*)d_in[3],
                                   (const __bf16*)d_in[4], (const __bf16*)d_in[5], (const __bf16*)d_in[6], (const __bf16*)d_in[7],
                                   (char*)d_ws, (__bf16*)d_out);
}
```

```cpp
#include <hip/hip_runtime.h>
#include <hip/hip_bf16.h>
#include <stdint.h>

#define LAS __attribute__((address_space(3)))
#define GAS __attribute__((address_space(1)))
typedef __attribute__((ext_vector_type(8))) __bf16 bf16x8;
typedef __attribute__((ext_vector_type(4))) __bf16 bf16x4;
typedef __attribute__((ext_vector_type(2))) __bf16 bf16x2;
typedef __attribute__((ext_vector_type(2))) float f32x2;
typedef __attribute__((ext_vector_type(4))) short s16x4;
typedef __attribute__((ext_vector_type(4))) float f32x4;
typedef __attribute__((ext_vector_type(4))) unsigned u32x4;
typedef __attribute__((ext_vector_type(2))) unsigned u32x2;
typedef __amdgpu_buffer_rsrc_t rsrc_t;

#define NOSTAMPS 1
#define STAMP_N 16
#define STAMP_OFF (64u<<20)
#define STAMP(k) do{}while(0)

constexpr int SEQ = 2048, DM = 512;
constexpr int SLOT = 32768;
constexpr int T2_SLOT = 43008;
constexpr int T2_BASE = 32768;

constexpr unsigned WS_FLAGS = 0;
constexpr unsigned WS_ABT   = 1u << 20;
constexpr unsigned WS_O     = WS_ABT + (4u << 20);
constexpr unsigned WS_PART  = WS_O + (2u << 20);
constexpr int FW = 32;
constexpr int F1_IDX = 8, F2_IDX = 72, F3_IDX = 88, NFLAGS = 88 + 256;
constexpr int NT = 0;
#define NE 3
constexpr unsigned SPIN_CAP = 1u << 17;

__device__ __forceinline__ rsrc_t mk_rsrc(const void* p, unsigned bytes) {
    return __builtin_amdgcn_make_buffer_rsrc((void*)p, 0, bytes, 0x00020000);
}
template <int AUX>
__device__ __forceinline__ void glds(rsrc_t r, LAS char* dst, int voff, int soff) {
    __builtin_amdgcn_raw_ptr_buffer_load_lds(r, (LAS void*)dst, 16, voff, soff, 0, AUX);
}
template <int N> __device__ __forceinline__ void bar_vm() {
    asm volatile("s_waitcnt vmcnt(%0)\n\ts_barrier" ::"n"(N) : "memory");
}
__device__ __forceinline__ void bar_only() { asm volatile("s_barrier" ::: "memory"); }
__device__ __forceinline__ void bar_lgkm() {
    asm volatile("s_waitcnt lgkmcnt(0)\n\ts_barrier" ::: "memory");
}
__device__ __forceinline__ void drain_vm() { asm volatile("s_waitcnt vmcnt(0)" ::: "memory"); }
__device__ __forceinline__ f32x4 mfma32(bf16x8 a, bf16x8 b, f32x4 c) {
    return __builtin_amdgcn_mfma_f32_16x16x32_bf16(a, b, c, 0, 0, 0);
}
__device__ __forceinline__ f32x4 mfma16(u32x2 a, u32x2 b, f32x4 c) {
    return __builtin_amdgcn_mfma_f32_16x16x16bf16_1k(__builtin_bit_cast(s16x4, a), __builtin_bit_cast(s16x4, b), c, 0, 0, 0);
}
__device__ __forceinline__ bf16x8 lds_b128(LAS char* p) { return *(LAS bf16x8*)p; }
__device__ __forceinline__ bf16x8 lds_tr2(LAS char* p) {
    bf16x4 t0 = __builtin_amdgcn_ds_read_tr16_b64_v4bf16((LAS bf16x4*)p);
    bf16x4 t1 = __builtin_amdgcn_ds_read_tr16_b64_v4bf16((LAS bf16x4*)(p + 512));
    return __builtin_shufflevector(t0, t1, 0, 1, 2, 3, 4, 5, 6, 7);
}
__device__ __forceinline__ unsigned cvtpk(float a, float b) {
    f32x2 v = {a, b};
    unsigned u = __builtin_bit_cast(unsigned, __builtin_convertvector(v, bf16x2));
    asm("" : "+v"(u));
    return u;
}
__device__ __forceinline__ u32x2 cvt4u(f32x4 v) { u32x2 r = {cvtpk(v[0], v[1]), cvtpk(v[2], v[3])}; return r; }
__device__ __forceinline__ u32x4 pack8(f32x4 lo, f32x4 hi) {
    u32x4 r = {cvtpk(lo[0], lo[1]), cvtpk(lo[2], lo[3]), cvtpk(hi[0], hi[1]), cvtpk(hi[2], hi[3])};
    return r;
}
__device__ __forceinline__ void unpack8_add(u32x4 v, f32x4& lo, f32x4& hi) {
    unsigned a = v[0], b = v[1], c = v[2], d = v[3];
    lo[0] += __uint_as_float(a << 16); lo[1] += __uint_as_float(a & 0xffff0000u);
    lo[2] += __uint_as_float(b << 16); lo[3] += __uint_as_float(b & 0xffff0000u);
    hi[0] += __uint_as_float(c << 16); hi[1] += __uint_as_float(c & 0xffff0000u);
    hi[2] += __uint_as_float(d << 16); hi[3] += __uint_as_float(d & 0xffff0000u);
}
__device__ __forceinline__ float fmx(float a, float b) { return __builtin_elementwise_maximum(a, b); }
__device__ __forceinline__ void swap16(float& a, float& b) {
    auto r = __builtin_amdgcn_permlane16_swap(__float_as_uint(a), __float_as_uint(b), false, false);
    unsigned x = r[0], y = r[1]; a = __uint_as_float(x); b = __uint_as_float(y);
}
__device__ __forceinline__ void swap32(float& a, float& b) {
    auto r = __builtin_amdgcn_permlane32_swap(__float_as_uint(a), __float_as_uint(b), false, false);
    unsigned x = r[0], y = r[1]; a = __uint_as_float(x); b = __uint_as_float(y);
}
template <bool MAX> __device__ __forceinline__ float op2(float a, float b) { return MAX ? fmx(a, b) : a + b; }
template <bool MAX> __device__ __forceinline__ void allred4(float* v) {
    swap16(v[0], v[1]); float c = op2<MAX>(v[0], v[1]);
    swap16(v[2], v[3]); float d = op2<MAX>(v[2], v[3]);
    swap32(c, d); float e = op2<MAX>(c, d);
    if (!MAX) e = __builtin_amdgcn_rcpf(e);
    float e2 = e; swap32(e, e2);
    float f = e; swap16(e, f);
    float h = e2; swap16(e2, h);
    v[0] = e; v[1] = f; v[2] = e2; v[3] = h;
}
__device__ __forceinline__ unsigned ld_flag(GAS unsigned* f) {
    return __builtin_amdgcn_readfirstlane(__hip_atomic_load(f, __ATOMIC_RELAXED, __HIP_MEMORY_SCOPE_AGENT));
}
__device__ __forceinline__ void add_flag(GAS unsigned* f, int lane) {
    if (lane == 0) (void)__hip_atomic_fetch_add(f, 1u, __ATOMIC_RELAXED, __HIP_MEMORY_SCOPE_AGENT);
}
#define LD_FLAG_ASYNC(REG, f) asm volatile("global_load_dword " REG ", %0, off sc1" ::"v"(f) : "memory", REG)
#define RD_FLAG(REG, out) asm volatile("v_readfirstlane_b32 %0, " REG : "=s"(out)::"memory")
#define RD_VEC(REG, out) asm volatile("v_mov_b32 %0, " REG : "=v"(out)::"memory")
__device__ __forceinline__ void add_flag_async(GAS unsigned* f, int lane) {
    unsigned one = 1u;
    if (lane == 0) asm volatile("global_atomic_add %0, %1, off" ::"v"(f), "v"(one) : "memory");
}
__device__ __forceinline__ void wait_ge(GAS unsigned* f, unsigned target, GAS unsigned* hb, int lane) {
    unsigned spins = 0;
    while (ld_flag(f) < target) {
        ++spins;
        if ((spins & 31u) == 0u) {
            if (lane < 8) (void)__hip_atomic_fetch_add(hb + lane * FW, 1u, __ATOMIC_RELAXED, __HIP_MEMORY_SCOPE_AGENT);
            if (spins > SPIN_CAP) break;
        }
        __builtin_amdgcn_s_sleep(1);
    }
    asm volatile("" ::: "memory");
}
struct Frag { bf16x8 a[4], b[4]; };
#define SGB() do { _Pragma("unroll") for (int _i = 0; _i < 8; ++_i) { __builtin_amdgcn_sched_group_barrier(0x8, 1, 0); __builtin_amdgcn_sched_group_barrier(0x100, 1, 0); } __builtin_amdgcn_sched_group_barrier(0x8, 8, 0); } while (0)
__device__ __forceinline__ void nt_read(Frag& f, LAS char* pa, LAS char* pb) {
    f.a[0] = lds_b128(pa);
#pragma unroll
    for (int n = 0; n < 4; ++n) f.b[n] = lds_b128(pb + n * 2048);
#pragma unroll
    for (int m = 1; m < 4; ++m) f.a[m] = lds_b128(pa + m * 2048);
}
__device__ __forceinline__ void nt_mma(f32x4 (&acc)[4][4], const Frag& f) {
#pragma unroll
    for (int m = 0; m < 4; ++m)
#pragma unroll
        for (int n = 0; n < 4; ++n) acc[m][n] = mfma32(f.a[m], f.b[n], acc[m][n]);
}
__device__ __forceinline__ void bar_cons() {
    asm volatile("" ::: "memory");
    __builtin_amdgcn_s_waitcnt(0x0070);
    __builtin_amdgcn_s_barrier();
    asm volatile("" ::: "memory");
}
#define SGBV() do { _Pragma("unroll") for (int _i = 0; _i < 8; ++_i) { __builtin_amdgcn_sched_group_barrier(0x8, 1, 0); __builtin_amdgcn_sched_group_barrier(0x100, 1, 0); } \
    _Pragma("unroll") for (int _i = 0; _i < 2; ++_i) { __builtin_amdgcn_sched_group_barrier(0x8, 1, 0); __builtin_amdgcn_sched_group_barrier(0x4, 2, 0); } \
    _Pragma("unroll") for (int _i = 0; _i < 2; ++_i) { __builtin_amdgcn_sched_group_barrier(0x8, 1, 0); __builtin_amdgcn_sched_group_barrier(0x2, 1, 0); } \
    __builtin_amdgcn_sched_group_barrier(0x8, 4, 0); } while (0)
__device__ __forceinline__ void nt_pin(f32x4 (&acc)[4][4]) {
    asm volatile("" : "+v"(acc[0][0]), "+v"(acc[0][1]), "+v"(acc[0][2]), "+v"(acc[0][3]), "+v"(acc[1][0]), "+v"(acc[1][1]), "+v"(acc[1][2]), "+v"(acc[1][3]));
    asm volatile("" : "+v"(acc[2][0]), "+v"(acc[2][1]), "+v"(acc[2][2]), "+v"(acc[2][3]), "+v"(acc[3][0]), "+v"(acc[3][1]), "+v"(acc[3][2]), "+v"(acc[3][3]));
}
template <bool LASTBAR>
__device__ __forceinline__ int nt_consume(f32x4 (&acc)[4][4], LAS char* smem, int slot, int nst, int w, int lane) {
    const int r = lane & 15, g = lane >> 4;
    const int c0 = ((g ^ (r >> 1)) << 4), c1 = c0 ^ 64;
    const int oa = ((w & 1) * 64 + r) * 128, ob = 16384 + ((w >> 1) * 64 + r) * 128;
    const int oa0 = oa + c0, ob0 = ob + c0, oa1 = oa + c1, ob1 = ob + c1;
    Frag f0, f1;
    int so = slot * SLOT;
    nt_read(f0, smem + so + oa0, smem + so + ob0);
    int pa = so + oa1, pb = so + ob1;
#pragma unroll 1
    for (int it = 0; it < nst - 1; ++it) {
        __builtin_amdgcn_sched_barrier(0);
        nt_read(f1, smem + pa, smem + pb);
        nt_mma(acc, f0);
        so = (so == 4 * SLOT) ? 0 : so + SLOT;
        int qa = so + oa0, qb = so + ob0;
        SGBV();
        __builtin_amdgcn_sched_barrier(0);
        asm volatile("" : "+v"(qa), "+v"(qb));
        bar_cons();
        __builtin_amdgcn_sched_barrier(0);
        nt_read(f0, smem + qa, smem + qb);
        nt_mma(acc, f1);
        pa = so + oa1; pb = so + ob1;
        SGBV();
        __builtin_amdgcn_sched_barrier(0);
        asm volatile("" : "+v"(pa), "+v"(pb));
    }
    nt_read(f1, smem + pa, smem + pb);
    nt_mma(acc, f0);
    SGB();
    __builtin_amdgcn_sched_barrier(0);
    so = (so == 4 * SLOT) ? 0 : so + SLOT;
    if (LASTBAR) bar_cons();
    __builtin_amdgcn_sched_barrier(0);
    nt_mma(acc, f1);
    return so / SLOT;
}

#define PIN4(a, b, c, d) asm volatile("" : "+v"(a), "+v"(b), "+v"(c), "+v"(d))
__device__ __forceinline__ void t2_pin(f32x4 (&aq)[8], f32x4 (&ak)[9], f32x4 (&av)[9]) {
    asm volatile("" : "+v"(aq[0]), "+v"(aq[1]), "+v"(aq[2]), "+v"(aq[3]), "+v"(aq[4]), "+v"(aq[5]), "+v"(aq[6]), "+v"(aq[7]),
                      "+v"(ak[0]), "+v"(ak[1]), "+v"(ak[2]), "+v"(ak[3]), "+v"(ak[4]));
    asm volatile("" : "+v"(ak[5]), "+v"(ak[6]), "+v"(ak[7]), "+v"(ak[8]),
                      "+v"(av[0]), "+v"(av[1]), "+v"(av[2]), "+v"(av[3]), "+v"(av[4]), "+v"(av[5]), "+v"(av[6]), "+v"(av[7]), "+v"(av[8]));
}
struct Frag2 { bf16x8 x[9], w[3]; };
__device__ __forceinline__ void t2_read(Frag2& f, LAS char* px, LAS char* pw) {
    f.w[1] = lds_tr2(pw + 1024);
    f.x[0] = lds_b128(px);
    f.w[2] = lds_tr2(pw + 2048);
    f.w[0] = lds_tr2(pw);
#pragma unroll
    for (int rb = 1; rb < 9; ++rb) f.x[rb] = lds_b128(px + rb * 2048);
}
__device__ __forceinline__ void t2_mma(f32x4 (&aq)[8], f32x4 (&ak)[9], f32x4 (&av)[9], const Frag2& f) {
#pragma unroll
    for (int rb = 0; rb < 9; ++rb) {
        ak[rb] = mfma32(f.w[1], f.x[rb], ak[rb]);
        av[rb] = mfma32(f.x[rb], f.w[2], av[rb]);
        if (rb > 0) aq[rb - 1] = mfma32(f.w[0], f.x[rb], aq[rb - 1]);
    }
}
#define SGB2() do { _Pragma("unroll") for (int _i = 0; _i < 15; ++_i) { __builtin_amdgcn_sched_group_barrier(0x8, 1, 0); __builtin_amdgcn_sched_group_barrier(0x100, 1, 0); } \
    _Pragma("unroll") for (int _i = 0; _i < 2; ++_i) { __builtin_amdgcn_sched_group_barrier(0x8, 1, 0); __builtin_amdgcn_sched_group_barrier(0x4, 2, 0); } \
    _Pragma("unroll") for (int _i = 0; _i < 2; ++_i) { __builtin_amdgcn_sched_group_barrier(0x8, 1, 0); __builtin_amdgcn_sched_group_barrier(0x2, 1, 0); } \
    __builtin_amdgcn_sched_group_barrier(0x8, 7, 0); } while (0)

struct Frag4 { bf16x8 w[4][2], o[4][2]; };
#define SGB4() do { _Pragma("unroll") for (int _i = 0; _i < 12; ++_i) { __builtin_amdgcn_sched_group_barrier(0x100, 2, 0); __builtin_amdgcn_sched_group_barrier(0x8, 1, 0); } __builtin_amdgcn_sched_group_barrier(0x8, 4, 0); } while (0)
__device__ __forceinline__ void pf(rsrc_t r, int voff, int soff) {
    asm volatile("s_nop 4\n\tbuffer_load_dword v255, %0, %1, %2 offen" ::"v"(voff), "s"(r), "s"(__builtin_amdgcn_readfirstlane(soff)) : "memory", "v255");
}

__global__ __launch_bounds__(512, 2) void fused(const __bf16* __restrict__ xa, const __bf16* __restrict__ cre, const __bf16* __restrict__ cim,
                                                const __bf16* __restrict__ hre, const __bf16* __restrict__ him, const __bf16* __restrict__ xb,
                                                const __bf16* __restrict__ wqkv, const __bf16* __restrict__ wo,
                                                char* __restrict__ ws, __bf16* __restrict__ out) {
    __shared__ __attribute__((aligned(1024))) char smem_[163840];
    LAS char* smem = (LAS char*)smem_;
    asm volatile("" ::"s"(xa), "s"(cre), "s"(cim), "s"(hre), "s"(him), "s"(xb), "s"(wqkv), "s"(wo), "s"(ws), "s"(out));
    const int tid = threadIdx.x, lane = tid & 63;
    const int wave = __builtin_amdgcn_readfirstlane(tid >> 6);
    const int bid = blockIdx.x, xcd = bid & 7, idx = bid >> 3;
    const int q = xcd & 3, th = xcd >> 2;
    const int ttl = idx >> 2, et = idx & 3, tt = th * 8 + ttl;
    const int t0 = tt * 128, e0 = et * 128;
    const bool typeA = (ttl & 1) == 0;
    const int pg = q;
    const int s1 = typeA ? ((q & 1) * 8 + th * 4 + (ttl >> 1)) * 128 : ((xcd >> 1) * 4 + (ttl >> 1)) * 128;
    const int hg = (xcd & 1) * 4 + et;
    const int s4 = (xcd * 4 + (idx >> 3)) * 64, n4 = (idx & 7) * 64;
    GAS unsigned* flags = (GAS unsigned*)(ws + WS_FLAGS);
    const int slot3 = typeA ? 3 : 0;
#define ROT1 (typeA ? 0 : (ttl & 6))
#define ROT3 (4 * et)
    const int rot1 = ROT1, rot3 = ROT3;

    if (__builtin_expect(wave >= 6, 1)) {
        const int lw = wave - 6;
        const int row = 8 * lw + (lane >> 3);
        const int ch = ((lane & 7) ^ (4 * lw + (lane >> 4))) * 16;
        const int cht = ((lane & 7) ^ (2 * (((lane >> 4) & 1) | (lw << 1)))) * 16;
        const bool master = (bid < 8 && lw == 0);
        GAS unsigned* myhb = flags + bid * FW;
        if (master && lane < 42) __hip_atomic_store(flags + (8 + bid * 42 + lane) * FW, 0u, __ATOMIC_RELAXED, __HIP_MEMORY_SCOPE_AGENT);
        rsrc_t rc = mk_rsrc((q >> 1) ? cim : cre, SEQ * SEQ * 2);
        const int vb = row * 4096 + ch;
        const int sb = t0 * 4096 + (q & 1) * 2048;
        auto issueC = [&](int st, int slot) {
            const int so = sb + ((st + rot3) & 15) * 128;
#pragma unroll
            for (int u = 0; u < 8; ++u) glds<NT>(rc, smem + slot * SLOT + 16384 + (lw + 2 * u) * 1024, vb, so + 16 * ((u & 4) | ((u + q) & 3)) * 4096);
        };
        rsrc_t rw = mk_rsrc(wo, DM * DM * 2);
        int slot = 0;
        if (typeA) {
            rsrc_t rh = mk_rsrc((pg >> 1) ? him : hre, DM * DM * 2);
            const int vh = row * 1024 + ch;
#pragma unroll 1
            for (int st = 0; st < 8; ++st) {
                if (st == 5) bar_vm<32>();
                if (st >= 5) { if (master) add_flag_async(myhb, lane); bar_vm<24>(); }
                const int so = e0 * 1024 + ((st + rot1) & 7) * 128;
#pragma unroll
                for (int u = 0; u < 8; ++u) glds<NT>(rh, smem + slot * SLOT + 16384 + (lw + 2 * u) * 1024, vh, so + 16 * u * 1024);
                slot = (slot + 1 == 5) ? 0 : slot + 1;
            }
        } else {
            rsrc_t rq = mk_rsrc(wqkv, DM * 3 * DM * 2);
            const int vq = row * 3072 + cht;
            int pos = 1;
#pragma unroll 1
            for (int st = 0; st < 8; ++st) {
                if (st == 3) bar_vm<24>();
                if (st >= 3) { if (master && st < 6) add_flag_async(myhb, lane); bar_vm<12>(); }
                LAS char* sl = smem + T2_BASE + pos * T2_SLOT;
                const int so = ((st + rot1) & 7) * 64 * 3072 + hg * 128;
#pragma unroll
                for (int v = 0; v < 4; ++v)
#pragma unroll
                    for (int cg = 0; cg < 3; ++cg)
                        glds<NT>(rq, sl + 18432 + ((lw + 2 * v) * 3 + cg) * 1024, vq, so + 16 * v * 3072 + cg * 1024);
                pos = (pos + 1 == 3) ? 0 : pos + 1;
            }
        }
#pragma unroll 1
        for (int st = 0; st < 16; ++st) {
            if (st >= 5) {
                if (st == 5 && (!typeA || NE == 5)) { if (typeA) bar_only(); bar_vm<32>(); }
                bar_vm<24>();
            } else if (typeA) {
                if (NE == 5) { if (st < 4) bar_vm<24>(); else bar_only(); }
                else if (st < NE) bar_vm<24>();
                else if (st == NE) {
                    if (NE == 1) { bar_vm<24>(); bar_vm<16>(); bar_vm<8>(); }
                    if (NE == 2) { bar_vm<24>(); bar_vm<16>(); }
                    if (NE == 3) bar_vm<24>();
                    bar_only(); bar_only(); bar_vm<8 * (NE - 1)>();
                }
            } else {
                if (st == 1) bar_vm<20>();
                if (st == 2) bar_vm<16>();
                if (st == 3) bar_only();
            }
            issueC(st, slot);
            slot = (slot + 1 == 5) ? 0 : slot + 1;
            if (st == 7) pf(rw, lane * 128, idx * 16384 + lw * 8192);
        }
        const int vw = row * 1024 + cht;
#pragma unroll 1
        for (int st = 0; st < 4; ++st) {
            bar_vm<24>();
#pragma unroll
            for (int h = 0; h < 2; ++h)
#pragma unroll
                for (int u = 0; u < 4; ++u)
                    glds<NT>(rw, smem + slot * SLOT + h * 16384 + 8192 + (lw + 2 * u) * 1024, vw, ((st * 2 + h) * 64 + 16 * u) * 1024 + n4 * 2);
            slot = (slot + 1 == 5) ? 0 : slot + 1;
        }
        bar_vm<8>(); bar_vm<0>();
    } else if (__builtin_expect(wave >= 4, 1)) {
        const int lw = wave - 4;
        const int ch = ((lane & 7) ^ (4 * lw + (lane >> 4))) * 16;
        const int prow = 8 * (lane >> 5) + ((lane >> 3) & 3) + 16 * lw;
        const int row = 8 * lw + (lane >> 3);
        GAS unsigned* hbp = flags + (lane & 7) * FW;
        if (typeA) {
            rsrc_t rx = mk_rsrc(xa, SEQ * DM * 2);
            const int va = prow * 1024 + ch;
            int sl = 0;
#pragma unroll 1
            for (int st = 0; st < 8; ++st) {
                if (st == 5) bar_vm<32>();
                if (st >= 5) bar_vm<24>();
                if (st == 1 && lw == 0) LD_FLAG_ASYNC("v254", hbp);
                if (st == 7 && lw == 0) LD_FLAG_ASYNC("v253", hbp);
                const int so = s1 * 1024 + ((st + rot1) & 7) * 128;
#pragma unroll
                for (int u = 0; u < 8; ++u) glds<NT>(rx, smem + sl * SLOT + (lw + 2 * u) * 1024, va, so + (32 * (u >> 1) + 4 * (u & 1)) * 1024);
                sl = (sl + 1 == 5) ? 0 : sl + 1;
            }
            bar_vm<24>(); bar_vm<16>(); bar_vm<8>();
        } else {
            rsrc_t rx = mk_rsrc(xb, SEQ * DM * 2);
            const int vx = row * 1024 + ch;
            const int sxm = s1 * 1024, sxh = (s1 >= 16 ? s1 - 16 : 0) * 1024;
            int pos = 1;
#pragma unroll 1
            for (int st = 0; st < 8; ++st) {
                if (st == 3) bar_vm<18>();
                if (st >= 3) bar_vm<9>();
                if (st == 1 && lw == 0) LD_FLAG_ASYNC("v254", hbp);
                if (st == 7 && lw == 0) LD_FLAG_ASYNC("v253", hbp);
                LAS char* sl = smem + T2_BASE + pos * T2_SLOT;
                const int ko = ((st + rot1) & 7) * 128;
                glds<NT>(rx, sl + lw * 1024, vx, sxh + ko);
#pragma unroll
                for (int u = 0; u < 8; ++u) glds<NT>(rx, sl + 2048 + (lw + 2 * u) * 1024, vx, sxm + 16 * u * 1024 + ko);
                pos = (pos + 1 == 3) ? 0 : pos + 1;
            }
            bar_vm<9>();
        }
        bar_vm<0>();
        if (lw == 0) {
            unsigned a, b, spins = 0;
            RD_VEC("v254", a); RD_VEC("v253", b);
            bool moved = (a != b) || ((lane & 7) == bid);
            while (!__all(moved)) {
                if (bid < 8) add_flag(flags + bid * FW, lane);
                __builtin_amdgcn_s_sleep(1);
                b = __hip_atomic_load(hbp, __ATOMIC_RELAXED, __HIP_MEMORY_SCOPE_AGENT);
                moved = moved || (a != b);
                if (++spins > SPIN_CAP) break;
            }
        }
        bar_only();
        rsrc_t ra = mk_rsrc(ws + WS_ABT, 1024 * SEQ * 2), ro = mk_rsrc(ws + WS_O, SEQ * DM * 2);
        const int va = prow * 64 + (ch & 48) + (ch >> 6) * 8192;
        const int sa = (((q >> 1) * 4 + et) * 64 + (q & 1) * 32) * 8192;
        const int vo = row * 32 + (ch & 16) + (ch >> 5) * 65536;
        if (typeA) {
            bar_only();
            if (lw == 0 && lane < 4) (void)__hip_atomic_fetch_add(flags + (F1_IDX + (pg * 4 + et) * 4 + lane) * FW, 1u, __ATOMIC_RELAXED, __HIP_MEMORY_SCOPE_AGENT);
        }
        wait_ge(flags + (F1_IDX + (q * 4 + et) * 4 + lw + 2 * th) * FW, 8u, flags, lane);
        if (wave == 4) STAMP(6);
        int slot = slot3;
        GAS unsigned* f2 = flags + (F2_IDX + (s4 >> 7)) * FW;
#pragma unroll 1
        for (int st = 0; st < 16; ++st) {
            if (st == 5) bar_vm<32>();
            if (st >= 5) bar_vm<24>();
            if (st == 5 && lw == 1 && !typeA) add_flag_async(flags + (F2_IDX + (s1 >> 7)) * FW, lane);
            const int so = sa + ((st + rot3) & 15) * 16384;
#pragma unroll
            for (int u = 0; u < 8; ++u) glds<16>(ra, smem + slot * SLOT + (lw + 2 * u) * 1024, va, so + (32 * (u >> 1) + 4 * (u & 1)) * 64);
            slot = (slot + 1 == 5) ? 0 : slot + 1;
            if (st == 12) LD_FLAG_ASYNC("v252", f2);
        }
#pragma unroll 1
        for (int st = 0; st < 4; ++st) {
            bar_vm<24>();
            if (st == 0) {
                unsigned f2v;
                RD_FLAG("v252", f2v);
                if (f2v < 8u) wait_ge(f2, 8u, flags, lane);
            }
#pragma unroll
            for (int h = 0; h < 2; ++h)
#pragma unroll
                for (int u = 0; u < 4; ++u)
                    glds<16>(ro, smem + slot * SLOT + h * 16384 + (lw + 2 * u) * 1024, vo, (s4 + 16 * u) * 32 + (st * 2 + h) * 4 * 65536);
            slot = (slot + 1 == 5) ? 0 : slot + 1;
        }
        bar_vm<8>(); bar_vm<0>();
    } else {
        const int r = lane & 15, g = lane >> 4;
        if (wave == 0) STAMP(0);
        const int tile = tt * 4 + et;
        rsrc_t rp = mk_rsrc(ws + WS_PART, 64 * 16 * 4 * 2048);
        const int inb = (tile * 4 + q) * 32768 + wave * 2048 + lane * 16;
        if (typeA) {
            f32x4 acc[4][4] = {};
            nt_pin(acc);
            bar_lgkm();
            if (wave == 0) STAMP(1);
            nt_consume<true>(acc, smem, 0, 8, wave, lane);
            if (wave == 0) STAMP(2);
            const unsigned sg = (pg >> 1) ? 0x80008000u : 0u;
            rsrc_t rs = mk_rsrc(ws + WS_ABT, 1024 * SEQ * 2);
            const int vs = r * 64 + g * 16;
            const int ss = (((pg >> 1) * 4 + et) * 64 + (s1 >> 5) + (wave & 1) * 2) * 8192 + (wave >> 1) * 4096;
#pragma unroll
            for (int n = 0; n < 4; ++n)
#pragma unroll
                for (int u = 0; u < 2; ++u)
                    __builtin_amdgcn_raw_buffer_store_b128(pack8(acc[2 * u][n], acc[2 * u + 1][n]) ^ sg, rs, vs + ss + n * 1024 + u * 8192, 0, 16);
            if (wave == 0) STAMP(7);
            bar_vm<0>();
            if (wave == 0) STAMP(3);
        } else {
            const int c0 = ((g ^ (r >> 1)) << 4);
            const int ox = r * 128;
            const int qq = (lane & 15) >> 2, p = lane & 3;
            const int ow = 18432 + g * 3072 + qq * 128 + (((2 * wave + (p >> 1)) ^ (2 * ((qq >> 1) | ((g & 1) << 1)))) << 4) + 8 * (p & 1);
            f32x4 aq[8] = {}, ak[9] = {}, av[9] = {};
            Frag2 f0, f1;
            int so = T2_BASE + T2_SLOT;
            t2_pin(aq, ak, av);
            bar_lgkm();
            if (wave == 0) STAMP(1);
            t2_read(f0, smem + so + ox + c0, smem + so + ow);
            int px = so + ox + (c0 ^ 64), pw = so + ow;
#pragma unroll 1
            for (int it = 0; it < 7; ++it) {
                __builtin_amdgcn_sched_barrier(0);
                t2_read(f1, smem + px, smem + pw + 12288);
                t2_mma(aq, ak, av, f0);
                so = (so == T2_BASE + 2 * T2_SLOT) ? T2_BASE : so + T2_SLOT;
                int qx = so + ox + c0, qw = so + ow;
                SGB2();
                __builtin_amdgcn_sched_barrier(0);
                asm volatile("" : "+v"(qx), "+v"(qw));
                t2_pin(aq, ak, av);
                bar_cons();
                __builtin_amdgcn_sched_barrier(0);
                t2_read(f0, smem + qx, smem + qw);
                t2_mma(aq, ak, av, f1);
                px = qx ^ 64; pw = qw;
                SGB2();
                __builtin_amdgcn_sched_barrier(0);
                asm volatile("" : "+v"(px));
            }
            t2_read(f1, smem + px, smem + pw + 12288);
            t2_mma(aq, ak, av, f0);
            SGB2();
            __builtin_amdgcn_sched_barrier(0);
            t2_pin(aq, ak, av);
            bar_cons();
            __builtin_amdgcn_sched_barrier(0);
            t2_mma(aq, ak, av, f1);
            if (wave == 0) STAMP(4);
            const int j = r, par = j & 1;
            const bool uc0 = (4 * g + par) <= j, uc1 = (4 * g + par + 2) <= j;
            const unsigned s0v = par ? 0x01000c0cu : 0x0c0c0100u, s1v = par ? 0x03020c0cu : 0x0c0c0302u, zs = 0x0c0c0c0cu;
            const unsigned selc0 = uc0 ? s0v : zs, selp0 = uc0 ? zs : s0v, selc1 = uc1 ? s1v : zs, selp1 = uc1 ? zs : s1v;
            rsrc_t rs = mk_rsrc(ws + WS_O, SEQ * DM * 2);
            const int vs = j * 32 + g * 8;
            const int ss = (hg * 4 + wave) * 65536 + s1 * 32;
            const f32x4 z = {0.f, 0.f, 0.f, 0.f};
            const float C = 0.25f * 1.44269504088896f;
            u32x2 kb[9], vb[9];
            float l0[8], l1[8], m[8];
#pragma unroll
            for (int i = 0; i < 9; ++i) kb[i] = cvt4u(ak[i]);
#pragma unroll
            for (int b = 0; b < 8; ++b) {
                u32x2 qb = cvt4u(aq[b]);
                f32x4 sc = mfma16(kb[b + 1], qb, z), sp = mfma16(kb[b], qb, z);
                float c0v = par ? sc[1] : sc[0], c1v = par ? sc[3] : sc[2];
                float p0v = par ? sp[1] : sp[0], p1v = par ? sp[3] : sp[2];
                if (b == 0 && s1 == 0) { p0v = -4e9f; p1v = -4e9f; }
                unsigned pk = cvtpk(uc0 ? c0v : p0v, uc1 ? c1v : p1v);
                l0[b] = __uint_as_float(pk << 16); l1[b] = __uint_as_float(pk & 0xffff0000u);
                m[b] = fmx(l0[b], l1[b]);
            }
            allred4<true>(m); allred4<true>(m + 4);
#pragma unroll
            for (int i = 0; i < 9; ++i) vb[i] = cvt4u(av[i]);
#pragma unroll
            for (int b = 0; b < 8; ++b) {
                l0[b] = __builtin_amdgcn_exp2f((l0[b] - m[b]) * C); l1[b] = __builtin_amdgcn_exp2f((l1[b] - m[b]) * C);
                m[b] = l0[b] + l1[b];
            }
            allred4<false>(m); allred4<false>(m + 4);
            f32x4 ov[8];
#pragma unroll
            for (int b = 0; b < 8; ++b) {
                unsigned P = cvtpk(l0[b] * m[b], l1[b] * m[b]);
                u32x2 pc = {__builtin_amdgcn_perm(0u, P, selc0), __builtin_amdgcn_perm(0u, P, selc1)};
                u32x2 pp = {__builtin_amdgcn_perm(0u, P, selp0), __builtin_amdgcn_perm(0u, P, selp1)};
                ov[b] = mfma16(vb[b], pp, mfma16(vb[b + 1], pc, z));
            }
#pragma unroll
            for (int b = 0; b < 8; ++b) __builtin_amdgcn_raw_buffer_store_b64(cvt4u(ov[b]), rs, vs, ss + b * 512, 16);
            if (wave == 0) STAMP(5);
        }
        f32x4 acc[4][4] = {};
        nt_pin(acc);
        bar_lgkm();
        if (wave == 0) STAMP(9);
        int slot = nt_consume<false>(acc, smem, slot3, 16, wave, lane);
        if (wave == 0) STAMP(10);
        u32x4 sv[3][2];
#pragma unroll
        for (int n = 1; n < 4; ++n) { sv[n - 1][0] = pack8(acc[0][n], acc[1][n]); sv[n - 1][1] = pack8(acc[2][n], acc[3][n]); }
        auto send = [&](int n) {
            const int base = (((tile * 4 + ((q + n) & 3)) * 4 + q) * 4 + wave) * 2048 + lane * 16;
            __builtin_amdgcn_raw_buffer_store_b128(sv[n - 1][0], rp, base, 0, 16);
            __builtin_amdgcn_raw_buffer_store_b128(sv[n - 1][1], rp, base + 1024, 0, 16);
        };
#define ILV 1
        send(1);
        if (!ILV) { send(2); send(3); }
        f32x4 own[4];
#pragma unroll
        for (int m = 0; m < 4; ++m) own[m] = acc[m][0];
        f32x4 a4[2][2] = {};
        {
            const int c0 = ((g ^ (r >> 1)) << 4);
            const int oo = ((wave >> 1) * 32 + r) * 128;
            const int qq = (lane & 15) >> 2, p = lane & 3;
            const int swz = 2 * ((qq >> 1) | ((g & 1) << 1));
            const int ow0 = 8192 + g * 1024 + qq * 128 + ((((wave & 1) * 4 + (p >> 1)) ^ swz) << 4) + 8 * (p & 1);
            const int ow1 = ow0 ^ 32;
            Frag4 h0, h1;
            auto rd = [&](Frag4& f) {
                LAS char* sl = smem + slot * SLOT;
#pragma unroll
                for (int i = 0; i < 4; ++i) {
                    const int ho = (i >> 1) * 16384, kk = i & 1;
                    f.w[i][0] = lds_tr2(sl + ho + ow0 + kk * 4096);
                    f.o[i][0] = lds_b128(sl + ho + oo + (c0 ^ (kk * 64)));
                    f.o[i][1] = lds_b128(sl + ho + oo + (c0 ^ (kk * 64)) + 2048);
                    f.w[i][1] = lds_tr2(sl + ho + ow1 + kk * 4096);
                }
                slot = (slot + 1 == 5) ? 0 : slot + 1;
            };
            auto mm = [&](const Frag4& f) {
#pragma unroll
                for (int i = 0; i < 4; ++i) {
                    a4[0][0] = mfma32(f.w[i][0], f.o[i][0], a4[0][0]);
                    a4[0][1] = mfma32(f.w[i][0], f.o[i][1], a4[0][1]);
                    a4[1][0] = mfma32(f.w[i][1], f.o[i][0], a4[1][0]);
                    a4[1][1] = mfma32(f.w[i][1], f.o[i][1], a4[1][1]);
                }
                asm volatile("" : "+v"(a4[0][0]), "+v"(a4[0][1]), "+v"(a4[1][0]), "+v"(a4[1][1]));
            };
            asm volatile("" : "+v"(own[0]), "+v"(own[1]), "+v"(own[2]), "+v"(own[3]));
            bar_only();
            __builtin_amdgcn_sched_barrier(0);
            rd(h0);
            __builtin_amdgcn_sched_barrier(0);
            if (ILV) send(2);
            __builtin_amdgcn_sched_barrier(0);
            rd(h1); mm(h0); SGB4();
            __builtin_amdgcn_sched_barrier(0);
            if (ILV) send(3);
            __builtin_amdgcn_sched_barrier(0);
            rd(h0); mm(h1); SGB4();
            __builtin_amdgcn_sched_barrier(0);
            bar_only();
            __builtin_amdgcn_sched_barrier(0);
            rd(h1); mm(h0); SGB4();
            __builtin_amdgcn_sched_barrier(0);
            mm(h1);
            drain_vm();
            if (lane < 4 && lane != q)
                (void)__hip_atomic_fetch_add(flags + (F3_IDX + tile * 4 + lane) * FW, 1u, __ATOMIC_RELAXED, __HIP_MEMORY_SCOPE_AGENT);
#pragma unroll
            for (int n = 0; n < 2; ++n) {
                u32x2 lo = cvt4u(a4[0][n]), hi = cvt4u(a4[1][n]);
                auto r0 = __builtin_amdgcn_permlane16_swap(lo[0], hi[0], false, false);
                auto r1 = __builtin_amdgcn_permlane16_swap(lo[1], hi[1], false, false);
                u32x4 v = {r0[0], r1[0], r0[1], r1[1]};
                *(u32x4*)(out + (size_t)(s4 + (wave >> 1) * 32 + n * 16 + r) * 1024 + 512 + n4 + (wave & 1) * 32 + (g & 1) * 16 + (g >> 1) * 8) = v;
            }
        }
        if (wave == 0) STAMP(11);
        wait_ge(flags + (F3_IDX + tile * 4 + q) * FW, 12u, flags, lane);
        if (wave == 0) STAMP(12);
        u32x4 pv[3][2];
#pragma unroll
        for (int k = 0; k < 3; ++k) {
            const int o = inb + ((q + 1 + k) & 3) * 8192;
            pv[k][0] = __builtin_amdgcn_raw_buffer_load_b128(rp, o, 0, 16);
            pv[k][1] = __builtin_amdgcn_raw_buffer_load_b128(rp, o + 1024, 0, 16);
        }
#pragma unroll
        for (int k = 0; k < 3; ++k) {
            unpack8_add(pv[k][0], own[0], own[1]);
            unpack8_add(pv[k][1], own[2], own[3]);
        }
        const int trow = t0 + (wave >> 1) * 64 + q * 16 + r;
#pragma unroll
        for (int u = 0; u < 2; ++u)
            *(u32x4*)(out + (size_t)trow * 1024 + e0 + (wave & 1) * 64 + 32 * u + 8 * g) = pack8(own[2 * u], own[2 * u + 1]);
    }
}

extern "C" void kernel_launch(void* const* d_in, const int* in_sizes, int n_in,
                              void* d_out, int out_size, void* d_ws, size_t ws_size,
                              hipStream_t stream) {
    fused<<<256, 512, 0, stream>>>((const __bf16*)d_in[0], (const __bf16*)d_in[1], (const __bf16*)d_in[2], (const __bf16*)d_in[3],
                                   (const __bf16*)d_in[4], (const __bf16*)d_in[5], (const __bf16*)d_in[6], (const __bf16*)d_in[7],
                                   (char*)d_ws, (__bf16*)d_out);
}
```

```cpp
#define ROT1 (4*(((ttl>>1)+et)&1))
#include <hip/hip_runtime.h>
#include <hip/hip_bf16.h>
#include <stdint.h>

#define LAS __attribute__((address_space(3)))
#define GAS __attribute__((address_space(1)))
typedef __attribute__((ext_vector_type(8))) __bf16 bf16x8;
typedef __attribute__((ext_vector_type(4))) __bf16 bf16x4;
typedef __attribute__((ext_vector_type(2))) __bf16 bf16x2;
typedef __attribute__((ext_vector_type(2))) float f32x2;
typedef __attribute__((ext_vector_type(4))) short s16x4;
typedef __attribute__((ext_vector_type(4))) float f32x4;
typedef __attribute__((ext_vector_type(4))) unsigned u32x4;
typedef __attribute__((ext_vector_type(2))) unsigned u32x2;
typedef __amdgpu_buffer_rsrc_t rsrc_t;

#define NOSTAMPS 1
#define STAMP_N 16
#define STAMP_OFF (64u<<20)
#define STAMP(k) do{}while(0)

constexpr int SEQ = 2048, DM = 512;
constexpr int SLOT = 32768;
constexpr int T2_SLOT = 43008;
constexpr int T2_BASE = 32768;

constexpr unsigned WS_FLAGS = 0;
constexpr unsigned WS_ABT   = 1u << 20;
constexpr unsigned WS_O     = WS_ABT + (4u << 20);
constexpr unsigned WS_PART  = WS_O + (2u << 20);
constexpr int FW = 32;
constexpr int F1_IDX = 8, F2_IDX = 72, F3_IDX = 88, NFLAGS = 88 + 256;
constexpr int NT = 0;
#define NE 3
constexpr unsigned SPIN_CAP = 1u << 17;

__device__ __forceinline__ rsrc_t mk_rsrc(const void* p, unsigned bytes) {
    return __builtin_amdgcn_make_buffer_rsrc((void*)p, 0, bytes, 0x00020000);
}
template <int AUX>
__device__ __forceinline__ void glds(rsrc_t r, LAS char* dst, int voff, int soff) {
    __builtin_amdgcn_raw_ptr_buffer_load_lds(r, (LAS void*)dst, 16, voff, soff, 0, AUX);
}
template <int N> __device__ __forceinline__ void bar_vm() {
    asm volatile("s_waitcnt vmcnt(%0)\n\ts_barrier" ::"n"(N) : "memory");
}
__device__ __forceinline__ void bar_only() { asm volatile("s_barrier" ::: "memory"); }
__device__ __forceinline__ void bar_lgkm() {
    asm volatile("s_waitcnt lgkmcnt(0)\n\ts_barrier" ::: "memory");
}
__device__ __forceinline__ void drain_vm() { asm volatile("s_waitcnt vmcnt(0)" ::: "memory"); }
__device__ __forceinline__ f32x4 mfma32(bf16x8 a, bf16x8 b, f32x4 c) {
    return __builtin_amdgcn_mfma_f32_16x16x32_bf16(a, b, c, 0, 0, 0);
}
__device__ __forceinline__ f32x4 mfma16(u32x2 a, u32x2 b, f32x4 c) {
    return __builtin_amdgcn_mfma_f32_16x16x16bf16_1k(__builtin_bit_cast(s16x4, a), __builtin_bit_cast(s16x4, b), c, 0, 0, 0);
}
__device__ __forceinline__ bf16x8 lds_b128(LAS char* p) { return *(LAS bf16x8*)p; }
__device__ __forceinline__ bf16x8 lds_tr2(LAS char* p) {
    bf16x4 t0 = __builtin_amdgcn_ds_read_tr16_b64_v4bf16((LAS bf16x4*)p);
    bf16x4 t1 = __builtin_amdgcn_ds_read_tr16_b64_v4bf16((LAS bf16x4*)(p + 512));
    return __builtin_shufflevector(t0, t1, 0, 1, 2, 3, 4, 5, 6, 7);
}
__device__ __forceinline__ unsigned cvtpk(float a, float b) {
    f32x2 v = {a, b};
    unsigned u = __builtin_bit_cast(unsigned, __builtin_convertvector(v, bf16x2));
    asm("" : "+v"(u));
    return u;
}
__device__ __forceinline__ u32x2 cvt4u(f32x4 v) { u32x2 r = {cvtpk(v[0], v[1]), cvtpk(v[2], v[3])}; return r; }
__device__ __forceinline__ u32x4 pack8(f32x4 lo, f32x4 hi) {
    u32x4 r = {cvtpk(lo[0], lo[1]), cvtpk(lo[2], lo[3]), cvtpk(hi[0], hi[1]), cvtpk(hi[2], hi[3])};
    return r;
}
__device__ __forceinline__ void unpack8_add(u32x4 v, f32x4& lo, f32x4& hi) {
    unsigned a = v[0], b = v[1], c = v[2], d = v[3];
    lo[0] += __uint_as_float(a << 16); lo[1] += __uint_as_float(a & 0xffff0000u);
    lo[2] += __uint_as_float(b << 16); lo[3] += __uint_as_float(b & 0xffff0000u);
    hi[0] += __uint_as_float(c << 16); hi[1] += __uint_as_float(c & 0xffff0000u);
    hi[2] += __uint_as_float(d << 16); hi[3] += __uint_as_float(d & 0xffff0000u);
}
__device__ __forceinline__ float fmx(float a, float b) { return __builtin_elementwise_maximum(a, b); }
__device__ __forceinline__ void swap16(float& a, float& b) {
    auto r = __builtin_amdgcn_permlane16_swap(__float_as_uint(a), __float_as_uint(b), false, false);
    unsigned x = r[0], y = r[1]; a = __uint_as_float(x); b = __uint_as_float(y);
}
__device__ __forceinline__ void swap32(float& a, float& b) {
    auto r = __builtin_amdgcn_permlane32_swap(__float_as_uint(a), __float_as_uint(b), false, false);
    unsigned x = r[0], y = r[1]; a = __uint_as_float(x); b = __uint_as_float(y);
}
template <bool MAX> __device__ __forceinline__ float op2(float a, float b) { return MAX ? fmx(a, b) : a + b; }
template <bool MAX> __device__ __forceinline__ void allred4(float* v) {
    swap16(v[0], v[1]); float c = op2<MAX>(v[0], v[1]);
    swap16(v[2], v[3]); float d = op2<MAX>(v[2], v[3]);
    swap32(c, d); float e = op2<MAX>(c, d);
    if (!MAX) e = __builtin_amdgcn_rcpf(e);
    float e2 = e; swap32(e, e2);
    float f = e; swap16(e, f);
    float h = e2; swap16(e2, h);
    v[0] = e; v[1] = f; v[2] = e2; v[3] = h;
}
__device__ __forceinline__ unsigned ld_flag(GAS unsigned* f) {
    return __builtin_amdgcn_readfirstlane(__hip_atomic_load(f, __ATOMIC_RELAXED, __HIP_MEMORY_SCOPE_AGENT));
}
__device__ __forceinline__ void add_flag(GAS unsigned* f, int lane) {
    if (lane == 0) (void)__hip_atomic_fetch_add(f, 1u, __ATOMIC_RELAXED, __HIP_MEMORY_SCOPE_AGENT);
}
#define LD_FLAG_ASYNC(REG, f) asm volatile("global_load_dword " REG ", %0, off sc1" ::"v"(f) : "memory", REG)
#define RD_FLAG(REG, out) asm volatile("v_readfirstlane_b32 %0, " REG : "=s"(out)::"memory")
#define RD_VEC(REG, out) asm volatile("v_mov_b32 %0, " REG : "=v"(out)::"memory")
__device__ __forceinline__ void add_flag_async(GAS unsigned* f, int lane) {
    unsigned one = 1u;
    if (lane == 0) asm volatile("global_atomic_add %0, %1, off" ::"v"(f), "v"(one) : "memory");
}
__device__ __forceinline__ void wait_ge(GAS unsigned* f, unsigned target, GAS unsigned* hb, int lane) {
    unsigned spins = 0;
    while (ld_flag(f) < target) {
        ++spins;
        if ((spins & 31u) == 0u) {
            if (lane < 8) (void)__hip_atomic_fetch_add(hb + lane * FW, 1u, __ATOMIC_RELAXED, __HIP_MEMORY_SCOPE_AGENT);
            if (spins > SPIN_CAP) break;
        }
        __builtin_amdgcn_s_sleep(1);
    }
    asm volatile("" ::: "memory");
}
struct Frag { bf16x8 a[4], b[4]; };
#define SGB() do { _Pragma("unroll") for (int _i = 0; _i < 8; ++_i) { __builtin_amdgcn_sched_group_barrier(0x8, 1, 0); __builtin_amdgcn_sched_group_barrier(0x100, 1, 0); } __builtin_amdgcn_sched_group_barrier(0x8, 8, 0); } while (0)
__device__ __forceinline__ void nt_read(Frag& f, LAS char* pa, LAS char* pb) {
    f.a[0] = lds_b128(pa);
#pragma unroll
    for (int n = 0; n < 4; ++n) f.b[n] = lds_b128(pb + n * 2048);
#pragma unroll
    for (int m = 1; m < 4; ++m) f.a[m] = lds_b128(pa + m * 2048);
}
__device__ __forceinline__ void nt_mma(f32x4 (&acc)[4][4], const Frag& f) {
#pragma unroll
    for (int m = 0; m < 4; ++m)
#pragma unroll
        for (int n = 0; n < 4; ++n) acc[m][n] = mfma32(f.a[m], f.b[n], acc[m][n]);
}
__device__ __forceinline__ void bar_cons() {
    asm volatile("" ::: "memory");
    __builtin_amdgcn_s_waitcnt(0x0070);
    __builtin_amdgcn_s_barrier();
    asm volatile("" ::: "memory");
}
#define SGBV() do { _Pragma("unroll") for (int _i = 0; _i < 8; ++_i) { __builtin_amdgcn_sched_group_barrier(0x8, 1, 0); __builtin_amdgcn_sched_group_barrier(0x100, 1, 0); } \
    _Pragma("unroll") for (int _i = 0; _i < 2; ++_i) { __builtin_amdgcn_sched_group_barrier(0x8, 1, 0); __builtin_amdgcn_sched_group_barrier(0x4, 2, 0); } \
    _Pragma("unroll") for (int _i = 0; _i < 2; ++_i) { __builtin_amdgcn_sched_group_barrier(0x8, 1, 0); __builtin_amdgcn_sched_group_barrier(0x2, 1, 0); } \
    __builtin_amdgcn_sched_group_barrier(0x8, 4, 0); } while (0)
__device__ __forceinline__ void nt_pin(f32x4 (&acc)[4][4]) {
    asm volatile("" : "+v"(acc[0][0]), "+v"(acc[0][1]), "+v"(acc[0][2]), "+v"(acc[0][3]), "+v"(acc[1][0]), "+v"(acc[1][1]), "+v"(acc[1][2]), "+v"(acc[1][3]));
    asm volatile("" : "+v"(acc[2][0]), "+v"(acc[2][1]), "+v"(acc[2][2]), "+v"(acc[2][3]), "+v"(acc[3][0]), "+v"(acc[3][1]), "+v"(acc[3][2]), "+v"(acc[3][3]));
}
template <bool LASTBAR>
__device__ __forceinline__ int nt_consume(f32x4 (&acc)[4][4], LAS char* smem, int slot, int nst, int w, int lane) {
    const int r = lane & 15, g = lane >> 4;
    const int c0 = ((g ^ (r >> 1)) << 4), c1 = c0 ^ 64;
    const int oa = ((w & 1) * 64 + r) * 128, ob = 16384 + ((w >> 1) * 64 + r) * 128;
    const int oa0 = oa + c0, ob0 = ob + c0, oa1 = oa + c1, ob1 = ob + c1;
    Frag f0, f1;
    int so = slot * SLOT;
    nt_read(f0, smem + so + oa0, smem + so + ob0);
    int pa = so + oa1, pb = so + ob1;
#pragma unroll 1
    for (int it = 0; it < nst - 1; ++it) {
        __builtin_amdgcn_sched_barrier(0);
        nt_read(f1, smem + pa, smem + pb);
        nt_mma(acc, f0);
        so = (so == 4 * SLOT) ? 0 : so + SLOT;
        int qa = so + oa0, qb = so + ob0;
        SGBV();
        __builtin_amdgcn_sched_barrier(0);
        asm volatile("" : "+v"(qa), "+v"(qb));
        bar_cons();
        __builtin_amdgcn_sched_barrier(0);
        nt_read(f0, smem + qa, smem + qb);
        nt_mma(acc, f1);
        pa = so + oa1; pb = so + ob1;
        SGBV();
        __builtin_amdgcn_sched_barrier(0);
        asm volatile("" : "+v"(pa), "+v"(pb));
    }
    nt_read(f1, smem + pa, smem + pb);
    nt_mma(acc, f0);
    SGB();
    __builtin_amdgcn_sched_barrier(0);
    so = (so == 4 * SLOT) ? 0 : so + SLOT;
    if (LASTBAR) bar_cons();
    __builtin_amdgcn_sched_barrier(0);
    nt_mma(acc, f1);
    return so / SLOT;
}

#define PIN4(a, b, c, d) asm volatile("" : "+v"(a), "+v"(b), "+v"(c), "+v"(d))
__device__ __forceinline__ void t2_pin(f32x4 (&aq)[8], f32x4 (&ak)[9], f32x4 (&av)[9]) {
    asm volatile("" : "+v"(aq[0]), "+v"(aq[1]), "+v"(aq[2]), "+v"(aq[3]), "+v"(aq[4]), "+v"(aq[5]), "+v"(aq[6]), "+v"(aq[7]),
                      "+v"(ak[0]), "+v"(ak[1]), "+v"(ak[2]), "+v"(ak[3]), "+v"(ak[4]));
    asm volatile("" : "+v"(ak[5]), "+v"(ak[6]), "+v"(ak[7]), "+v"(ak[8]),
                      "+v"(av[0]), "+v"(av[1]), "+v"(av[2]), "+v"(av[3]), "+v"(av[4]), "+v"(av[5]), "+v"(av[6]), "+v"(av[7]), "+v"(av[8]));
}
struct Frag2 { bf16x8 x[9], w[3]; };
__device__ __forceinline__ void t2_read(Frag2& f, LAS char* px, LAS char* pw) {
    f.w[1] = lds_tr2(pw + 1024);
    f.x[0] = lds_b128(px);
    f.w[2] = lds_tr2(pw + 2048);
    f.w[0] = lds_tr2(pw);
#pragma unroll
    for (int rb = 1; rb < 9; ++rb) f.x[rb] = lds_b128(px + rb * 2048);
}
__device__ __forceinline__ void t2_mma(f32x4 (&aq)[8], f32x4 (&ak)[9], f32x4 (&av)[9], const Frag2& f) {
#pragma unroll
    for (int rb = 0; rb < 9; ++rb) {
        ak[rb] = mfma32(f.w[1], f.x[rb], ak[rb]);
        av[rb] = mfma32(f.x[rb], f.w[2], av[rb]);
        if (rb > 0) aq[rb - 1] = mfma32(f.w[0], f.x[rb], aq[rb - 1]);
    }
}
#define SGB2() do { _Pragma("unroll") for (int _i = 0; _i < 15; ++_i) { __builtin_amdgcn_sched_group_barrier(0x8, 1, 0); __builtin_amdgcn_sched_group_barrier(0x100, 1, 0); } \
    _Pragma("unroll") for (int _i = 0; _i < 2; ++_i) { __builtin_amdgcn_sched_group_barrier(0x8, 1, 0); __builtin_amdgcn_sched_group_barrier(0x4, 2, 0); } \
    _Pragma("unroll") for (int _i = 0; _i < 2; ++_i) { __builtin_amdgcn_sched_group_barrier(0x8, 1, 0); __builtin_amdgcn_sched_group_barrier(0x2, 1, 0); } \
    __builtin_amdgcn_sched_group_barrier(0x8, 7, 0); } while (0)

struct Frag4 { bf16x8 w[4][2], o[4][2]; };
#define SGB4() do { _Pragma("unroll") for (int _i = 0; _i < 12; ++_i) { __builtin_amdgcn_sched_group_barrier(0x100, 2, 0); __builtin_amdgcn_sched_group_barrier(0x8, 1, 0); } __builtin_amdgcn_sched_group_barrier(0x8, 4, 0); } while (0)
__device__ __forceinline__ void pf(rsrc_t r, int voff, int soff) {
    asm volatile("s_nop 4\n\tbuffer_load_dword v255, %0, %1, %2 offen" ::"v"(voff), "s"(r), "s"(__builtin_amdgcn_readfirstlane(soff)) : "memory", "v255");
}

__global__ __launch_bounds__(512, 2) void fused(const __bf16* __restrict__ xa, const __bf16* __restrict__ cre, const __bf16* __restrict__ cim,
                                                const __bf16* __restrict__ hre, const __bf16* __restrict__ him, const __bf16* __restrict__ xb,
                                                const __bf16* __restrict__ wqkv, const __bf16* __restrict__ wo,
                                                char* __restrict__ ws, __bf16* __restrict__ out) {
    __shared__ __attribute__((aligned(1024))) char smem_[163840];
    LAS char* smem = (LAS char*)smem_;
    asm volatile("" ::"s"(xa), "s"(cre), "s"(cim), "s"(hre), "s"(him), "s"(xb), "s"(wqkv), "s"(wo), "s"(ws), "s"(out));
    const int tid = threadIdx.x, lane = tid & 63;
    const int wave = __builtin_amdgcn_readfirstlane(tid >> 6);
    const int bid = blockIdx.x, xcd = bid & 7, idx = bid >> 3;
    const int q = xcd & 3, th = xcd >> 2;
    const int ttl = idx >> 2, et = idx & 3, tt = th * 8 + ttl;
    const int t0 = tt * 128, e0 = et * 128;
    const bool typeA = (ttl & 1) == 0;
    const int pg = q;
    const int s1 = typeA ? ((q & 1) * 8 + th * 4 + (ttl >> 1)) * 128 : ((xcd >> 1) * 4 + (ttl >> 1)) * 128;
    const int hg = (xcd & 1) * 4 + et;
    const int s4 = (xcd * 4 + (idx >> 3)) * 64, n4 = (idx & 7) * 64;
    GAS unsigned* flags = (GAS unsigned*)(ws + WS_FLAGS);
    const int slot3 = typeA ? 3 : 0;
#define ROT3 (4 * et)
    const int rot1 = ROT1, rot3 = ROT3;

    if (__builtin_expect(wave >= 6, 1)) {
        const int lw = wave - 6;
        const int row = 8 * lw + (lane >> 3);
        const int ch = ((lane & 7) ^ (4 * lw + (lane >> 4))) * 16;
        const int cht = ((lane & 7) ^ (2 * (((lane >> 4) & 1) | (lw << 1)))) * 16;
        const bool master = (bid < 8 && lw == 0);
        GAS unsigned* myhb = flags + bid * FW;
        if (master && lane < 42) __hip_atomic_store(flags + (8 + bid * 42 + lane) * FW, 0u, __ATOMIC_RELAXED, __HIP_MEMORY_SCOPE_AGENT);
        rsrc_t rc = mk_rsrc((q >> 1) ? cim : cre, SEQ * SEQ * 2);
        const int vb = row * 4096 + ch;
        const int sb = t0 * 4096 + (q & 1) * 2048;
        auto issueC = [&](int st, int slot) {
            const int so = sb + ((st + rot3) & 15) * 128;
#pragma unroll
            for (int u = 0; u < 8; ++u) glds<NT>(rc, smem + slot * SLOT + 16384 + (lw + 2 * u) * 1024, vb, so + 16 * ((u & 4) | ((u + q) & 3)) * 4096);
        };
        rsrc_t rw = mk_rsrc(wo, DM * DM * 2);
        int slot = 0;
        if (typeA) {
            rsrc_t rh = mk_rsrc((pg >> 1) ? him : hre, DM * DM * 2);
            const int vh = row * 1024 + ch;
#pragma unroll 1
            for (int st = 0; st < 8; ++st) {
                if (st == 2) bar_vm<8>();
                if (st == 4) bar_vm<16>();
                if (st >= 6) bar_vm<24>();
                if (master && st >= 2 && st < 7 && !(st & 1)) add_flag_async(myhb, lane);
                const int so = e0 * 1024 + ((st + rot1) & 7) * 128;
#pragma unroll
                for (int u = 0; u < 8; ++u) glds<NT>(rh, smem + slot * SLOT + 16384 + (lw + 2 * u) * 1024, vh, so + 16 * u * 1024);
                slot = (slot + 1 == 5) ? 0 : slot + 1;
            }
        } else {
            rsrc_t rq = mk_rsrc(wqkv, DM * 3 * DM * 2);
            const int vq = row * 3072 + cht;
            int pos = 1;
#pragma unroll 1
            for (int st = 0; st < 8; ++st) {
                if (st >= 2) bar_vm<12>();
                LAS char* sl = smem + T2_BASE + pos * T2_SLOT;
                const int so = ((st + rot1) & 7) * 64 * 3072 + hg * 128;
#pragma unroll
                for (int v = 0; v < 4; ++v)
#pragma unroll
                    for (int cg = 0; cg < 3; ++cg)
                        glds<NT>(rq, sl + 18432 + ((lw + 2 * v) * 3 + cg) * 1024, vq, so + 16 * v * 3072 + cg * 1024);
                pos = (pos + 1 == 3) ? 0 : pos + 1;
            }
        }
#pragma unroll 1
        for (int st = 0; st < 16; ++st) {
            if (st >= 5) {
                if (st == 5 && (!typeA || NE == 5)) { if (typeA) bar_only(); bar_vm<32>(); }
                bar_vm<24>();
            } else if (typeA) {
                if (NE == 5) { if (st < 4) bar_vm<24>(); else bar_only(); }
                else if (st < NE) bar_vm<24>();
                else if (st == NE) {
                    if (NE == 1) { bar_vm<24>(); bar_vm<16>(); bar_vm<8>(); }
                    if (NE == 2) { bar_vm<24>(); bar_vm<16>(); }
                    if (NE == 3) bar_vm<24>();
                    bar_only(); bar_only(); bar_vm<8 * (NE - 1)>();
                }
            } else {
                if (st == 1) bar_vm<20>();
                if (st == 2) bar_vm<16>();
                if (st == 3) bar_only();
            }
            issueC(st, slot);
            slot = (slot + 1 == 5) ? 0 : slot + 1;
            if (st == 7) pf(rw, lane * 128, idx * 16384 + lw * 8192);
        }
        const int vw = row * 1024 + cht;
#pragma unroll 1
        for (int st = 0; st < 4; ++st) {
            bar_vm<24>();
#pragma unroll
            for (int h = 0; h < 2; ++h)
#pragma unroll
                for (int u = 0; u < 4; ++u)
                    glds<NT>(rw, smem + slot * SLOT + h * 16384 + 8192 + (lw + 2 * u) * 1024, vw, ((st * 2 + h) * 64 + 16 * u) * 1024 + n4 * 2);
            slot = (slot + 1 == 5) ? 0 : slot + 1;
        }
        bar_vm<8>(); bar_vm<0>();
    } else if (__builtin_expect(wave >= 4, 1)) {
        const int lw = wave - 4;
        const int ch = ((lane & 7) ^ (4 * lw + (lane >> 4))) * 16;
        const int prow = 8 * (lane >> 5) + ((lane >> 3) & 3) + 16 * lw;
        const int row = 8 * lw + (lane >> 3);
        GAS unsigned* hbp = flags + (lane & 7) * FW;
        if (typeA) {
            rsrc_t rx = mk_rsrc(xa, SEQ * DM * 2);
            const int va = prow * 1024 + ch;
            int sl = 0;
#pragma unroll 1
            for (int st = 0; st < 8; ++st) {
                if (st == 2) bar_vm<8>();
                if (st == 4) bar_vm<16>();
                if (st >= 6) bar_vm<24>();
                if (st == 1 && lw == 0) LD_FLAG_ASYNC("v254", hbp);
                if (st == 7 && lw == 0) LD_FLAG_ASYNC("v253", hbp);
                const int so = s1 * 1024 + ((st + rot1) & 7) * 128;
#pragma unroll
                for (int u = 0; u < 8; ++u) glds<NT>(rx, smem + sl * SLOT + (lw + 2 * u) * 1024, va, so + (32 * (u >> 1) + 4 * (u & 1)) * 1024);
                sl = (sl + 1 == 5) ? 0 : sl + 1;
            }
            bar_vm<24>(); bar_vm<16>(); bar_vm<8>();
        } else {
            rsrc_t rx = mk_rsrc(xb, SEQ * DM * 2);
            const int vx = row * 1024 + ch;
            const int sxm = s1 * 1024, sxh = (s1 >= 16 ? s1 - 16 : 0) * 1024;
            int pos = 1;
#pragma unroll 1
            for (int st = 0; st < 8; ++st) {
                if (st >= 2) bar_vm<9>();
                if (st == 1 && lw == 0) LD_FLAG_ASYNC("v254", hbp);
                if (st == 7 && lw == 0) LD_FLAG_ASYNC("v253", hbp);
                LAS char* sl = smem + T2_BASE + pos * T2_SLOT;
                const int ko = ((st + rot1) & 7) * 128;
                glds<NT>(rx, sl + lw * 1024, vx, sxh + ko);
#pragma unroll
                for (int u = 0; u < 8; ++u) glds<NT>(rx, sl + 2048 + (lw + 2 * u) * 1024, vx, sxm + 16 * u * 1024 + ko);
                pos = (pos + 1 == 3) ? 0 : pos + 1;
            }
            bar_vm<9>();
        }
        bar_vm<0>();
        if (lw == 0) {
            unsigned a, b, spins = 0;
            RD_VEC("v254", a); RD_VEC("v253", b);
            bool moved = (a != b) || ((lane & 7) == bid);
            while (!__all(moved)) {
                if (bid < 8) add_flag(flags + bid * FW, lane);
                __builtin_amdgcn_s_sleep(1);
                b = __hip_atomic_load(hbp, __ATOMIC_RELAXED, __HIP_MEMORY_SCOPE_AGENT);
                moved = moved || (a != b);
                if (++spins > SPIN_CAP) break;
            }
        }
        bar_only();
        rsrc_t ra = mk_rsrc(ws + WS_ABT, 1024 * SEQ * 2), ro = mk_rsrc(ws + WS_O, SEQ * DM * 2);
        const int va = prow * 64 + (ch & 48) + (ch >> 6) * 8192;
        const int sa = (((q >> 1) * 4 + et) * 64 + (q & 1) * 32) * 8192;
        const int vo = row * 32 + (ch & 16) + (ch >> 5) * 65536;
        if (typeA) {
            bar_only();
            if (lw == 0 && lane < 4) (void)__hip_atomic_fetch_add(flags + (F1_IDX + (pg * 4 + et) * 4 + lane) * FW, 1u, __ATOMIC_RELAXED, __HIP_MEMORY_SCOPE_AGENT);
        }
        wait_ge(flags + (F1_IDX + (q * 4 + et) * 4 + lw + 2 * th) * FW, 8u, flags, lane);
        STAMP(6);
        int slot = slot3;
        GAS unsigned* f2 = flags + (F2_IDX + (s4 >> 7)) * FW;
#pragma unroll 1
        for (int st = 0; st < 16; ++st) {
            if (st == 2) bar_vm<8>();
            if (st == 4) bar_vm<16>();
            if (st >= 6) bar_vm<24>();
            if (st == 4 && lw == 1 && !typeA) add_flag_async(flags + (F2_IDX + (s1 >> 7)) * FW, lane);
            const int so = sa + ((st + rot3) & 15) * 16384;
#pragma unroll
            for (int u = 0; u < 8; ++u) glds<16>(ra, smem + slot * SLOT + (lw + 2 * u) * 1024, va, so + (32 * (u >> 1) + 4 * (u & 1)) * 64);
            slot = (slot + 1 == 5) ? 0 : slot + 1;
            if (st == 12) LD_FLAG_ASYNC("v252", f2);
        }
#pragma unroll 1
        for (int st = 0; st < 4; ++st) {
            bar_vm<24>();
            if (st == 0) {
                unsigned f2v;
                RD_FLAG("v252", f2v);
                if (f2v < 8u) wait_ge(f2, 8u, flags, lane);
            }
#pragma unroll
            for (int h = 0; h < 2; ++h)
#pragma unroll
                for (int u = 0; u < 4; ++u)
                    glds<16>(ro, smem + slot * SLOT + h * 16384 + (lw + 2 * u) * 1024, vo, (s4 + 16 * u) * 32 + (st * 2 + h) * 4 * 65536);
            slot = (slot + 1 == 5) ? 0 : slot + 1;
        }
        bar_vm<8>(); bar_vm<0>();
    } else {
        const int r = lane & 15, g = lane >> 4;
        STAMP(8);
        const int tile = tt * 4 + et;
        rsrc_t rp = mk_rsrc(ws + WS_PART, 64 * 16 * 4 * 2048);
        const int inb = (tile * 4 + q) * 32768 + wave * 2048 + lane * 16;
        if (typeA) {
            f32x4 acc[4][4] = {};
            nt_pin(acc);
            bar_lgkm();
            STAMP(1);
            nt_consume<true>(acc, smem, 0, 8, wave, lane);
            STAMP(2);
            const unsigned sg = (pg >> 1) ? 0x80008000u : 0u;
            rsrc_t rs = mk_rsrc(ws + WS_ABT, 1024 * SEQ * 2);
            const int vs = r * 64 + g * 16;
            const int ss = (((pg >> 1) * 4 + et) * 64 + (s1 >> 5) + (wave & 1) * 2) * 8192 + (wave >> 1) * 4096;
#pragma unroll
            for (int n = 0; n < 4; ++n)
#pragma unroll
                for (int u = 0; u < 2; ++u)
                    __builtin_amdgcn_raw_buffer_store_b128(pack8(acc[2 * u][n], acc[2 * u + 1][n]) ^ sg, rs, vs + ss + n * 1024 + u * 8192, 0, 16);
            STAMP(7);
            bar_vm<0>();
            STAMP(3);
        } else {
            const int c0 = ((g ^ (r >> 1)) << 4);
            const int ox = r * 128;
            const int qq = (lane & 15) >> 2, p = lane & 3;
            const int ow = 18432 + g * 3072 + qq * 128 + (((2 * wave + (p >> 1)) ^ (2 * ((qq >> 1) | ((g & 1) << 1)))) << 4) + 8 * (p & 1);
            f32x4 aq[8] = {}, ak[9] = {}, av[9] = {};
            Frag2 f0, f1;
            int so = T2_BASE + T2_SLOT;
            t2_pin(aq, ak, av);
            bar_lgkm();
            STAMP(1);
            t2_read(f0, smem + so + ox + c0, smem + so + ow);
            int px = so + ox + (c0 ^ 64), pw = so + ow;
#pragma unroll 1
            for (int it = 0; it < 7; ++it) {
                __builtin_amdgcn_sched_barrier(0);
                t2_read(f1, smem + px, smem + pw + 12288);
                t2_mma(aq, ak, av, f0);
                so = (so == T2_BASE + 2 * T2_SLOT) ? T2_BASE : so + T2_SLOT;
                int qx = so + ox + c0, qw = so + ow;
                SGB2();
                __builtin_amdgcn_sched_barrier(0);
                asm volatile("" : "+v"(qx), "+v"(qw));
                t2_pin(aq, ak, av);
                bar_cons();
                __builtin_amdgcn_sched_barrier(0);
                t2_read(f0, smem + qx, smem + qw);
                t2_mma(aq, ak, av, f1);
                px = qx ^ 64; pw = qw;
                SGB2();
                __builtin_amdgcn_sched_barrier(0);
                asm volatile("" : "+v"(px));
            }
            t2_read(f1, smem + px, smem + pw + 12288);
            t2_mma(aq, ak, av, f0);
            SGB2();
            __builtin_amdgcn_sched_barrier(0);
            t2_pin(aq, ak, av);
            bar_cons();
            __builtin_amdgcn_sched_barrier(0);
            t2_mma(aq, ak, av, f1);
            STAMP(4);
            const int j = r, par = j & 1;
            const bool uc0 = (4 * g + par) <= j, uc1 = (4 * g + par + 2) <= j;
            const unsigned s0v = par ? 0x01000c0cu : 0x0c0c0100u, s1v = par ? 0x03020c0cu : 0x0c0c0302u, zs = 0x0c0c0c0cu;
            const unsigned selc0 = uc0 ? s0v : zs, selp0 = uc0 ? zs : s0v, selc1 = uc1 ? s1v : zs, selp1 = uc1 ? zs : s1v;
            rsrc_t rs = mk_rsrc(ws + WS_O, SEQ * DM * 2);
            const int vs = j * 32 + g * 8;
            const int ss = (hg * 4 + wave) * 65536 + s1 * 32;
            const f32x4 z = {0.f, 0.f, 0.f, 0.f};
            const float C = 0.25f * 1.44269504088896f;
            u32x2 kb[9], vb[9];
            float l0[8], l1[8], m[8];
#pragma unroll
            for (int i = 0; i < 9; ++i) kb[i] = cvt4u(ak[i]);
#pragma unroll
            for (int b = 0; b < 8; ++b) {
                u32x2 qb = cvt4u(aq[b]);
                f32x4 sc = mfma16(kb[b + 1], qb, z), sp = mfma16(kb[b], qb, z);
                float c0v = par ? sc[1] : sc[0], c1v = par ? sc[3] : sc[2];
                float p0v = par ? sp[1] : sp[0], p1v = par ? sp[3] : sp[2];
                if (b == 0 && s1 == 0) { p0v = -4e9f; p1v = -4e9f; }
                unsigned pk = cvtpk(uc0 ? c0v : p0v, uc1 ? c1v : p1v);
                l0[b] = __uint_as_float(pk << 16); l1[b] = __uint_as_float(pk & 0xffff0000u);
                m[b] = fmx(l0[b], l1[b]);
            }
            allred4<true>(m); allred4<true>(m + 4);
#pragma unroll
            for (int i = 0; i < 9; ++i) vb[i] = cvt4u(av[i]);
#pragma unroll
            for (int b = 0; b < 8; ++b) {
                l0[b] = __builtin_amdgcn_exp2f((l0[b] - m[b]) * C); l1[b] = __builtin_amdgcn_exp2f((l1[b] - m[b]) * C);
                m[b] = l0[b] + l1[b];
            }
            allred4<false>(m); allred4<false>(m + 4);
            f32x4 ov[8];
#pragma unroll
            for (int b = 0; b < 8; ++b) {
                unsigned P = cvtpk(l0[b] * m[b], l1[b] * m[b]);
                u32x2 pc = {__builtin_amdgcn_perm(0u, P, selc0), __builtin_amdgcn_perm(0u, P, selc1)};
                u32x2 pp = {__builtin_amdgcn_perm(0u, P, selp0), __builtin_amdgcn_perm(0u, P, selp1)};
                ov[b] = mfma16(vb[b], pp, mfma16(vb[b + 1], pc, z));
            }
#pragma unroll
            for (int b = 0; b < 8; ++b) __builtin_amdgcn_raw_buffer_store_b64(cvt4u(ov[b]), rs, vs, ss + b * 512, 16);
            STAMP(5);
        }
        f32x4 acc[4][4] = {};
        nt_pin(acc);
        bar_lgkm();
        STAMP(9);
        int slot = nt_consume<false>(acc, smem, slot3, 16, wave, lane);
        STAMP(10);
        u32x4 sv[3][2];
#pragma unroll
        for (int n = 1; n < 4; ++n) { sv[n - 1][0] = pack8(acc[0][n], acc[1][n]); sv[n - 1][1] = pack8(acc[2][n], acc[3][n]); }
        auto send = [&](int n) {
            const int base = (((tile * 4 + ((q + n) & 3)) * 4 + q) * 4 + wave) * 2048 + lane * 16;
            __builtin_amdgcn_raw_buffer_store_b128(sv[n - 1][0], rp, base, 0, 16);
            __builtin_amdgcn_raw_buffer_store_b128(sv[n - 1][1], rp, base + 1024, 0, 16);
        };
#define ILV 1
        send(1);
        if (!ILV) { send(2); send(3); }
        f32x4 own[4];
#pragma unroll
        for (int m = 0; m < 4; ++m) own[m] = acc[m][0];
        f32x4 a4[2][2] = {};
        {
            const int c0 = ((g ^ (r >> 1)) << 4);
            const int oo = ((wave >> 1) * 32 + r) * 128;
            const int qq = (lane & 15) >> 2, p = lane & 3;
            const int swz = 2 * ((qq >> 1) | ((g & 1) << 1));
            const int ow0 = 8192 + g * 1024 + qq * 128 + ((((wave & 1) * 4 + (p >> 1)) ^ swz) << 4) + 8 * (p & 1);
            const int ow1 = ow0 ^ 32;
            Frag4 h0, h1;
            auto rd = [&](Frag4& f) {
                LAS char* sl = smem + slot * SLOT;
#pragma unroll
                for (int i = 0; i < 4; ++i) {
                    const int ho = (i >> 1) * 16384, kk = i & 1;
                    f.w[i][0] = lds_tr2(sl + ho + ow0 + kk * 4096);
                    f.o[i][0] = lds_b128(sl + ho + oo + (c0 ^ (kk * 64)));
                    f.o[i][1] = lds_b128(sl + ho + oo + (c0 ^ (kk * 64)) + 2048);
                    f.w[i][1] = lds_tr2(sl + ho + ow1 + kk * 4096);
                }
                slot = (slot + 1 == 5) ? 0 : slot + 1;
            };
            auto mm = [&](const Frag4& f) {
#pragma unroll
                for (int i = 0; i < 4; ++i) {
                    a4[0][0] = mfma32(f.w[i][0], f.o[i][0], a4[0][0]);
                    a4[0][1] = mfma32(f.w[i][0], f.o[i][1], a4[0][1]);
                    a4[1][0] = mfma32(f.w[i][1], f.o[i][0], a4[1][0]);
                    a4[1][1] = mfma32(f.w[i][1], f.o[i][1], a4[1][1]);
                }
                asm volatile("" : "+v"(a4[0][0]), "+v"(a4[0][1]), "+v"(a4[1][0]), "+v"(a4[1][1]));
            };
            asm volatile("" : "+v"(own[0]), "+v"(own[1]), "+v"(own[2]), "+v"(own[3]));
            bar_only();
            __builtin_amdgcn_sched_barrier(0);
            rd(h0);
            __builtin_amdgcn_sched_barrier(0);
            if (ILV) send(2);
            __builtin_amdgcn_sched_barrier(0);
            rd(h1); mm(h0); SGB4();
            __builtin_amdgcn_sched_barrier(0);
            if (ILV) send(3);
            __builtin_amdgcn_sched_barrier(0);
            rd(h0); mm(h1); SGB4();
            __builtin_amdgcn_sched_barrier(0);
            bar_only();
            __builtin_amdgcn_sched_barrier(0);
            rd(h1); mm(h0); SGB4();
            __builtin_amdgcn_sched_barrier(0);
            mm(h1);
            drain_vm();
            if (lane < 4 && lane != q)
                (void)__hip_atomic_fetch_add(flags + (F3_IDX + tile * 4 + lane) * FW, 1u, __ATOMIC_RELAXED, __HIP_MEMORY_SCOPE_AGENT);
#pragma unroll
            for (int n = 0; n < 2; ++n) {
                u32x2 lo = cvt4u(a4[0][n]), hi = cvt4u(a4[1][n]);
                auto r0 = __builtin_amdgcn_permlane16_swap(lo[0], hi[0], false, false);
                auto r1 = __builtin_amdgcn_permlane16_swap(lo[1], hi[1], false, false);
                u32x4 v = {r0[0], r1[0], r0[1], r1[1]};
                *(u32x4*)(out + (size_t)(s4 + (wave >> 1) * 32 + n * 16 + r) * 1024 + 512 + n4 + (wave & 1) * 32 + (g & 1) * 16 + (g >> 1) * 8) = v;
            }
        }
        STAMP(11);
        wait_ge(flags + (F3_IDX + tile * 4 + q) * FW, 12u, flags, lane);
        STAMP(12);
        u32x4 pv[3][2];
#pragma unroll
        for (int k = 0; k < 3; ++k) {
            const int o = inb + ((q + 1 + k) & 3) * 8192;
            pv[k][0] = __builtin_amdgcn_raw_buffer_load_b128(rp, o, 0, 16);
            pv[k][1] = __builtin_amdgcn_raw_buffer_load_b128(rp, o + 1024, 0, 16);
        }
#pragma unroll
        for (int k = 0; k < 3; ++k) {
            unpack8_add(pv[k][0], own[0], own[1]);
            unpack8_add(pv[k][1], own[2], own[3]);
        }
        const int trow = t0 + (wave >> 1) * 64 + q * 16 + r;
#pragma unroll
        for (int u = 0; u < 2; ++u)
            *(u32x4*)(out + (size_t)trow * 1024 + e0 + (wave & 1) * 64 + 32 * u + 8 * g) = pack8(own[2 * u], own[2 * u + 1]);
    }
}

extern "C" void kernel_launch(void* const* d_in, const int* in_sizes, int n_in,
                              void* d_out, int out_size, void* d_ws, size_t ws_size,
                              hipStream_t stream) {
    fused<<<256, 512, 0, stream>>>((const __bf16*)d_in[0], (const __bf16*)d_in[1], (const __bf16*)d_in[2], (const __bf16*)d_in[3],
                                   (const __bf16*)d_in[4], (const __bf16*)d_in[5], (const __bf16*)d_in[6], (const __bf16*)d_in[7],
                                   (char*)d_ws, (__bf16*)d_out);
}
```

```cpp
#define PFCODE
#define ROT1 (4*(((ttl>>1)+et)&1))
#define SA 10
#define SB 13
#define MULT 2
#define DBID 9
#include <hip/hip_runtime.h>
#include <hip/hip_bf16.h>
#include <stdint.h>

#define LAS __attribute__((address_space(3)))
#define GAS __attribute__((address_space(1)))
typedef __attribute__((ext_vector_type(8))) __bf16 bf16x8;
typedef __attribute__((ext_vector_type(4))) __bf16 bf16x4;
typedef __attribute__((ext_vector_type(2))) __bf16 bf16x2;
typedef __attribute__((ext_vector_type(2))) float f32x2;
typedef __attribute__((ext_vector_type(4))) short s16x4;
typedef __attribute__((ext_vector_type(4))) float f32x4;
typedef __attribute__((ext_vector_type(4))) unsigned u32x4;
typedef __attribute__((ext_vector_type(2))) unsigned u32x2;
typedef __amdgpu_buffer_rsrc_t rsrc_t;

#define STAMPS 1
#define STAMP_N 16
#define STAMP_OFF (64u<<20)
#define STAMP(k) do{ ts[k]=(unsigned)__builtin_amdgcn_s_memrealtime(); }while(0)

constexpr int SEQ = 2048, DM = 512;
constexpr int SLOT = 32768;
constexpr int T2_SLOT = 43008;
constexpr int T2_BASE = 32768;

constexpr unsigned WS_FLAGS = 0;
constexpr unsigned WS_ABT   = 1u << 20;
constexpr unsigned WS_O     = WS_ABT + (4u << 20);
constexpr unsigned WS_PART  = WS_O + (2u << 20);
constexpr int FW = 32;
constexpr int F1_IDX = 8, F2_IDX = 72, F3_IDX = 88, NFLAGS = 88 + 256;
constexpr int NT = 0;
#define NE 3
constexpr unsigned SPIN_CAP = 1u << 17;

__device__ __forceinline__ rsrc_t mk_rsrc(const void* p, unsigned bytes) {
    return __builtin_amdgcn_make_buffer_rsrc((void*)p, 0, bytes, 0x00020000);
}
template <int AUX>
__device__ __forceinline__ void glds(rsrc_t r, LAS char* dst, int voff, int soff) {
    __builtin_amdgcn_raw_ptr_buffer_load_lds(r, (LAS void*)dst, 16, voff, soff, 0, AUX);
}
template <int N> __device__ __forceinline__ void bar_vm() {
    asm volatile("s_waitcnt vmcnt(%0)\n\ts_barrier" ::"n"(N) : "memory");
}
__device__ __forceinline__ void bar_only() { asm volatile("s_barrier" ::: "memory"); }
__device__ __forceinline__ void bar_lgkm() {
    asm volatile("s_waitcnt lgkmcnt(0)\n\ts_barrier" ::: "memory");
}
__device__ __forceinline__ void drain_vm() { asm volatile("s_waitcnt vmcnt(0)" ::: "memory"); }
__device__ __forceinline__ f32x4 mfma32(bf16x8 a, bf16x8 b, f32x4 c) {
    return __builtin_amdgcn_mfma_f32_16x16x32_bf16(a, b, c, 0, 0, 0);
}
__device__ __forceinline__ f32x4 mfma16(u32x2 a, u32x2 b, f32x4 c) {
    return __builtin_amdgcn_mfma_f32_16x16x16bf16_1k(__builtin_bit_cast(s16x4, a), __builtin_bit_cast(s16x4, b), c, 0, 0, 0);
}
__device__ __forceinline__ bf16x8 lds_b128(LAS char* p) { return *(LAS bf16x8*)p; }
__device__ __forceinline__ bf16x8 lds_tr2(LAS char* p) {
    bf16x4 t0 = __builtin_amdgcn_ds_read_tr16_b64_v4bf16((LAS bf16x4*)p);
    bf16x4 t1 = __builtin_amdgcn_ds_read_tr16_b64_v4bf16((LAS bf16x4*)(p + 512));
    return __builtin_shufflevector(t0, t1, 0, 1, 2, 3, 4, 5, 6, 7);
}
__device__ __forceinline__ unsigned cvtpk(float a, float b) {
    f32x2 v = {a, b};
    unsigned u = __builtin_bit_cast(unsigned, __builtin_convertvector(v, bf16x2));
    asm("" : "+v"(u));
    return u;
}
__device__ __forceinline__ u32x2 cvt4u(f32x4 v) { u32x2 r = {cvtpk(v[0], v[1]), cvtpk(v[2], v[3])}; return r; }
__device__ __forceinline__ u32x4 pack8(f32x4 lo, f32x4 hi) {
    u32x4 r = {cvtpk(lo[0], lo[1]), cvtpk(lo[2], lo[3]), cvtpk(hi[0], hi[1]), cvtpk(hi[2], hi[3])};
    return r;
}
__device__ __forceinline__ void unpack8_add(u32x4 v, f32x4& lo, f32x4& hi) {
    unsigned a = v[0], b = v[1], c = v[2], d = v[3];
    lo[0] += __uint_as_float(a << 16); lo[1] += __uint_as_float(a & 0xffff0000u);
    lo[2] += __uint_as_float(b << 16); lo[3] += __uint_as_float(b & 0xffff0000u);
    hi[0] += __uint_as_float(c << 16); hi[1] += __uint_as_float(c & 0xffff0000u);
    hi[2] += __uint_as_float(d << 16); hi[3] += __uint_as_float(d & 0xffff0000u);
}
__device__ __forceinline__ float fmx(float a, float b) { return __builtin_elementwise_maximum(a, b); }
__device__ __forceinline__ void swap16(float& a, float& b) {
    auto r = __builtin_amdgcn_permlane16_swap(__float_as_uint(a), __float_as_uint(b), false, false);
    unsigned x = r[0], y = r[1]; a = __uint_as_float(x); b = __uint_as_float(y);
}
__device__ __forceinline__ void swap32(float& a, float& b) {
    auto r = __builtin_amdgcn_permlane32_swap(__float_as_uint(a), __float_as_uint(b), false, false);
    unsigned x = r[0], y = r[1]; a = __uint_as_float(x); b = __uint_as_float(y);
}
template <bool MAX> __device__ __forceinline__ float op2(float a, float b) { return MAX ? fmx(a, b) : a + b; }
template <bool MAX> __device__ __forceinline__ void allred4(float* v) {
    swap16(v[0], v[1]); float c = op2<MAX>(v[0], v[1]);
    swap16(v[2], v[3]); float d = op2<MAX>(v[2], v[3]);
    swap32(c, d); float e = op2<MAX>(c, d);
    if (!MAX) e = __builtin_amdgcn_rcpf(e);
    float e2 = e; swap32(e, e2);
    float f = e; swap16(e, f);
    float h = e2; swap16(e2, h);
    v[0] = e; v[1] = f; v[2] = e2; v[3] = h;
}
__device__ __forceinline__ unsigned ld_flag(GAS unsigned* f) {
    return __builtin_amdgcn_readfirstlane(__hip_atomic_load(f, __ATOMIC_RELAXED, __HIP_MEMORY_SCOPE_AGENT));
}
__device__ __forceinline__ void add_flag(GAS unsigned* f, int lane) {
    if (lane == 0) (void)__hip_atomic_fetch_add(f, 1u, __ATOMIC_RELAXED, __HIP_MEMORY_SCOPE_AGENT);
}
#define LD_FLAG_ASYNC(REG, f) asm volatile("global_load_dword " REG ", %0, off sc1" ::"v"(f) : "memory", REG)
#define RD_FLAG(REG, out) asm volatile("v_readfirstlane_b32 %0, " REG : "=s"(out)::"memory")
#define RD_VEC(REG, out) asm volatile("v_mov_b32 %0, " REG : "=v"(out)::"memory")
__device__ __forceinline__ void add_flag_async(GAS unsigned* f, int lane) {
    unsigned one = 1u;
    if (lane == 0) asm volatile("global_atomic_add %0, %1, off" ::"v"(f), "v"(one) : "memory");
}
__device__ __forceinline__ void wait_ge(GAS unsigned* f, unsigned target, GAS unsigned* hb, int lane) {
    unsigned spins = 0;
    while (ld_flag(f) < target) {
        ++spins;
        if ((spins & 31u) == 0u) {
            if (lane < 8) (void)__hip_atomic_fetch_add(hb + lane * FW, 1u, __ATOMIC_RELAXED, __HIP_MEMORY_SCOPE_AGENT);
            if (spins > SPIN_CAP) break;
        }
        __builtin_amdgcn_s_sleep(1);
    }
    asm volatile("" ::: "memory");
}
struct Frag { bf16x8 a[4], b[4]; };
#define SGB() do { _Pragma("unroll") for (int _i = 0; _i < 8; ++_i) { __builtin_amdgcn_sched_group_barrier(0x8, 1, 0); __builtin_amdgcn_sched_group_barrier(0x100, 1, 0); } __builtin_amdgcn_sched_group_barrier(0x8, 8, 0); } while (0)
__device__ __forceinline__ void nt_read(Frag& f, LAS char* pa, LAS char* pb) {
    f.a[0] = lds_b128(pa);
#pragma unroll
    for (int n = 0; n < 4; ++n) f.b[n] = lds_b128(pb + n * 2048);
#pragma unroll
    for (int m = 1; m < 4; ++m) f.a[m] = lds_b128(pa + m * 2048);
}
__device__ __forceinline__ void nt_mma(f32x4 (&acc)[4][4], const Frag& f) {
#pragma unroll
    for (int m = 0; m < 4; ++m)
#pragma unroll
        for (int n = 0; n < 4; ++n) acc[m][n] = mfma32(f.a[m], f.b[n], acc[m][n]);
}
__device__ __forceinline__ void bar_cons() {
    asm volatile("" ::: "memory");
    __builtin_amdgcn_s_waitcnt(0x0070);
    __builtin_amdgcn_s_barrier();
    asm volatile("" ::: "memory");
}
#define SGBV() do { _Pragma("unroll") for (int _i = 0; _i < 8; ++_i) { __builtin_amdgcn_sched_group_barrier(0x8, 1, 0); __builtin_amdgcn_sched_group_barrier(0x100, 1, 0); } \
    _Pragma("unroll") for (int _i = 0; _i < 2; ++_i) { __builtin_amdgcn_sched_group_barrier(0x8, 1, 0); __builtin_amdgcn_sched_group_barrier(0x4, 2, 0); } \
    _Pragma("unroll") for (int _i = 0; _i < 2; ++_i) { __builtin_amdgcn_sched_group_barrier(0x8, 1, 0); __builtin_amdgcn_sched_group_barrier(0x2, 1, 0); } \
    __builtin_amdgcn_sched_group_barrier(0x8, 4, 0); } while (0)
__device__ __forceinline__ void nt_pin(f32x4 (&acc)[4][4]) {
    asm volatile("" : "+v"(acc[0][0]), "+v"(acc[0][1]), "+v"(acc[0][2]), "+v"(acc[0][3]), "+v"(acc[1][0]), "+v"(acc[1][1]), "+v"(acc[1][2]), "+v"(acc[1][3]));
    asm volatile("" : "+v"(acc[2][0]), "+v"(acc[2][1]), "+v"(acc[2][2]), "+v"(acc[2][3]), "+v"(acc[3][0]), "+v"(acc[3][1]), "+v"(acc[3][2]), "+v"(acc[3][3]));
}
template <bool LASTBAR>
__device__ __forceinline__ int nt_consume(f32x4 (&acc)[4][4], LAS char* smem, int slot, int nst, int w, int lane) {
    const int r = lane & 15, g = lane >> 4;
    const int c0 = ((g ^ (r >> 1)) << 4), c1 = c0 ^ 64;
    const int oa = ((w & 1) * 64 + r) * 128, ob = 16384 + ((w >> 1) * 64 + r) * 128;
    const int oa0 = oa + c0, ob0 = ob + c0, oa1 = oa + c1, ob1 = ob + c1;
    Frag f0, f1;
    int so = slot * SLOT;
    nt_read(f0, smem + so + oa0, smem + so + ob0);
    int pa = so + oa1, pb = so + ob1;
#pragma unroll 1
    for (int it = 0; it < nst - 1; ++it) {
        __builtin_amdgcn_sched_barrier(0);
        nt_read(f1, smem + pa, smem + pb);
        nt_mma(acc, f0);
        so = (so == 4 * SLOT) ? 0 : so + SLOT;
        int qa = so + oa0, qb = so + ob0;
        SGBV();
        __builtin_amdgcn_sched_barrier(0);
        asm volatile("" : "+v"(qa), "+v"(qb));
        bar_cons();
        __builtin_amdgcn_sched_barrier(0);
        nt_read(f0, smem + qa, smem + qb);
        nt_mma(acc, f1);
        pa = so + oa1; pb = so + ob1;
        SGBV();
        __builtin_amdgcn_sched_barrier(0);
        asm volatile("" : "+v"(pa), "+v"(pb));
    }
    nt_read(f1, smem + pa, smem + pb);
    nt_mma(acc, f0);
    SGB();
    __builtin_amdgcn_sched_barrier(0);
    so = (so == 4 * SLOT) ? 0 : so + SLOT;
    if (LASTBAR) bar_cons();
    __builtin_amdgcn_sched_barrier(0);
    nt_mma(acc, f1);
    return so / SLOT;
}

#define PIN4(a, b, c, d) asm volatile("" : "+v"(a), "+v"(b), "+v"(c), "+v"(d))
__device__ __forceinline__ void t2_pin(f32x4 (&aq)[8], f32x4 (&ak)[9], f32x4 (&av)[9]) {
    asm volatile("" : "+v"(aq[0]), "+v"(aq[1]), "+v"(aq[2]), "+v"(aq[3]), "+v"(aq[4]), "+v"(aq[5]), "+v"(aq[6]), "+v"(aq[7]),
                      "+v"(ak[0]), "+v"(ak[1]), "+v"(ak[2]), "+v"(ak[3]), "+v"(ak[4]));
    asm volatile("" : "+v"(ak[5]), "+v"(ak[6]), "+v"(ak[7]), "+v"(ak[8]),
                      "+v"(av[0]), "+v"(av[1]), "+v"(av[2]), "+v"(av[3]), "+v"(av[4]), "+v"(av[5]), "+v"(av[6]), "+v"(av[7]), "+v"(av[8]));
}
struct Frag2 { bf16x8 x[9], w[3]; };
__device__ __forceinline__ void t2_read(Frag2& f, LAS char* px, LAS char* pw) {
    f.w[1] = lds_tr2(pw + 1024);
    f.x[0] = lds_b128(px);
    f.w[2] = lds_tr2(pw + 2048);
    f.w[0] = lds_tr2(pw);
#pragma unroll
    for (int rb = 1; rb < 9; ++rb) f.x[rb] = lds_b128(px + rb * 2048);
}
__device__ __forceinline__ void t2_mma(f32x4 (&aq)[8], f32x4 (&ak)[9], f32x4 (&av)[9], const Frag2& f) {
#pragma unroll
    for (int rb = 0; rb < 9; ++rb) {
        ak[rb] = mfma32(f.w[1], f.x[rb], ak[rb]);
        av[rb] = mfma32(f.x[rb], f.w[2], av[rb]);
        if (rb > 0) aq[rb - 1] = mfma32(f.w[0], f.x[rb], aq[rb - 1]);
    }
}
#define SGB2() do { _Pragma("unroll") for (int _i = 0; _i < 15; ++_i) { __builtin_amdgcn_sched_group_barrier(0x8, 1, 0); __builtin_amdgcn_sched_group_barrier(0x100, 1, 0); } \
    _Pragma("unroll") for (int _i = 0; _i < 2; ++_i) { __builtin_amdgcn_sched_group_barrier(0x8, 1, 0); __builtin_amdgcn_sched_group_barrier(0x4, 2, 0); } \
    _Pragma("unroll") for (int _i = 0; _i < 2; ++_i) { __builtin_amdgcn_sched_group_barrier(0x8, 1, 0); __builtin_amdgcn_sched_group_barrier(0x2, 1, 0); } \
    __builtin_amdgcn_sched_group_barrier(0x8, 7, 0); } while (0)

struct Frag4 { bf16x8 w[4][2], o[4][2]; };
#define SGB4() do { _Pragma("unroll") for (int _i = 0; _i < 12; ++_i) { __builtin_amdgcn_sched_group_barrier(0x100, 2, 0); __builtin_amdgcn_sched_group_barrier(0x8, 1, 0); } __builtin_amdgcn_sched_group_barrier(0x8, 4, 0); } while (0)
__device__ __forceinline__ void pf(rsrc_t r, int voff, int soff) {
    asm volatile("s_nop 4\n\tbuffer_load_dword v255, %0, %1, %2 offen" ::"v"(voff), "s"(r), "s"(__builtin_amdgcn_readfirstlane(soff)) : "memory", "v255");
}

__global__ __launch_bounds__(512, 2) void fused(const __bf16* __restrict__ xa, const __bf16* __restrict__ cre, const __bf16* __restrict__ cim,
                                                const __bf16* __restrict__ hre, const __bf16* __restrict__ him, const __bf16* __restrict__ xb,
                                                const __bf16* __restrict__ wqkv, const __bf16* __restrict__ wo,
                                                char* __restrict__ ws, __bf16* __restrict__ out) {
    __shared__ __attribute__((aligned(1024))) char smem_[163840];
    LAS char* smem = (LAS char*)smem_;
    unsigned long long tent = __builtin_amdgcn_s_memrealtime();
    unsigned ts[16] = {};
    asm volatile("" ::"s"(xa), "s"(cre), "s"(cim), "s"(hre), "s"(him), "s"(xb), "s"(wqkv), "s"(wo), "s"(ws), "s"(out));
    const int tid = threadIdx.x, lane = tid & 63;
    const int wave = __builtin_amdgcn_readfirstlane(tid >> 6);
    const int bid = blockIdx.x, xcd = bid & 7, idx = bid >> 3;
    const int q = xcd & 3, th = xcd >> 2;
    const int ttl = idx >> 2, et = idx & 3, tt = th * 8 + ttl;
    const int t0 = tt * 128, e0 = et * 128;
    const bool typeA = (ttl & 1) == 0;
    const int pg = q;
    const int s1 = typeA ? ((q & 1) * 8 + th * 4 + (ttl >> 1)) * 128 : ((xcd >> 1) * 4 + (ttl >> 1)) * 128;
    const int hg = (xcd & 1) * 4 + et;
    const int s4 = (xcd * 4 + (idx >> 3)) * 64, n4 = (idx & 7) * 64;
    GAS unsigned* flags = (GAS unsigned*)(ws + WS_FLAGS);
    const int slot3 = typeA ? 3 : 0;
#define ROT3 (4 * et)
    const int rot1 = ROT1, rot3 = ROT3;

    if (__builtin_expect(wave >= 6, 1)) {
        const int lw = wave - 6;
        const int row = 8 * lw + (lane >> 3);
        const int ch = ((lane & 7) ^ (4 * lw + (lane >> 4))) * 16;
        const int cht = ((lane & 7) ^ (2 * (((lane >> 4) & 1) | (lw << 1)))) * 16;
        const bool master = (bid < 8 && lw == 0);
        GAS unsigned* myhb = flags + bid * FW;
        if (master && lane < 42) __hip_atomic_store(flags + (8 + bid * 42 + lane) * FW, 0u, __ATOMIC_RELAXED, __HIP_MEMORY_SCOPE_AGENT);
        rsrc_t rc = mk_rsrc((q >> 1) ? cim : cre, SEQ * SEQ * 2);
        const int vb = row * 4096 + ch;
        const int sb = t0 * 4096 + (q & 1) * 2048;
        auto issueC = [&](int st, int slot) {
            const int so = sb + ((st + rot3) & 15) * 128;
#pragma unroll
            for (int u = 0; u < 8; ++u) glds<NT>(rc, smem + slot * SLOT + 16384 + (lw + 2 * u) * 1024, vb, so + 16 * ((u & 4) | ((u + q) & 3)) * 4096);
        };
        rsrc_t rw = mk_rsrc(wo, DM * DM * 2);
        int slot = 0;
        if (typeA) {
            rsrc_t rh = mk_rsrc((pg >> 1) ? him : hre, DM * DM * 2);
            const int vh = row * 1024 + ch;
#pragma unroll 1
            for (int st = 0; st < 8; ++st) {
                if (st == 2) bar_vm<8>();
                if (st == 4) bar_vm<16>();
                if (st >= 6) bar_vm<24>();
                if (master && st >= 2 && st < 7 && !(st & 1)) add_flag_async(myhb, lane);
                const int so = e0 * 1024 + ((st + rot1) & 7) * 128;
#pragma unroll
                for (int u = 0; u < 8; ++u) glds<NT>(rh, smem + slot * SLOT + 16384 + (lw + 2 * u) * 1024, vh, so + 16 * u * 1024);
                slot = (slot + 1 == 5) ? 0 : slot + 1;
            }
        } else {
            rsrc_t rq = mk_rsrc(wqkv, DM * 3 * DM * 2);
            const int vq = row * 3072 + cht;
            int pos = 1;
#pragma unroll 1
            for (int st = 0; st < 8; ++st) {
                if (st >= 2) bar_vm<12>();
                LAS char* sl = smem + T2_BASE + pos * T2_SLOT;
                const int so = ((st + rot1) & 7) * 64 * 3072 + hg * 128;
#pragma unroll
                for (int v = 0; v < 4; ++v)
#pragma unroll
                    for (int cg = 0; cg < 3; ++cg)
                        glds<NT>(rq, sl + 18432 + ((lw + 2 * v) * 3 + cg) * 1024, vq, so + 16 * v * 3072 + cg * 1024);
                pos = (pos + 1 == 3) ? 0 : pos + 1;
            }
        }
#pragma unroll 1
        for (int st = 0; st < 16; ++st) {
            if (st >= 5) {
                if (st == 5 && (!typeA || NE == 5)) { if (typeA) bar_only(); bar_vm<32>(); }
                bar_vm<24>();
            } else if (typeA) {
                if (NE == 5) { if (st < 4) bar_vm<24>(); else bar_only(); }
                else if (st < NE) bar_vm<24>();
                else if (st == NE) {
                    if (NE == 1) { bar_vm<24>(); bar_vm<16>(); bar_vm<8>(); }
                    if (NE == 2) { bar_vm<24>(); bar_vm<16>(); }
                    if (NE == 3) bar_vm<24>();
                    bar_only(); bar_only(); bar_vm<8 * (NE - 1)>();
                }
            } else {
                if (st == 1) bar_vm<20>();
                if (st == 2) bar_vm<16>();
                if (st == 3) bar_only();
            }
            issueC(st, slot);
            slot = (slot + 1 == 5) ? 0 : slot + 1;
            if (st == 7) pf(rw, lane * 128, idx * 16384 + lw * 8192);
        }
        const int vw = row * 1024 + cht;
#pragma unroll 1
        for (int st = 0; st < 4; ++st) {
            bar_vm<24>();
#pragma unroll
            for (int h = 0; h < 2; ++h)
#pragma unroll
                for (int u = 0; u < 4; ++u)
                    glds<NT>(rw, smem + slot * SLOT + h * 16384 + 8192 + (lw + 2 * u) * 1024, vw, ((st * 2 + h) * 64 + 16 * u) * 1024 + n4 * 2);
            slot = (slot + 1 == 5) ? 0 : slot + 1;
        }
        bar_vm<8>(); bar_vm<0>();
    } else if (__builtin_expect(wave >= 4, 1)) {
        const int lw = wave - 4;
        const int ch = ((lane & 7) ^ (4 * lw + (lane >> 4))) * 16;
        const int prow = 8 * (lane >> 5) + ((lane >> 3) & 3) + 16 * lw;
        const int row = 8 * lw + (lane >> 3);
        GAS unsigned* hbp = flags + (lane & 7) * FW;
        if (typeA) {
            rsrc_t rx = mk_rsrc(xa, SEQ * DM * 2);
            const int va = prow * 1024 + ch;
            int sl = 0;
#pragma unroll 1
            for (int st = 0; st < 8; ++st) {
                if (st == 2) bar_vm<8>();
                if (st == 4) bar_vm<16>();
                if (st >= 6) bar_vm<24>();
                if (st == 1 && lw == 0) LD_FLAG_ASYNC("v254", hbp);
                if (st == 7 && lw == 0) LD_FLAG_ASYNC("v253", hbp);
                const int so = s1 * 1024 + ((st + rot1) & 7) * 128;
#pragma unroll
                for (int u = 0; u < 8; ++u) glds<NT>(rx, smem + sl * SLOT + (lw + 2 * u) * 1024, va, so + (32 * (u >> 1) + 4 * (u & 1)) * 1024);
                sl = (sl + 1 == 5) ? 0 : sl + 1;
            }
            bar_vm<24>(); bar_vm<16>(); bar_vm<8>();
        } else {
            rsrc_t rx = mk_rsrc(xb, SEQ * DM * 2);
            const int vx = row * 1024 + ch;
            const int sxm = s1 * 1024, sxh = (s1 >= 16 ? s1 - 16 : 0) * 1024;
            int pos = 1;
#pragma unroll 1
            for (int st = 0; st < 8; ++st) {
                if (st >= 2) bar_vm<9>();
                if (st == 1 && lw == 0) LD_FLAG_ASYNC("v254", hbp);
                if (st == 7 && lw == 0) LD_FLAG_ASYNC("v253", hbp);
                LAS char* sl = smem + T2_BASE + pos * T2_SLOT;
                const int ko = ((st + rot1) & 7) * 128;
                glds<NT>(rx, sl + lw * 1024, vx, sxh + ko);
#pragma unroll
                for (int u = 0; u < 8; ++u) glds<NT>(rx, sl + 2048 + (lw + 2 * u) * 1024, vx, sxm + 16 * u * 1024 + ko);
                pos = (pos + 1 == 3) ? 0 : pos + 1;
            }
            bar_vm<9>();
        }
        bar_vm<0>();
        if (lw == 0) {
            unsigned a, b, spins = 0;
            RD_VEC("v254", a); RD_VEC("v253", b);
            bool moved = (a != b) || ((lane & 7) == bid);
            while (!__all(moved)) {
                if (bid < 8) add_flag(flags + bid * FW, lane);
                __builtin_amdgcn_s_sleep(1);
                b = __hip_atomic_load(hbp, __ATOMIC_RELAXED, __HIP_MEMORY_SCOPE_AGENT);
                moved = moved || (a != b);
                if (++spins > SPIN_CAP) break;
            }
        }
        bar_only();
        rsrc_t ra = mk_rsrc(ws + WS_ABT, 1024 * SEQ * 2), ro = mk_rsrc(ws + WS_O, SEQ * DM * 2);
        const int va = prow * 64 + (ch & 48) + (ch >> 6) * 8192;
        const int sa = (((q >> 1) * 4 + et) * 64 + (q & 1) * 32) * 8192;
        const int vo = row * 32 + (ch & 16) + (ch >> 5) * 65536;
        if (typeA) {
            bar_only();
            if (lw == 0 && lane < 4) (void)__hip_atomic_fetch_add(flags + (F1_IDX + (pg * 4 + et) * 4 + lane) * FW, 1u, __ATOMIC_RELAXED, __HIP_MEMORY_SCOPE_AGENT);
        }
        wait_ge(flags + (F1_IDX + (q * 4 + et) * 4 + lw + 2 * th) * FW, 8u, flags, lane);
        STAMP(6);
        int slot = slot3;
        GAS unsigned* f2 = flags + (F2_IDX + (s4 >> 7)) * FW;
#pragma unroll 1
        for (int st = 0; st < 16; ++st) {
            if (st == 2) bar_vm<8>();
            if (st == 4) bar_vm<16>();
            if (st >= 6) bar_vm<24>();
            if (st == 4 && lw == 1 && !typeA) add_flag_async(flags + (F2_IDX + (s1 >> 7)) * FW, lane);
            const int so = sa + ((st + rot3) & 15) * 16384;
#pragma unroll
            for (int u = 0; u < 8; ++u) glds<16>(ra, smem + slot * SLOT + (lw + 2 * u) * 1024, va, so + (32 * (u >> 1) + 4 * (u & 1)) * 64);
            slot = (slot + 1 == 5) ? 0 : slot + 1;
            if (st == 12) LD_FLAG_ASYNC("v252", f2);
        }
#pragma unroll 1
        for (int st = 0; st < 4; ++st) {
            bar_vm<24>();
            if (st == 0) {
                unsigned f2v;
                RD_FLAG("v252", f2v);
                if (f2v < 8u) wait_ge(f2, 8u, flags, lane);
            }
#pragma unroll
            for (int h = 0; h < 2; ++h)
#pragma unroll
                for (int u = 0; u < 4; ++u)
                    glds<16>(ro, smem + slot * SLOT + h * 16384 + (lw + 2 * u) * 1024, vo, (s4 + 16 * u) * 32 + (st * 2 + h) * 4 * 65536);
            slot = (slot + 1 == 5) ? 0 : slot + 1;
        }
        bar_vm<8>(); bar_vm<0>();
    } else {
        const int r = lane & 15, g = lane >> 4;
        STAMP(8);
        if (wave == 1 && ttl == 1) {
            unsigned long long pc; int off, len;
            asm volatile("s_getpc_b64 %0\n.Lpf_here:\n\ts_mov_b32 %1, .Lpf_here-_Z5fusedPKDF16bS0_S0_S0_S0_S0_S0_S0_PcPDF16b\n\ts_mov_b32 %2, _Z8code_endPj-_Z5fusedPKDF16bS0_S0_S0_S0_S0_S0_S0_PcPDF16b" : "=s"(pc), "=s"(off), "=s"(len));
            const int o = (et * 64 + lane) * 128;
            if (o < len) { unsigned v = *(const unsigned*)(pc - (unsigned)off + o); asm volatile("" ::"v"(v)); }
        }
        const int tile = tt * 4 + et;
        rsrc_t rp = mk_rsrc(ws + WS_PART, 64 * 16 * 4 * 2048);
        const int inb = (tile * 4 + q) * 32768 + wave * 2048 + lane * 16;
        if (typeA) {
            f32x4 acc[4][4] = {};
            nt_pin(acc);
            bar_lgkm();
            STAMP(1);
            nt_consume<true>(acc, smem, 0, 8, wave, lane);
            STAMP(2);
            const unsigned sg = (pg >> 1) ? 0x80008000u : 0u;
            rsrc_t rs = mk_rsrc(ws + WS_ABT, 1024 * SEQ * 2);
            const int vs = r * 64 + g * 16;
            const int ss = (((pg >> 1) * 4 + et) * 64 + (s1 >> 5) + (wave & 1) * 2) * 8192 + (wave >> 1) * 4096;
#pragma unroll
            for (int n = 0; n < 4; ++n)
#pragma unroll
                for (int u = 0; u < 2; ++u)
                    __builtin_amdgcn_raw_buffer_store_b128(pack8(acc[2 * u][n], acc[2 * u + 1][n]) ^ sg, rs, vs + ss + n * 1024 + u * 8192, 0, 16);
            STAMP(7);
            bar_vm<0>();
            STAMP(3);
        } else {
            const int c0 = ((g ^ (r >> 1)) << 4);
            const int ox = r * 128;
            const int qq = (lane & 15) >> 2, p = lane & 3;
            const int ow = 18432 + g * 3072 + qq * 128 + (((2 * wave + (p >> 1)) ^ (2 * ((qq >> 1) | ((g & 1) << 1)))) << 4) + 8 * (p & 1);
            f32x4 aq[8] = {}, ak[9] = {}, av[9] = {};
            Frag2 f0, f1;
            int so = T2_BASE + T2_SLOT;
            t2_pin(aq, ak, av);
            bar_lgkm();
            STAMP(1);
            t2_read(f0, smem + so + ox + c0, smem + so + ow);
            int px = so + ox + (c0 ^ 64), pw = so + ow;
#pragma unroll 1
            for (int it = 0; it < 7; ++it) {
                __builtin_amdgcn_sched_barrier(0);
                t2_read(f1, smem + px, smem + pw + 12288);
                t2_mma(aq, ak, av, f0);
                so = (so == T2_BASE + 2 * T2_SLOT) ? T2_BASE : so + T2_SLOT;
                int qx = so + ox + c0, qw = so + ow;
                SGB2();
                __builtin_amdgcn_sched_barrier(0);
                asm volatile("" : "+v"(qx), "+v"(qw));
                t2_pin(aq, ak, av);
                bar_cons();
                __builtin_amdgcn_sched_barrier(0);
                t2_read(f0, smem + qx, smem + qw);
                t2_mma(aq, ak, av, f1);
                px = qx ^ 64; pw = qw;
                SGB2();
                __builtin_amdgcn_sched_barrier(0);
                asm volatile("" : "+v"(px));
            }
            t2_read(f1, smem + px, smem + pw + 12288);
            t2_mma(aq, ak, av, f0);
            SGB2();
            __builtin_amdgcn_sched_barrier(0);
            t2_pin(aq, ak, av);
            bar_cons();
            __builtin_amdgcn_sched_barrier(0);
            t2_mma(aq, ak, av, f1);
            STAMP(4);
            const int j = r, par = j & 1;
            const bool uc0 = (4 * g + par) <= j, uc1 = (4 * g + par + 2) <= j;
            const unsigned s0v = par ? 0x01000c0cu : 0x0c0c0100u, s1v = par ? 0x03020c0cu : 0x0c0c0302u, zs = 0x0c0c0c0cu;
            const unsigned selc0 = uc0 ? s0v : zs, selp0 = uc0 ? zs : s0v, selc1 = uc1 ? s1v : zs, selp1 = uc1 ? zs : s1v;
            rsrc_t rs = mk_rsrc(ws + WS_O, SEQ * DM * 2);
            const int vs = j * 32 + g * 8;
            const int ss = (hg * 4 + wave) * 65536 + s1 * 32;
            const f32x4 z = {0.f, 0.f, 0.f, 0.f};
            const float C = 0.25f * 1.44269504088896f;
            u32x2 kb[9], vb[9];
            float l0[8], l1[8], m[8];
#pragma unroll
            for (int i = 0; i < 9; ++i) kb[i] = cvt4u(ak[i]);
#pragma unroll
            for (int b = 0; b < 8; ++b) {
                u32x2 qb = cvt4u(aq[b]);
                f32x4 sc = mfma16(kb[b + 1], qb, z), sp = mfma16(kb[b], qb, z);
                float c0v = par ? sc[1] : sc[0], c1v = par ? sc[3] : sc[2];
                float p0v = par ? sp[1] : sp[0], p1v = par ? sp[3] : sp[2];
                if (b == 0 && s1 == 0) { p0v = -4e9f; p1v = -4e9f; }
                unsigned pk = cvtpk(uc0 ? c0v : p0v, uc1 ? c1v : p1v);
                l0[b] = __uint_as_float(pk << 16); l1[b] = __uint_as_float(pk & 0xffff0000u);
                m[b] = fmx(l0[b], l1[b]);
            }
            allred4<true>(m); allred4<true>(m + 4);
#pragma unroll
            for (int i = 0; i < 9; ++i) vb[i] = cvt4u(av[i]);
#pragma unroll
            for (int b = 0; b < 8; ++b) {
                l0[b] = __builtin_amdgcn_exp2f((l0[b] - m[b]) * C); l1[b] = __builtin_amdgcn_exp2f((l1[b] - m[b]) * C);
                m[b] = l0[b] + l1[b];
            }
            allred4<false>(m); allred4<false>(m + 4);
            f32x4 ov[8];
#pragma unroll
            for (int b = 0; b < 8; ++b) {
                unsigned P = cvtpk(l0[b] * m[b], l1[b] * m[b]);
                u32x2 pc = {__builtin_amdgcn_perm(0u, P, selc0), __builtin_amdgcn_perm(0u, P, selc1)};
                u32x2 pp = {__builtin_amdgcn_perm(0u, P, selp0), __builtin_amdgcn_perm(0u, P, selp1)};
                ov[b] = mfma16(vb[b], pp, mfma16(vb[b + 1], pc, z));
            }
#pragma unroll
            for (int b = 0; b < 8; ++b) __builtin_amdgcn_raw_buffer_store_b64(cvt4u(ov[b]), rs, vs, ss + b * 512, 16);
            STAMP(5);
        }
        f32x4 acc[4][4] = {};
        nt_pin(acc);
        bar_lgkm();
        STAMP(9);
        unsigned long long cy0 = __builtin_amdgcn_s_memtime();
        int slot = nt_consume<false>(acc, smem, slot3, 16, wave, lane);
        STAMP(10);
        unsigned long long cy1 = __builtin_amdgcn_s_memtime();
        u32x4 sv[3][2];
#pragma unroll
        for (int n = 1; n < 4; ++n) { sv[n - 1][0] = pack8(acc[0][n], acc[1][n]); sv[n - 1][1] = pack8(acc[2][n], acc[3][n]); }
        auto send = [&](int n) {
            const int base = (((tile * 4 + ((q + n) & 3)) * 4 + q) * 4 + wave) * 2048 + lane * 16;
            __builtin_amdgcn_raw_buffer_store_b128(sv[n - 1][0], rp, base, 0, 16);
            __builtin_amdgcn_raw_buffer_store_b128(sv[n - 1][1], rp, base + 1024, 0, 16);
        };
#define ILV 1
        send(1);
        if (!ILV) { send(2); send(3); }
        f32x4 own[4];
#pragma unroll
        for (int m = 0; m < 4; ++m) own[m] = acc[m][0];
        f32x4 a4[2][2] = {};
        {
            const int c0 = ((g ^ (r >> 1)) << 4);
            const int oo = ((wave >> 1) * 32 + r) * 128;
            const int qq = (lane & 15) >> 2, p = lane & 3;
            const int swz = 2 * ((qq >> 1) | ((g & 1) << 1));
            const int ow0 = 8192 + g * 1024 + qq * 128 + ((((wave & 1) * 4 + (p >> 1)) ^ swz) << 4) + 8 * (p & 1);
            const int ow1 = ow0 ^ 32;
            Frag4 h0, h1;
            auto rd = [&](Frag4& f) {
                LAS char* sl = smem + slot * SLOT;
#pragma unroll
                for (int i = 0; i < 4; ++i) {
                    const int ho = (i >> 1) * 16384, kk = i & 1;
                    f.w[i][0] = lds_tr2(sl + ho + ow0 + kk * 4096);
                    f.o[i][0] = lds_b128(sl + ho + oo + (c0 ^ (kk * 64)));
                    f.o[i][1] = lds_b128(sl + ho + oo + (c0 ^ (kk * 64)) + 2048);
                    f.w[i][1] = lds_tr2(sl + ho + ow1 + kk * 4096);
                }
                slot = (slot + 1 == 5) ? 0 : slot + 1;
            };
            auto mm = [&](const Frag4& f) {
#pragma unroll
                for (int i = 0; i < 4; ++i) {
                    a4[0][0] = mfma32(f.w[i][0], f.o[i][0], a4[0][0]);
                    a4[0][1] = mfma32(f.w[i][0], f.o[i][1], a4[0][1]);
                    a4[1][0] = mfma32(f.w[i][1], f.o[i][0], a4[1][0]);
                    a4[1][1] = mfma32(f.w[i][1], f.o[i][1], a4[1][1]);
                }
                asm volatile("" : "+v"(a4[0][0]), "+v"(a4[0][1]), "+v"(a4[1][0]), "+v"(a4[1][1]));
            };
            asm volatile("" : "+v"(own[0]), "+v"(own[1]), "+v"(own[2]), "+v"(own[3]));
            bar_only();
            __builtin_amdgcn_sched_barrier(0);
            rd(h0);
            __builtin_amdgcn_sched_barrier(0);
            if (ILV) send(2);
            __builtin_amdgcn_sched_barrier(0);
            rd(h1); mm(h0); SGB4();
            __builtin_amdgcn_sched_barrier(0);
            if (ILV) send(3);
            __builtin_amdgcn_sched_barrier(0);
            rd(h0); mm(h1); SGB4();
            __builtin_amdgcn_sched_barrier(0);
            bar_only();
            __builtin_amdgcn_sched_barrier(0);
            rd(h1); mm(h0); SGB4();
            __builtin_amdgcn_sched_barrier(0);
            mm(h1);
            drain_vm();
            if (lane < 4 && lane != q)
                (void)__hip_atomic_fetch_add(flags + (F3_IDX + tile * 4 + lane) * FW, 1u, __ATOMIC_RELAXED, __HIP_MEMORY_SCOPE_AGENT);
#pragma unroll
            for (int n = 0; n < 2; ++n) {
                u32x2 lo = cvt4u(a4[0][n]), hi = cvt4u(a4[1][n]);
                auto r0 = __builtin_amdgcn_permlane16_swap(lo[0], hi[0], false, false);
                auto r1 = __builtin_amdgcn_permlane16_swap(lo[1], hi[1], false, false);
                u32x4 v = {r0[0], r1[0], r0[1], r1[1]};
                *(u32x4*)(out + (size_t)(s4 + (wave >> 1) * 32 + n * 16 + r) * 1024 + 512 + n4 + (wave & 1) * 32 + (g & 1) * 16 + (g >> 1) * 8) = v;
            }
        }
        STAMP(11);
        wait_ge(flags + (F3_IDX + tile * 4 + q) * FW, 12u, flags, lane);
        STAMP(12);
        u32x4 pv[3][2];
#pragma unroll
        for (int k = 0; k < 3; ++k) {
            const int o = inb + ((q + 1 + k) & 3) * 8192;
            pv[k][0] = __builtin_amdgcn_raw_buffer_load_b128(rp, o, 0, 16);
            pv[k][1] = __builtin_amdgcn_raw_buffer_load_b128(rp, o + 1024, 0, 16);
        }
#pragma unroll
        for (int k = 0; k < 3; ++k) {
            unpack8_add(pv[k][0], own[0], own[1]);
            unpack8_add(pv[k][1], own[2], own[3]);
        }
        const int trow = t0 + (wave >> 1) * 64 + q * 16 + r;
#pragma unroll
        for (int u = 0; u < 2; ++u)
            *(u32x4*)(out + (size_t)trow * 1024 + e0 + (wave & 1) * 64 + 32 * u + 8 * g) = pack8(own[2 * u], own[2 * u + 1]);
        drain_vm();
        STAMP(13);
        ts[0] = (unsigned)tent;
        if (wave == 0 && bid == DBID && MULT > 0) {
            unsigned long long d = (unsigned long long)(ts[SB] - ts[SA]) * MULT, t = __builtin_amdgcn_s_memrealtime();
            if (d > 20000) d = 20000;
            while (__builtin_amdgcn_s_memrealtime() - t < d) __builtin_amdgcn_s_sleep(2);
        }
    }
}

__global__ void code_end(unsigned* p) { p[0] = 0; }

extern "C" void kernel_launch(void* const* d_in, const int* in_sizes, int n_in,
                              void* d_out, int out_size, void* d_ws, size_t ws_size,
                              hipStream_t stream) {
    fused<<<256, 512, 0, stream>>>((const __bf16*)d_in[0], (const __bf16*)d_in[1], (const __bf16*)d_in[2], (const __bf16*)d_in[3],
                                   (const __bf16*)d_in[4], (const __bf16*)d_in[5], (const __bf16*)d_in[6], (const __bf16*)d_in[7],
                                   (char*)d_ws, (__bf16*)d_out);
}
```

```cpp
#include <hip/hip_runtime.h>
#include <hip/hip_bf16.h>
#include <stdint.h>

#define LAS __attribute__((address_space(3)))
#define GAS __attribute__((address_space(1)))
typedef __attribute__((ext_vector_type(8))) __bf16 bf16x8;
typedef __attribute__((ext_vector_type(4))) __bf16 bf16x4;
typedef __attribute__((ext_vector_type(2))) __bf16 bf16x2;
typedef __attribute__((ext_vector_type(2))) float f32x2;
typedef __attribute__((ext_vector_type(4))) short s16x4;
typedef __attribute__((ext_vector_type(4))) float f32x4;
typedef __attribute__((ext_vector_type(4))) unsigned u32x4;
typedef __attribute__((ext_vector_type(2))) unsigned u32x2;
typedef __amdgpu_buffer_rsrc_t rsrc_t;

#define NOSTAMPS 1
#define STAMP_N 16
#define STAMP_OFF (64u<<20)
#define STAMP(k) do{}while(0)

constexpr int SEQ = 2048, DM = 512;
constexpr int SLOT = 32768;
constexpr int T2_SLOT = 43008;
constexpr int T2_BASE = 32768;

constexpr unsigned WS_FLAGS = 0;
constexpr unsigned WS_ABT   = 1u << 20;
constexpr unsigned WS_O     = WS_ABT + (4u << 20);
constexpr unsigned WS_PART  = WS_O + (2u << 20);
constexpr int FW = 32;
constexpr int F1_IDX = 8, F2_IDX = 72, F3_IDX = 88, NFLAGS = 88 + 256;
constexpr int NT = 0;
#define NE 3
constexpr unsigned SPIN_CAP = 1u << 17;

__device__ __forceinline__ rsrc_t mk_rsrc(const void* p, unsigned bytes) {
    return __builtin_amdgcn_make_buffer_rsrc((void*)p, 0, bytes, 0x00020000);
}
template <int AUX>
__device__ __forceinline__ void glds(rsrc_t r, LAS char* dst, int voff, int soff) {
    __builtin_amdgcn_raw_ptr_buffer_load_lds(r, (LAS void*)dst, 16, voff, soff, 0, AUX);
}
template <int N> __device__ __forceinline__ void bar_vm() {
    asm volatile("s_waitcnt vmcnt(%0)\n\ts_barrier" ::"n"(N) : "memory");
}
__device__ __forceinline__ void bar_only() { asm volatile("s_barrier" ::: "memory"); }
__device__ __forceinline__ void bar_lgkm() {
    asm volatile("s_waitcnt lgkmcnt(0)\n\ts_barrier" ::: "memory");
}
__device__ __forceinline__ void drain_vm() { asm volatile("s_waitcnt vmcnt(0)" ::: "memory"); }
__device__ __forceinline__ f32x4 mfma32(bf16x8 a, bf16x8 b, f32x4 c) {
    return __builtin_amdgcn_mfma_f32_16x16x32_bf16(a, b, c, 0, 0, 0);
}
__device__ __forceinline__ f32x4 mfma16(u32x2 a, u32x2 b, f32x4 c) {
    return __builtin_amdgcn_mfma_f32_16x16x16bf16_1k(__builtin_bit_cast(s16x4, a), __builtin_bit_cast(s16x4, b), c, 0, 0, 0);
}
__device__ __forceinline__ bf16x8 lds_b128(LAS char* p) { return *(LAS bf16x8*)p; }
__device__ __forceinline__ bf16x8 lds_tr2(LAS char* p) {
    bf16x4 t0 = __builtin_amdgcn_ds_read_tr16_b64_v4bf16((LAS bf16x4*)p);
    bf16x4 t1 = __builtin_amdgcn_ds_read_tr16_b64_v4bf16((LAS bf16x4*)(p + 512));
    return __builtin_shufflevector(t0, t1, 0, 1, 2, 3, 4, 5, 6, 7);
}
__device__ __forceinline__ unsigned cvtpk(float a, float b) {
    f32x2 v = {a, b};
    unsigned u = __builtin_bit_cast(unsigned, __builtin_convertvector(v, bf16x2));
    asm("" : "+v"(u));
    return u;
}
__device__ __forceinline__ u32x2 cvt4u(f32x4 v) { u32x2 r = {cvtpk(v[0], v[1]), cvtpk(v[2], v[3])}; return r; }
__device__ __forceinline__ u32x4 pack8(f32x4 lo, f32x4 hi) {
    u32x4 r = {cvtpk(lo[0], lo[1]), cvtpk(lo[2], lo[3]), cvtpk(hi[0], hi[1]), cvtpk(hi[2], hi[3])};
    return r;
}
__device__ __forceinline__ void unpack8_add(u32x4 v, f32x4& lo, f32x4& hi) {
    unsigned a = v[0], b = v[1], c = v[2], d = v[3];
    lo[0] += __uint_as_float(a << 16); lo[1] += __uint_as_float(a & 0xffff0000u);
    lo[2] += __uint_as_float(b << 16); lo[3] += __uint_as_float(b & 0xffff0000u);
    hi[0] += __uint_as_float(c << 16); hi[1] += __uint_as_float(c & 0xffff0000u);
    hi[2] += __uint_as_float(d << 16); hi[3] += __uint_as_float(d & 0xffff0000u);
}
__device__ __forceinline__ float fmx(float a, float b) { return __builtin_elementwise_maximum(a, b); }
__device__ __forceinline__ void swap16(float& a, float& b) {
    auto r = __builtin_amdgcn_permlane16_swap(__float_as_uint(a), __float_as_uint(b), false, false);
    unsigned x = r[0], y = r[1]; a = __uint_as_float(x); b = __uint_as_float(y);
}
__device__ __forceinline__ void swap32(float& a, float& b) {
    auto r = __builtin_amdgcn_permlane32_swap(__float_as_uint(a), __float_as_uint(b), false, false);
    unsigned x = r[0], y = r[1]; a = __uint_as_float(x); b = __uint_as_float(y);
}
template <bool MAX> __device__ __forceinline__ float op2(float a, float b) { return MAX ? fmx(a, b) : a + b; }
template <bool MAX> __device__ __forceinline__ void allred4(float* v) {
    swap16(v[0], v[1]); float c = op2<MAX>(v[0], v[1]);
    swap16(v[2], v[3]); float d = op2<MAX>(v[2], v[3]);
    swap32(c, d); float e = op2<MAX>(c, d);
    if (!MAX) e = __builtin_amdgcn_rcpf(e);
    float e2 = e; swap32(e, e2);
    float f = e; swap16(e, f);
    float h = e2; swap16(e2, h);
    v[0] = e; v[1] = f; v[2] = e2; v[3] = h;
}
__device__ __forceinline__ unsigned ld_flag(GAS unsigned* f) {
    return __builtin_amdgcn_readfirstlane(__hip_atomic_load(f, __ATOMIC_RELAXED, __HIP_MEMORY_SCOPE_AGENT));
}
__device__ __forceinline__ void add_flag(GAS unsigned* f, int lane) {
    if (lane == 0) (void)__hip_atomic_fetch_add(f, 1u, __ATOMIC_RELAXED, __HIP_MEMORY_SCOPE_AGENT);
}
#define LD_FLAG_ASYNC(REG, f) asm volatile("global_load_dword " REG ", %0, off sc1" ::"v"(f) : "memory", REG)
#define RD_FLAG(REG, out) asm volatile("v_readfirstlane_b32 %0, " REG : "=s"(out)::"memory")
#define RD_VEC(REG, out) asm volatile("v_mov_b32 %0, " REG : "=v"(out)::"memory")
__device__ __forceinline__ void add_flag_async(GAS unsigned* f, int lane) {
    unsigned one = 1u;
    if (lane == 0) asm volatile("global_atomic_add %0, %1, off" ::"v"(f), "v"(one) : "memory");
}
__device__ __forceinline__ void wait_ge(GAS unsigned* f, unsigned target, GAS unsigned* hb, int lane) {
    unsigned spins = 0;
    while (ld_flag(f) < target) {
        ++spins;
        if ((spins & 31u) == 0u) {
            if (lane < 8) (void)__hip_atomic_fetch_add(hb + lane * FW, 1u, __ATOMIC_RELAXED, __HIP_MEMORY_SCOPE_AGENT);
            if (spins > SPIN_CAP) break;
        }
        __builtin_amdgcn_s_sleep(1);
    }
    asm volatile("" ::: "memory");
}
struct Frag { bf16x8 a[4], b[4]; };
#define SGB() do { _Pragma("unroll") for (int _i = 0; _i < 8; ++_i) { __builtin_amdgcn_sched_group_barrier(0x8, 1, 0); __builtin_amdgcn_sched_group_barrier(0x100, 1, 0); } __builtin_amdgcn_sched_group_barrier(0x8, 8, 0); } while (0)
__device__ __forceinline__ void nt_read(Frag& f, LAS char* pa, LAS char* pb) {
    f.a[0] = lds_b128(pa);
#pragma unroll
    for (int n = 0; n < 4; ++n) f.b[n] = lds_b128(pb + n * 2048);
#pragma unroll
    for (int m = 1; m < 4; ++m) f.a[m] = lds_b128(pa + m * 2048);
}
__device__ __forceinline__ void nt_mma(f32x4 (&acc)[4][4], const Frag& f) {
#pragma unroll
    for (int m = 0; m < 4; ++m)
#pragma unroll
        for (int n = 0; n < 4; ++n) acc[m][n] = mfma32(f.a[m], f.b[n], acc[m][n]);
}
__device__ __forceinline__ void bar_cons() {
    asm volatile("" ::: "memory");
    __builtin_amdgcn_s_waitcnt(0x0070);
    __builtin_amdgcn_s_barrier();
    asm volatile("" ::: "memory");
}
#define SGBV() do { _Pragma("unroll") for (int _i = 0; _i < 8; ++_i) { __builtin_amdgcn_sched_group_barrier(0x8, 1, 0); __builtin_amdgcn_sched_group_barrier(0x100, 1, 0); } \
    _Pragma("unroll") for (int _i = 0; _i < 2; ++_i) { __builtin_amdgcn_sched_group_barrier(0x8, 1, 0); __builtin_amdgcn_sched_group_barrier(0x4, 2, 0); } \
    _Pragma("unroll") for (int _i = 0; _i < 2; ++_i) { __builtin_amdgcn_sched_group_barrier(0x8, 1, 0); __builtin_amdgcn_sched_group_barrier(0x2, 1, 0); } \
    __builtin_amdgcn_sched_group_barrier(0x8, 4, 0); } while (0)
__device__ __forceinline__ void nt_pin(f32x4 (&acc)[4][4]) {
    asm volatile("" : "+v"(acc[0][0]), "+v"(acc[0][1]), "+v"(acc[0][2]), "+v"(acc[0][3]), "+v"(acc[1][0]), "+v"(acc[1][1]), "+v"(acc[1][2]), "+v"(acc[1][3]));
    asm volatile("" : "+v"(acc[2][0]), "+v"(acc[2][1]), "+v"(acc[2][2]), "+v"(acc[2][3]), "+v"(acc[3][0]), "+v"(acc[3][1]), "+v"(acc[3][2]), "+v"(acc[3][3]));
}
template <bool LASTBAR>
__device__ __forceinline__ int nt_consume(f32x4 (&acc)[4][4], LAS char* smem, int slot, int nst, int w, int lane) {
    const int r = lane & 15, g = lane >> 4;
    const int c0 = ((g ^ (r >> 1)) << 4), c1 = c0 ^ 64;
    const int oa = ((w & 1) * 64 + r) * 128, ob = 16384 + ((w >> 1) * 64 + r) * 128;
    const int oa0 = oa + c0, ob0 = ob + c0, oa1 = oa + c1, ob1 = ob + c1;
    Frag f0, f1;
    int so = slot * SLOT;
    nt_read(f0, smem + so + oa0, smem + so + ob0);
    int pa = so + oa1, pb = so + ob1;
#pragma unroll 1
    for (int it = 0; it < nst - 1; ++it) {
        __builtin_amdgcn_sched_barrier(0);
        nt_read(f1, smem + pa, smem + pb);
        nt_mma(acc, f0);
        so = (so == 4 * SLOT) ? 0 : so + SLOT;
        int qa = so + oa0, qb = so + ob0;
        SGBV();
        __builtin_amdgcn_sched_barrier(0);
        asm volatile("" : "+v"(qa), "+v"(qb));
        bar_cons();
        __builtin_amdgcn_sched_barrier(0);
        nt_read(f0, smem + qa, smem + qb);
        nt_mma(acc, f1);
        pa = so + oa1; pb = so + ob1;
        SGBV();
        __builtin_amdgcn_sched_barrier(0);
        asm volatile("" : "+v"(pa), "+v"(pb));
    }
    nt_read(f1, smem + pa, smem + pb);
    nt_mma(acc, f0);
    SGB();
    __builtin_amdgcn_sched_barrier(0);
    so = (so == 4 * SLOT) ? 0 : so + SLOT;
    if (LASTBAR) bar_cons();
    __builtin_amdgcn_sched_barrier(0);
    nt_mma(acc, f1);
    return so / SLOT;
}

#define PIN4(a, b, c, d) asm volatile("" : "+v"(a), "+v"(b), "+v"(c), "+v"(d))
__device__ __forceinline__ void t2_pin(f32x4 (&aq)[8], f32x4 (&ak)[9], f32x4 (&av)[9]) {
    asm volatile("" : "+v"(aq[0]), "+v"(aq[1]), "+v"(aq[2]), "+v"(aq[3]), "+v"(aq[4]), "+v"(aq[5]), "+v"(aq[6]), "+v"(aq[7]),
                      "+v"(ak[0]), "+v"(ak[1]), "+v"(ak[2]), "+v"(ak[3]), "+v"(ak[4]));
    asm volatile("" : "+v"(ak[5]), "+v"(ak[6]), "+v"(ak[7]), "+v"(ak[8]),
                      "+v"(av[0]), "+v"(av[1]), "+v"(av[2]), "+v"(av[3]), "+v"(av[4]), "+v"(av[5]), "+v"(av[6]), "+v"(av[7]), "+v"(av[8]));
}
struct Frag2 { bf16x8 x[9], w[3]; };
__device__ __forceinline__ void t2_read(Frag2& f, LAS char* px, LAS char* pw) {
    f.w[1] = lds_tr2(pw + 1024);
    f.x[0] = lds_b128(px);
    f.w[2] = lds_tr2(pw + 2048);
    f.w[0] = lds_tr2(pw);
#pragma unroll
    for (int rb = 1; rb < 9; ++rb) f.x[rb] = lds_b128(px + rb * 2048);
}
__device__ __forceinline__ void t2_mma(f32x4 (&aq)[8], f32x4 (&ak)[9], f32x4 (&av)[9], const Frag2& f) {
#pragma unroll
    for (int rb = 0; rb < 9; ++rb) {
        ak[rb] = mfma32(f.w[1], f.x[rb], ak[rb]);
        av[rb] = mfma32(f.x[rb], f.w[2], av[rb]);
        if (rb > 0) aq[rb - 1] = mfma32(f.w[0], f.x[rb], aq[rb - 1]);
    }
}
#define SGB2() do { _Pragma("unroll") for (int _i = 0; _i < 15; ++_i) { __builtin_amdgcn_sched_group_barrier(0x8, 1, 0); __builtin_amdgcn_sched_group_barrier(0x100, 1, 0); } \
    _Pragma("unroll") for (int _i = 0; _i < 2; ++_i) { __builtin_amdgcn_sched_group_barrier(0x8, 1, 0); __builtin_amdgcn_sched_group_barrier(0x4, 2, 0); } \
    _Pragma("unroll") for (int _i = 0; _i < 2; ++_i) { __builtin_amdgcn_sched_group_barrier(0x8, 1, 0); __builtin_amdgcn_sched_group_barrier(0x2, 1, 0); } \
    __builtin_amdgcn_sched_group_barrier(0x8, 7, 0); } while (0)

struct Frag4 { bf16x8 w[4][2], o[4][2]; };
#define SGB4() do { _Pragma("unroll") for (int _i = 0; _i < 12; ++_i) { __builtin_amdgcn_sched_group_barrier(0x100, 2, 0); __builtin_amdgcn_sched_group_barrier(0x8, 1, 0); } __builtin_amdgcn_sched_group_barrier(0x8, 4, 0); } while (0)
__device__ __forceinline__ void pf(rsrc_t r, int voff, int soff) {
    asm volatile("s_nop 4\n\tbuffer_load_dword v255, %0, %1, %2 offen" ::"v"(voff), "s"(r), "s"(__builtin_amdgcn_readfirstlane(soff)) : "memory", "v255");
}

__global__ __launch_bounds__(512, 2) void fused(const __bf16* __restrict__ xa, const __bf16* __restrict__ cre, const __bf16* __restrict__ cim,
                                                const __bf16* __restrict__ hre, const __bf16* __restrict__ him, const __bf16* __restrict__ xb,
                                                const __bf16* __restrict__ wqkv, const __bf16* __restrict__ wo,
                                                char* __restrict__ ws, __bf16* __restrict__ out) {
    __shared__ __attribute__((aligned(1024))) char smem_[163840];
    LAS char* smem = (LAS char*)smem_;
    asm volatile("s_load_dword s98, %0, 0x0\n\ts_load_dword s99, %0, 0x40" ::"s"(__builtin_amdgcn_kernarg_segment_ptr()) : "s98", "s99");
    const int tid = threadIdx.x, lane = tid & 63;
    const int wave = __builtin_amdgcn_readfirstlane(tid >> 6);
    const int bid = blockIdx.x, xcd = bid & 7, idx = bid >> 3;
    const int q = xcd & 3, th = xcd >> 2;
    const int ttl = idx >> 2, et = idx & 3, tt = th * 8 + ttl;
    const int t0 = tt * 128, e0 = et * 128;
    const bool typeA = (ttl & 1) == 0;
    const int pg = q;
    const int s1 = typeA ? ((q & 1) * 8 + th * 4 + (ttl >> 1)) * 128 : ((xcd >> 1) * 4 + (ttl >> 1)) * 128;
    const int hg = (xcd & 1) * 4 + et;
    const int s4 = (xcd * 4 + (idx >> 3)) * 64, n4 = (idx & 7) * 64;
    GAS unsigned* flags = (GAS unsigned*)(ws + WS_FLAGS);
    const int slot3 = typeA ? 3 : 0;
#define ROT1 (4 * (((ttl >> 1) + et) & 1))
#define ROT3 (4 * et)
    const int rot1 = ROT1, rot3 = ROT3;

    if (__builtin_expect(wave >= 6, 1)) {
        const int lw = wave - 6;
        const int row = 8 * lw + (lane >> 3);
        const int ch = ((lane & 7) ^ (4 * lw + (lane >> 4))) * 16;
        const int cht = ((lane & 7) ^ (2 * (((lane >> 4) & 1) | (lw << 1)))) * 16;
        const bool master = (bid < 8 && lw == 0);
        GAS unsigned* myhb = flags + bid * FW;
        if (master && lane < 42) __hip_atomic_store(flags + (8 + bid * 42 + lane) * FW, 0u, __ATOMIC_RELAXED, __HIP_MEMORY_SCOPE_AGENT);
        rsrc_t rc = mk_rsrc((q >> 1) ? cim : cre, SEQ * SEQ * 2);
        const int vb = row * 4096 + ch;
        const int sb = t0 * 4096 + (q & 1) * 2048;
        auto issueC = [&](int st, int slot) {
            const int so = sb + ((st + rot3) & 15) * 128;
#pragma unroll
            for (int u = 0; u < 8; ++u) glds<NT>(rc, smem + slot * SLOT + 16384 + (lw + 2 * u) * 1024, vb, so + 16 * ((u & 4) | ((u + q) & 3)) * 4096);
        };
        rsrc_t rw = mk_rsrc(wo, DM * DM * 2);
        int slot = 0;
        if (typeA) {
            rsrc_t rh = mk_rsrc((pg >> 1) ? him : hre, DM * DM * 2);
            const int vh = row * 1024 + ch;
#pragma unroll 1
            for (int st = 0; st < 8; ++st) {
                if (st == 2) bar_vm<8>();
                if (st == 4) bar_vm<16>();
                if (st >= 6) bar_vm<24>();
                if (master && st >= 2 && st < 7 && !(st & 1)) add_flag_async(myhb, lane);
                const int so = e0 * 1024 + ((st + rot1) & 7) * 128;
#pragma unroll
                for (int u = 0; u < 8; ++u) glds<NT>(rh, smem + slot * SLOT + 16384 + (lw + 2 * u) * 1024, vh, so + 16 * u * 1024);
                slot = (slot + 1 == 5) ? 0 : slot + 1;
            }
        } else {
            rsrc_t rq = mk_rsrc(wqkv, DM * 3 * DM * 2);
            const int vq = row * 3072 + cht;
            int pos = 1;
#pragma unroll 1
            for (int st = 0; st < 8; ++st) {
                if (st >= 2) bar_vm<12>();
                LAS char* sl = smem + T2_BASE + pos * T2_SLOT;
                const int so = ((st + rot1) & 7) * 64 * 3072 + hg * 128;
#pragma unroll
                for (int v = 0; v < 4; ++v)
#pragma unroll
                    for (int cg = 0; cg < 3; ++cg)
                        glds<NT>(rq, sl + 18432 + ((lw + 2 * v) * 3 + cg) * 1024, vq, so + 16 * v * 3072 + cg * 1024);
                pos = (pos + 1 == 3) ? 0 : pos + 1;
            }
        }
#pragma unroll 1
        for (int st = 0; st < 16; ++st) {
            if (st >= 5) {
                if (st == 5 && (!typeA || NE == 5)) { if (typeA) bar_only(); bar_vm<32>(); }
                bar_vm<24>();
            } else if (typeA) {
                if (NE == 5) { if (st < 4) bar_vm<24>(); else bar_only(); }
                else if (st < NE) bar_vm<24>();
                else if (st == NE) {
                    if (NE == 1) { bar_vm<24>(); bar_vm<16>(); bar_vm<8>(); }
                    if (NE == 2) { bar_vm<24>(); bar_vm<16>(); }
                    if (NE == 3) bar_vm<24>();
                    bar_only(); bar_only(); bar_vm<8 * (NE - 1)>();
                }
            } else {
                if (st == 1) bar_vm<20>();
                if (st == 2) bar_vm<16>();
                if (st == 3) bar_only();
            }
            issueC(st, slot);
            slot = (slot + 1 == 5) ? 0 : slot + 1;
            if (st == 7) pf(rw, lane * 128, idx * 16384 + lw * 8192);
        }
        const int vw = row * 1024 + cht;
#pragma unroll 1
        for (int st = 0; st < 4; ++st) {
            bar_vm<24>();
#pragma unroll
            for (int h = 0; h < 2; ++h)
#pragma unroll
                for (int u = 0; u < 4; ++u)
                    glds<NT>(rw, smem + slot * SLOT + h * 16384 + 8192 + (lw + 2 * u) * 1024, vw, ((st * 2 + h) * 64 + 16 * u) * 1024 + n4 * 2);
            slot = (slot + 1 == 5) ? 0 : slot + 1;
        }
        bar_vm<8>(); bar_vm<0>();
    } else if (__builtin_expect(wave >= 4, 1)) {
        const int lw = wave - 4;
        const int ch = ((lane & 7) ^ (4 * lw + (lane >> 4))) * 16;
        const int prow = 8 * (lane >> 5) + ((lane >> 3) & 3) + 16 * lw;
        const int row = 8 * lw + (lane >> 3);
        GAS unsigned* hbp = flags + (lane & 7) * FW;
        if (typeA) {
            rsrc_t rx = mk_rsrc(xa, SEQ * DM * 2);
            const int va = prow * 1024 + ch;
            int sl = 0;
#pragma unroll 1
            for (int st = 0; st < 8; ++st) {
                if (st == 2) bar_vm<8>();
                if (st == 4) bar_vm<16>();
                if (st >= 6) bar_vm<24>();
                if (st == 1 && lw == 0) LD_FLAG_ASYNC("v254", hbp);
                if (st == 7 && lw == 0) LD_FLAG_ASYNC("v253", hbp);
                const int so = s1 * 1024 + ((st + rot1) & 7) * 128;
#pragma unroll
                for (int u = 0; u < 8; ++u) glds<NT>(rx, smem + sl * SLOT + (lw + 2 * u) * 1024, va, so + (32 * (u >> 1) + 4 * (u & 1)) * 1024);
                sl = (sl + 1 == 5) ? 0 : sl + 1;
            }
            bar_vm<24>(); bar_vm<16>(); bar_vm<8>();
        } else {
            rsrc_t rx = mk_rsrc(xb, SEQ * DM * 2);
            const int vx = row * 1024 + ch;
            const int sxm = s1 * 1024, sxh = (s1 >= 16 ? s1 - 16 : 0) * 1024;
            int pos = 1;
#pragma unroll 1
            for (int st = 0; st < 8; ++st) {
                if (st >= 2) bar_vm<9>();
                if (st == 1 && lw == 0) LD_FLAG_ASYNC("v254", hbp);
                if (st == 7 && lw == 0) LD_FLAG_ASYNC("v253", hbp);
                LAS char* sl = smem + T2_BASE + pos * T2_SLOT;
                const int ko = ((st + rot1) & 7) * 128;
                glds<NT>(rx, sl + lw * 1024, vx, sxh + ko);
#pragma unroll
                for (int u = 0; u < 8; ++u) glds<NT>(rx, sl + 2048 + (lw + 2 * u) * 1024, vx, sxm + 16 * u * 1024 + ko);
                pos = (pos + 1 == 3) ? 0 : pos + 1;
            }
            bar_vm<9>();
        }
        bar_vm<0>();
        if (lw == 0) {
            unsigned a, b, spins = 0;
            RD_VEC("v254", a); RD_VEC("v253", b);
            bool moved = (a != b) || ((lane & 7) == bid);
            while (!__all(moved)) {
                if (bid < 8) add_flag(flags + bid * FW, lane);
                __builtin_amdgcn_s_sleep(1);
                b = __hip_atomic_load(hbp, __ATOMIC_RELAXED, __HIP_MEMORY_SCOPE_AGENT);
                moved = moved || (a != b);
                if (++spins > SPIN_CAP) break;
            }
        }
        bar_only();
        rsrc_t ra = mk_rsrc(ws + WS_ABT, 1024 * SEQ * 2), ro = mk_rsrc(ws + WS_O, SEQ * DM * 2);
        const int va = prow * 64 + (ch & 48) + (ch >> 6) * 8192;
        const int sa = (((q >> 1) * 4 + et) * 64 + (q & 1) * 32) * 8192;
        const int vo = row * 32 + (ch & 16) + (ch >> 5) * 65536;
        if (typeA) {
            bar_only();
            if (lw == 0 && lane < 4) (void)__hip_atomic_fetch_add(flags + (F1_IDX + (pg * 4 + et) * 4 + lane) * FW, 1u, __ATOMIC_RELAXED, __HIP_MEMORY_SCOPE_AGENT);
        }
        wait_ge(flags + (F1_IDX + (q * 4 + et) * 4 + lw + 2 * th) * FW, 8u, flags, lane);
        STAMP(6);
        int slot = slot3;
        GAS unsigned* f2 = flags + (F2_IDX + (s4 >> 7)) * FW;
#pragma unroll 1
        for (int st = 0; st < 16; ++st) {
            if (st == 2) bar_vm<8>();
            if (st == 4) bar_vm<16>();
            if (st >= 6) bar_vm<24>();
            if (st == 4 && lw == 1 && !typeA) add_flag_async(flags + (F2_IDX + (s1 >> 7)) * FW, lane);
            const int so = sa + ((st + rot3) & 15) * 16384;
#pragma unroll
            for (int u = 0; u < 8; ++u) glds<16>(ra, smem + slot * SLOT + (lw + 2 * u) * 1024, va, so + (32 * (u >> 1) + 4 * (u & 1)) * 64);
            slot = (slot + 1 == 5) ? 0 : slot + 1;
            if (st == 12) LD_FLAG_ASYNC("v252", f2);
        }
#pragma unroll 1
        for (int st = 0; st < 4; ++st) {
            bar_vm<24>();
            if (st == 0) {
                unsigned f2v;
                RD_FLAG("v252", f2v);
                if (f2v < 8u) wait_ge(f2, 8u, flags, lane);
            }
#pragma unroll
            for (int h = 0; h < 2; ++h)
#pragma unroll
                for (int u = 0; u < 4; ++u)
                    glds<16>(ro, smem + slot * SLOT + h * 16384 + (lw + 2 * u) * 1024, vo, (s4 + 16 * u) * 32 + (st * 2 + h) * 4 * 65536);
            slot = (slot + 1 == 5) ? 0 : slot + 1;
        }
        bar_vm<8>(); bar_vm<0>();
    } else {
        const int r = lane & 15, g = lane >> 4;
        STAMP(8);
        if (wave == 1 && ttl == 1) {
            unsigned long long pc; int off, len;
            asm volatile("s_getpc_b64 %0\n.Lpf_here:\n\ts_mov_b32 %1, .Lpf_here-_Z5fusedPKDF16bS0_S0_S0_S0_S0_S0_S0_PcPDF16b\n\ts_mov_b32 %2, _Z8code_endPj-_Z5fusedPKDF16bS0_S0_S0_S0_S0_S0_S0_PcPDF16b" : "=s"(pc), "=s"(off), "=s"(len));
            const int o = (et * 64 + lane) * 128;
            if (o < len) { unsigned v = *(const unsigned*)(pc - (unsigned)off + o); asm volatile("" ::"v"(v)); }
        }
        const int tile = tt * 4 + et;
        rsrc_t rp = mk_rsrc(ws + WS_PART, 64 * 16 * 4 * 2048);
        const int inb = (tile * 4 + q) * 32768 + wave * 2048 + lane * 16;
        if (typeA) {
            f32x4 acc[4][4] = {};
            nt_pin(acc);
            bar_lgkm();
            STAMP(1);
            nt_consume<true>(acc, smem, 0, 8, wave, lane);
            STAMP(2);
            const unsigned sg = (pg >> 1) ? 0x80008000u : 0u;
            rsrc_t rs = mk_rsrc(ws + WS_ABT, 1024 * SEQ * 2);
            const int vs = r * 64 + g * 16;
            const int ss = (((pg >> 1) * 4 + et) * 64 + (s1 >> 5) + (wave & 1) * 2) * 8192 + (wave >> 1) * 4096;
#pragma unroll
            for (int n = 0; n < 4; ++n)
#pragma unroll
                for (int u = 0; u < 2; ++u)
                    __builtin_amdgcn_raw_buffer_store_b128(pack8(acc[2 * u][n], acc[2 * u + 1][n]) ^ sg, rs, vs + ss + n * 1024 + u * 8192, 0, 16);
            STAMP(7);
            bar_vm<0>();
            STAMP(3);
        } else {
            const int c0 = ((g ^ (r >> 1)) << 4);
            const int ox = r * 128;
            const int qq = (lane & 15) >> 2, p = lane & 3;
            const int ow = 18432 + g * 3072 + qq * 128 + (((2 * wave + (p >> 1)) ^ (2 * ((qq >> 1) | ((g & 1) << 1)))) << 4) + 8 * (p & 1);
            f32x4 aq[8] = {}, ak[9] = {}, av[9] = {};
            Frag2 f0, f1;
            int so = T2_BASE + T2_SLOT;
            t2_pin(aq, ak, av);
            bar_lgkm();
            STAMP(1);
            t2_read(f0, smem + so + ox + c0, smem + so + ow);
            int px = so + ox + (c0 ^ 64), pw = so + ow;
#pragma unroll 1
            for (int it = 0; it < 7; ++it) {
                __builtin_amdgcn_sched_barrier(0);
                t2_read(f1, smem + px, smem + pw + 12288);
                t2_mma(aq, ak, av, f0);
                so = (so == T2_BASE + 2 * T2_SLOT) ? T2_BASE : so + T2_SLOT;
                int qx = so + ox + c0, qw = so + ow;
                SGB2();
                __builtin_amdgcn_sched_barrier(0);
                asm volatile("" : "+v"(qx), "+v"(qw));
                t2_pin(aq, ak, av);
                bar_cons();
                __builtin_amdgcn_sched_barrier(0);
                t2_read(f0, smem + qx, smem + qw);
                t2_mma(aq, ak, av, f1);
                px = qx ^ 64; pw = qw;
                SGB2();
                __builtin_amdgcn_sched_barrier(0);
                asm volatile("" : "+v"(px));
            }
            t2_read(f1, smem + px, smem + pw + 12288);
            t2_mma(aq, ak, av, f0);
            SGB2();
            __builtin_amdgcn_sched_barrier(0);
            t2_pin(aq, ak, av);
            bar_cons();
            __builtin_amdgcn_sched_barrier(0);
            t2_mma(aq, ak, av, f1);
            STAMP(4);
            const int j = r, par = j & 1;
            const bool uc0 = (4 * g + par) <= j, uc1 = (4 * g + par + 2) <= j;
            const unsigned s0v = par ? 0x01000c0cu : 0x0c0c0100u, s1v = par ? 0x03020c0cu : 0x0c0c0302u, zs = 0x0c0c0c0cu;
            const unsigned selc0 = uc0 ? s0v : zs, selp0 = uc0 ? zs : s0v, selc1 = uc1 ? s1v : zs, selp1 = uc1 ? zs : s1v;
            rsrc_t rs = mk_rsrc(ws + WS_O, SEQ * DM * 2);
            const int vs = j * 32 + g * 8;
            const int ss = (hg * 4 + wave) * 65536 + s1 * 32;
            const f32x4 z = {0.f, 0.f, 0.f, 0.f};
            const float C = 0.25f * 1.44269504088896f;
            u32x2 kb[9], vb[9];
            float l0[8], l1[8], m[8];
#pragma unroll
            for (int i = 0; i < 9; ++i) kb[i] = cvt4u(ak[i]);
#pragma unroll
            for (int b = 0; b < 8; ++b) {
                u32x2 qb = cvt4u(aq[b]);
                f32x4 sc = mfma16(kb[b + 1], qb, z), sp = mfma16(kb[b], qb, z);
                float c0v = par ? sc[1] : sc[0], c1v = par ? sc[3] : sc[2];
                float p0v = par ? sp[1] : sp[0], p1v = par ? sp[3] : sp[2];
                if (b == 0 && s1 == 0) { p0v = -4e9f; p1v = -4e9f; }
                unsigned pk = cvtpk(uc0 ? c0v : p0v, uc1 ? c1v : p1v);
                l0[b] = __uint_as_float(pk << 16); l1[b] = __uint_as_float(pk & 0xffff0000u);
                m[b] = fmx(l0[b], l1[b]);
            }
            allred4<true>(m); allred4<true>(m + 4);
#pragma unroll
            for (int i = 0; i < 9; ++i) vb[i] = cvt4u(av[i]);
#pragma unroll
            for (int b = 0; b < 8; ++b) {
                l0[b] = __builtin_amdgcn_exp2f((l0[b] - m[b]) * C); l1[b] = __builtin_amdgcn_exp2f((l1[b] - m[b]) * C);
                m[b] = l0[b] + l1[b];
            }
            allred4<false>(m); allred4<false>(m + 4);
            f32x4 ov[8];
#pragma unroll
            for (int b = 0; b < 8; ++b) {
                unsigned P = cvtpk(l0[b] * m[b], l1[b] * m[b]);
                u32x2 pc = {__builtin_amdgcn_perm(0u, P, selc0), __builtin_amdgcn_perm(0u, P, selc1)};
                u32x2 pp = {__builtin_amdgcn_perm(0u, P, selp0), __builtin_amdgcn_perm(0u, P, selp1)};
                ov[b] = mfma16(vb[b], pp, mfma16(vb[b + 1], pc, z));
            }
#pragma unroll
            for (int b = 0; b < 8; ++b) __builtin_amdgcn_raw_buffer_store_b64(cvt4u(ov[b]), rs, vs, ss + b * 512, 16);
            STAMP(5);
        }
        f32x4 acc[4][4] = {};
        nt_pin(acc);
        bar_lgkm();
        STAMP(9);
        int slot = nt_consume<false>(acc, smem, slot3, 16, wave, lane);
        STAMP(10);
        u32x4 sv[3][2];
#pragma unroll
        for (int n = 1; n < 4; ++n) { sv[n - 1][0] = pack8(acc[0][n], acc[1][n]); sv[n - 1][1] = pack8(acc[2][n], acc[3][n]); }
        auto send = [&](int n) {
            const int base = (((tile * 4 + ((q + n) & 3)) * 4 + q) * 4 + wave) * 2048 + lane * 16;
            __builtin_amdgcn_raw_buffer_store_b128(sv[n - 1][0], rp, base, 0, 16);
            __builtin_amdgcn_raw_buffer_store_b128(sv[n - 1][1], rp, base + 1024, 0, 16);
        };
#define ILV 1
        send(1);
        if (!ILV) { send(2); send(3); }
        f32x4 own[4];
#pragma unroll
        for (int m = 0; m < 4; ++m) own[m] = acc[m][0];
        f32x4 a4[2][2] = {};
        {
            const int c0 = ((g ^ (r >> 1)) << 4);
            const int oo = ((wave >> 1) * 32 + r) * 128;
            const int qq = (lane & 15) >> 2, p = lane & 3;
            const int swz = 2 * ((qq >> 1) | ((g & 1) << 1));
            const int ow0 = 8192 + g * 1024 + qq * 128 + ((((wave & 1) * 4 + (p >> 1)) ^ swz) << 4) + 8 * (p & 1);
            const int ow1 = ow0 ^ 32;
            Frag4 h0, h1;
            auto rd = [&](Frag4& f) {
                LAS char* sl = smem + slot * SLOT;
#pragma unroll
                for (int i = 0; i < 4; ++i) {
                    const int ho = (i >> 1) * 16384, kk = i & 1;
                    f.w[i][0] = lds_tr2(sl + ho + ow0 + kk * 4096);
                    f.o[i][0] = lds_b128(sl + ho + oo + (c0 ^ (kk * 64)));
                    f.o[i][1] = lds_b128(sl + ho + oo + (c0 ^ (kk * 64)) + 2048);
                    f.w[i][1] = lds_tr2(sl + ho + ow1 + kk * 4096);
                }
                slot = (slot + 1 == 5) ? 0 : slot + 1;
            };
            auto mm = [&](const Frag4& f) {
#pragma unroll
                for (int i = 0; i < 4; ++i) {
                    a4[0][0] = mfma32(f.w[i][0], f.o[i][0], a4[0][0]);
                    a4[0][1] = mfma32(f.w[i][0], f.o[i][1], a4[0][1]);
                    a4[1][0] = mfma32(f.w[i][1], f.o[i][0], a4[1][0]);
                    a4[1][1] = mfma32(f.w[i][1], f.o[i][1], a4[1][1]);
                }
                asm volatile("" : "+v"(a4[0][0]), "+v"(a4[0][1]), "+v"(a4[1][0]), "+v"(a4[1][1]));
            };
            asm volatile("" : "+v"(own[0]), "+v"(own[1]), "+v"(own[2]), "+v"(own[3]));
            bar_only();
            __builtin_amdgcn_sched_barrier(0);
            rd(h0);
            __builtin_amdgcn_sched_barrier(0);
            if (ILV) send(2);
            __builtin_amdgcn_sched_barrier(0);
            rd(h1); mm(h0); SGB4();
            __builtin_amdgcn_sched_barrier(0);
            if (ILV) send(3);
            __builtin_amdgcn_sched_barrier(0);
            rd(h0); mm(h1); SGB4();
            __builtin_amdgcn_sched_barrier(0);
            bar_only();
            __builtin_amdgcn_sched_barrier(0);
            rd(h1); mm(h0); SGB4();
            __builtin_amdgcn_sched_barrier(0);
            mm(h1);
            drain_vm();
            if (lane < 4 && lane != q)
                (void)__hip_atomic_fetch_add(flags + (F3_IDX + tile * 4 + lane) * FW, 1u, __ATOMIC_RELAXED, __HIP_MEMORY_SCOPE_AGENT);
#pragma unroll
            for (int n = 0; n < 2; ++n) {
                u32x2 lo = cvt4u(a4[0][n]), hi = cvt4u(a4[1][n]);
                auto r0 = __builtin_amdgcn_permlane16_swap(lo[0], hi[0], false, false);
                auto r1 = __builtin_amdgcn_permlane16_swap(lo[1], hi[1], false, false);
                u32x4 v = {r0[0], r1[0], r0[1], r1[1]};
                *(u32x4*)(out + (size_t)(s4 + (wave >> 1) * 32 + n * 16 + r) * 1024 + 512 + n4 + (wave & 1) * 32 + (g & 1) * 16 + (g >> 1) * 8) = v;
            }
        }
        STAMP(11);
        wait_ge(flags + (F3_IDX + tile * 4 + q) * FW, 12u, flags, lane);
        STAMP(12);
        u32x4 pv[3][2];
#pragma unroll
        for (int k = 0; k < 3; ++k) {
            const int o = inb + ((q + 1 + k) & 3) * 8192;
            pv[k][0] = __builtin_amdgcn_raw_buffer_load_b128(rp, o, 0, 16);
            pv[k][1] = __builtin_amdgcn_raw_buffer_load_b128(rp, o + 1024, 0, 16);
        }
#pragma unroll
        for (int k = 0; k < 3; ++k) {
            unpack8_add(pv[k][0], own[0], own[1]);
            unpack8_add(pv[k][1], own[2], own[3]);
        }
        const int trow = t0 + (wave >> 1) * 64 + q * 16 + r;
#pragma unroll
        for (int u = 0; u < 2; ++u)
            *(u32x4*)(out + (size_t)trow * 1024 + e0 + (wave & 1) * 64 + 32 * u + 8 * g) = pack8(own[2 * u], own[2 * u + 1]);
    }
}

__global__ void code_end(unsigned* p) { p[0] = 0; }

extern "C" void kernel_launch(void* const* d_in, const int* in_sizes, int n_in,
                              void* d_out, int out_size, void* d_ws, size_t ws_size,
                              hipStream_t stream) {
    fused<<<256, 512, 0, stream>>>((const __bf16*)d_in[0], (const __bf16*)d_in[1], (const __bf16*)d_in[2], (const __bf16*)d_in[3],
                                   (const __bf16*)d_in[4], (const __bf16*)d_in[5], (const __bf16*)d_in[6], (const __bf16*)d_in[7],
                                   (char*)d_ws, (__bf16*)d_out);
}
```

```cpp
#include <hip/hip_runtime.h>
#include <hip/hip_bf16.h>
#include <stdint.h>

#define LAS __attribute__((address_space(3)))
#define GAS __attribute__((address_space(1)))
typedef __attribute__((ext_vector_type(8))) __bf16 bf16x8;
typedef __attribute__((ext_vector_type(4))) __bf16 bf16x4;
typedef __attribute__((ext_vector_type(2))) __bf16 bf16x2;
typedef __attribute__((ext_vector_type(2))) float f32x2;
typedef __attribute__((ext_vector_type(4))) short s16x4;
typedef __attribute__((ext_vector_type(4))) float f32x4;
typedef __attribute__((ext_vector_type(4))) unsigned u32x4;
typedef __attribute__((ext_vector_type(2))) unsigned u32x2;
typedef __amdgpu_buffer_rsrc_t rsrc_t;

#define NOSTAMPS 1
#define STAMP_N 16
#define STAMP_OFF (64u<<20)
#define STAMP(k) do{}while(0)

constexpr int SEQ = 2048, DM = 512;
constexpr int SLOT = 32768;
constexpr int T2_SLOT = 43008;
constexpr int T2_BASE = 32768;

constexpr unsigned WS_FLAGS = 0;
constexpr unsigned WS_ABT   = 1u << 20;
constexpr unsigned WS_O     = WS_ABT + (4u << 20);
constexpr unsigned WS_PART  = WS_O + (2u << 20);
constexpr int FW = 32;
#define R1 16
#define PSL 1
constexpr int F1_IDX = 8, F2_IDX = F1_IDX + 16 * R1, F3_IDX = F2_IDX + 16, NCNT = (16 * R1 + 16 + 256) / 8;
constexpr int NT = 0;
#define NE 3
constexpr unsigned SPIN_CAP = 1u << 17;

__device__ __forceinline__ rsrc_t mk_rsrc(const void* p, unsigned bytes) {
    return __builtin_amdgcn_make_buffer_rsrc((void*)p, 0, bytes, 0x00020000);
}
template <int AUX>
__device__ __forceinline__ void glds(rsrc_t r, LAS char* dst, int voff, int soff) {
    __builtin_amdgcn_raw_ptr_buffer_load_lds(r, (LAS void*)dst, 16, voff, soff, 0, AUX);
}
template <int N> __device__ __forceinline__ void bar_vm() {
    asm volatile("s_waitcnt vmcnt(%0)\n\ts_barrier" ::"n"(N) : "memory");
}
__device__ __forceinline__ void bar_only() { asm volatile("s_barrier" ::: "memory"); }
__device__ __forceinline__ void bar_lgkm() {
    asm volatile("s_waitcnt lgkmcnt(0)\n\ts_barrier" ::: "memory");
}
__device__ __forceinline__ void drain_vm() { asm volatile("s_waitcnt vmcnt(0)" ::: "memory"); }
__device__ __forceinline__ f32x4 mfma32(bf16x8 a, bf16x8 b, f32x4 c) {
    return __builtin_amdgcn_mfma_f32_16x16x32_bf16(a, b, c, 0, 0, 0);
}
__device__ __forceinline__ f32x4 mfma16(u32x2 a, u32x2 b, f32x4 c) {
    return __builtin_amdgcn_mfma_f32_16x16x16bf16_1k(__builtin_bit_cast(s16x4, a), __builtin_bit_cast(s16x4, b), c, 0, 0, 0);
}
__device__ __forceinline__ bf16x8 lds_b128(LAS char* p) { return *(LAS bf16x8*)p; }
__device__ __forceinline__ bf16x8 lds_tr2(LAS char* p) {
    bf16x4 t0 = __builtin_amdgcn_ds_read_tr16_b64_v4bf16((LAS bf16x4*)p);
    bf16x4 t1 = __builtin_amdgcn_ds_read_tr16_b64_v4bf16((LAS bf16x4*)(p + 512));
    return __builtin_shufflevector(t0, t1, 0, 1, 2, 3, 4, 5, 6, 7);
}
__device__ __forceinline__ unsigned cvtpk(float a, float b) {
    f32x2 v = {a, b};
    unsigned u = __builtin_bit_cast(unsigned, __builtin_convertvector(v, bf16x2));
    asm("" : "+v"(u));
    return u;
}
__device__ __forceinline__ u32x2 cvt4u(f32x4 v) { u32x2 r = {cvtpk(v[0], v[1]), cvtpk(v[2], v[3])}; return r; }
__device__ __forceinline__ u32x4 pack8(f32x4 lo, f32x4 hi) {
    u32x4 r = {cvtpk(lo[0], lo[1]), cvtpk(lo[2], lo[3]), cvtpk(hi[0], hi[1]), cvtpk(hi[2], hi[3])};
    return r;
}
__device__ __forceinline__ void unpack8_add(u32x4 v, f32x4& lo, f32x4& hi) {
    unsigned a = v[0], b = v[1], c = v[2], d = v[3];
    lo[0] += __uint_as_float(a << 16); lo[1] += __uint_as_float(a & 0xffff0000u);
    lo[2] += __uint_as_float(b << 16); lo[3] += __uint_as_float(b & 0xffff0000u);
    hi[0] += __uint_as_float(c << 16); hi[1] += __uint_as_float(c & 0xffff0000u);
    hi[2] += __uint_as_float(d << 16); hi[3] += __uint_as_float(d & 0xffff0000u);
}
__device__ __forceinline__ float fmx(float a, float b) { return __builtin_elementwise_maximum(a, b); }
__device__ __forceinline__ void swap16(float& a, float& b) {
    auto r = __builtin_amdgcn_permlane16_swap(__float_as_uint(a), __float_as_uint(b), false, false);
    unsigned x = r[0], y = r[1]; a = __uint_as_float(x); b = __uint_as_float(y);
}
__device__ __forceinline__ void swap32(float& a, float& b) {
    auto r = __builtin_amdgcn_permlane32_swap(__float_as_uint(a), __float_as_uint(b), false, false);
    unsigned x = r[0], y = r[1]; a = __uint_as_float(x); b = __uint_as_float(y);
}
template <bool MAX> __device__ __forceinline__ float op2(float a, float b) { return MAX ? fmx(a, b) : a + b; }
template <bool MAX> __device__ __forceinline__ void allred4(float* v) {
    swap16(v[0], v[1]); float c = op2<MAX>(v[0], v[1]);
    swap16(v[2], v[3]); float d = op2<MAX>(v[2], v[3]);
    swap32(c, d); float e = op2<MAX>(c, d);
    if (!MAX) e = __builtin_amdgcn_rcpf(e);
    float e2 = e; swap32(e, e2);
    float f = e; swap16(e, f);
    float h = e2; swap16(e2, h);
    v[0] = e; v[1] = f; v[2] = e2; v[3] = h;
}
__device__ __forceinline__ unsigned ld_flag(GAS unsigned* f) {
    return __builtin_amdgcn_readfirstlane(__hip_atomic_load(f, __ATOMIC_RELAXED, __HIP_MEMORY_SCOPE_AGENT));
}
__device__ __forceinline__ void add_flag(GAS unsigned* f, int lane) {
    if (lane == 0) (void)__hip_atomic_fetch_add(f, 1u, __ATOMIC_RELAXED, __HIP_MEMORY_SCOPE_AGENT);
}
#define LD_FLAG_ASYNC(REG, f) asm volatile("global_load_dword " REG ", %0, off sc1" ::"v"(f) : "memory", REG)
#define RD_FLAG(REG, out) asm volatile("v_readfirstlane_b32 %0, " REG : "=s"(out)::"memory")
#define RD_VEC(REG, out) asm volatile("v_mov_b32 %0, " REG : "=v"(out)::"memory")
__device__ __forceinline__ void add_flag_async(GAS unsigned* f, int lane) {
    unsigned one = 1u;
    if (lane == 0) asm volatile("global_atomic_add %0, %1, off" ::"v"(f), "v"(one) : "memory");
}
template <int SLP = 1>
__device__ __forceinline__ void wait_ge(GAS unsigned* f, unsigned target, GAS unsigned* hb, int lane) {
    unsigned spins = 0;
    while (ld_flag(f) < target) {
        ++spins;
        if ((spins & 127u) == 0u) {
            if (lane < 8) (void)__hip_atomic_fetch_add(hb + lane * FW, 1u, __ATOMIC_RELAXED, __HIP_MEMORY_SCOPE_AGENT);
            if (spins > SPIN_CAP) break;
        }
        __builtin_amdgcn_s_sleep(SLP);
    }
    asm volatile("" ::: "memory");
}
struct Frag { bf16x8 a[4], b[4]; };
#define SGB() do { _Pragma("unroll") for (int _i = 0; _i < 8; ++_i) { __builtin_amdgcn_sched_group_barrier(0x8, 1, 0); __builtin_amdgcn_sched_group_barrier(0x100, 1, 0); } __builtin_amdgcn_sched_group_barrier(0x8, 8, 0); } while (0)
__device__ __forceinline__ void nt_read(Frag& f, LAS char* pa, LAS char* pb) {
    f.a[0] = lds_b128(pa);
#pragma unroll
    for (int n = 0; n < 4; ++n) f.b[n] = lds_b128(pb + n * 2048);
#pragma unroll
    for (int m = 1; m < 4; ++m) f.a[m] = lds_b128(pa + m * 2048);
}
__device__ __forceinline__ void nt_mma(f32x4 (&acc)[4][4], const Frag& f) {
#pragma unroll
    for (int m = 0; m < 4; ++m)
#pragma unroll
        for (int n = 0; n < 4; ++n) acc[m][n] = mfma32(f.a[m], f.b[n], acc[m][n]);
}
__device__ __forceinline__ void bar_cons() {
    asm volatile("" ::: "memory");
    __builtin_amdgcn_s_waitcnt(0x0070);
    __builtin_amdgcn_s_barrier();
    asm volatile("" ::: "memory");
}
#define SGBV() do { _Pragma("unroll") for (int _i = 0; _i < 8; ++_i) { __builtin_amdgcn_sched_group_barrier(0x8, 1, 0); __builtin_amdgcn_sched_group_barrier(0x100, 1, 0); } \
    _Pragma("unroll") for (int _i = 0; _i < 2; ++_i) { __builtin_amdgcn_sched_group_barrier(0x8, 1, 0); __builtin_amdgcn_sched_group_barrier(0x4, 2, 0); } \
    _Pragma("unroll") for (int _i = 0; _i < 2; ++_i) { __builtin_amdgcn_sched_group_barrier(0x8, 1, 0); __builtin_amdgcn_sched_group_barrier(0x2, 1, 0); } \
    __builtin_amdgcn_sched_group_barrier(0x8, 4, 0); } while (0)
__device__ __forceinline__ void nt_pin(f32x4 (&acc)[4][4]) {
    asm volatile("" : "+v"(acc[0][0]), "+v"(acc[0][1]), "+v"(acc[0][2]), "+v"(acc[0][3]), "+v"(acc[1][0]), "+v"(acc[1][1]), "+v"(acc[1][2]), "+v"(acc[1][3]));
    asm volatile("" : "+v"(acc[2][0]), "+v"(acc[2][1]), "+v"(acc[2][2]), "+v"(acc[2][3]), "+v"(acc[3][0]), "+v"(acc[3][1]), "+v"(acc[3][2]), "+v"(acc[3][3]));
}
template <bool LASTBAR>
__device__ __forceinline__ int nt_consume(f32x4 (&acc)[4][4], LAS char* smem, int slot, int nst, int w, int lane) {
    const int r = lane & 15, g = lane >> 4;
    const int c0 = ((g ^ (r >> 1)) << 4), c1 = c0 ^ 64;
    const int oa = ((w & 1) * 64 + r) * 128, ob = 16384 + ((w >> 1) * 64 + r) * 128;
    const int oa0 = oa + c0, ob0 = ob + c0, oa1 = oa + c1, ob1 = ob + c1;
    Frag f0, f1;
    int so = slot * SLOT;
    nt_read(f0, smem + so + oa0, smem + so + ob0);
    int pa = so + oa1, pb = so + ob1;
#pragma unroll 1
    for (int it = 0; it < nst - 1; ++it) {
        __builtin_amdgcn_sched_barrier(0);
        nt_read(f1, smem + pa, smem + pb);
        nt_mma(acc, f0);
        so = (so == 4 * SLOT) ? 0 : so + SLOT;
        int qa = so + oa0, qb = so + ob0;
        SGBV();
        __builtin_amdgcn_sched_barrier(0);
        asm volatile("" : "+v"(qa), "+v"(qb));
        bar_cons();
        __builtin_amdgcn_sched_barrier(0);
        nt_read(f0, smem + qa, smem + qb);
        nt_mma(acc, f1);
        pa = so + oa1; pb = so + ob1;
        SGBV();
        __builtin_amdgcn_sched_barrier(0);
        asm volatile("" : "+v"(pa), "+v"(pb));
    }
    nt_read(f1, smem + pa, smem + pb);
    nt_mma(acc, f0);
    SGB();
    __builtin_amdgcn_sched_barrier(0);
    so = (so == 4 * SLOT) ? 0 : so + SLOT;
    if (LASTBAR) bar_cons();
    __builtin_amdgcn_sched_barrier(0);
    nt_mma(acc, f1);
    return so / SLOT;
}

#define PIN4(a, b, c, d) asm volatile("" : "+v"(a), "+v"(b), "+v"(c), "+v"(d))
__device__ __forceinline__ void t2_pin(f32x4 (&aq)[8], f32x4 (&ak)[9], f32x4 (&av)[9]) {
    asm volatile("" : "+v"(aq[0]), "+v"(aq[1]), "+v"(aq[2]), "+v"(aq[3]), "+v"(aq[4]), "+v"(aq[5]), "+v"(aq[6]), "+v"(aq[7]),
                      "+v"(ak[0]), "+v"(ak[1]), "+v"(ak[2]), "+v"(ak[3]), "+v"(ak[4]));
    asm volatile("" : "+v"(ak[5]), "+v"(ak[6]), "+v"(ak[7]), "+v"(ak[8]),
                      "+v"(av[0]), "+v"(av[1]), "+v"(av[2]), "+v"(av[3]), "+v"(av[4]), "+v"(av[5]), "+v"(av[6]), "+v"(av[7]), "+v"(av[8]));
}
struct Frag2 { bf16x8 x[9], w[3]; };
__device__ __forceinline__ void t2_read(Frag2& f, LAS char* px, LAS char* pw) {
    f.w[1] = lds_tr2(pw + 1024);
    f.x[0] = lds_b128(px);
    f.w[2] = lds_tr2(pw + 2048);
    f.w[0] = lds_tr2(pw);
#pragma unroll
    for (int rb = 1; rb < 9; ++rb) f.x[rb] = lds_b128(px + rb * 2048);
}
__device__ __forceinline__ void t2_mma(f32x4 (&aq)[8], f32x4 (&ak)[9], f32x4 (&av)[9], const Frag2& f) {
#pragma unroll
    for (int rb = 0; rb < 9; ++rb) {
        ak[rb] = mfma32(f.w[1], f.x[rb], ak[rb]);
        av[rb] = mfma32(f.x[rb], f.w[2], av[rb]);
        if (rb > 0) aq[rb - 1] = mfma32(f.w[0], f.x[rb], aq[rb - 1]);
    }
}
#define SGB2() do { _Pragma("unroll") for (int _i = 0; _i < 15; ++_i) { __builtin_amdgcn_sched_group_barrier(0x8, 1, 0); __builtin_amdgcn_sched_group_barrier(0x100, 1, 0); } \
    _Pragma("unroll") for (int _i = 0; _i < 2; ++_i) { __builtin_amdgcn_sched_group_barrier(0x8, 1, 0); __builtin_amdgcn_sched_group_barrier(0x4, 2, 0); } \
    _Pragma("unroll") for (int _i = 0; _i < 2; ++_i) { __builtin_amdgcn_sched_group_barrier(0x8, 1, 0); __builtin_amdgcn_sched_group_barrier(0x2, 1, 0); } \
    __builtin_amdgcn_sched_group_barrier(0x8, 7, 0); } while (0)

struct Frag4 { bf16x8 w[4][2], o[4][2]; };
#define SGB4() do { _Pragma("unroll") for (int _i = 0; _i < 12; ++_i) { __builtin_amdgcn_sched_group_barrier(0x100, 2, 0); __builtin_amdgcn_sched_group_barrier(0x8, 1, 0); } __builtin_amdgcn_sched_group_barrier(0x8, 4, 0); } while (0)
__device__ __forceinline__ void pf(rsrc_t r, int voff, int soff) {
    asm volatile("s_nop 4\n\tbuffer_load_dword v255, %0, %1, %2 offen" ::"v"(voff), "s"(r), "s"(__builtin_amdgcn_readfirstlane(soff)) : "memory", "v255");
}

__global__ __launch_bounds__(512, 2) void fused(const __bf16* __restrict__ xa, const __bf16* __restrict__ cre, const __bf16* __restrict__ cim,
                                                const __bf16* __restrict__ hre, const __bf16* __restrict__ him, const __bf16* __restrict__ xb,
                                                const __bf16* __restrict__ wqkv, const __bf16* __restrict__ wo,
                                                char* __restrict__ ws, __bf16* __restrict__ out) {
    __shared__ __attribute__((aligned(1024))) char smem_[163840];
    LAS char* smem = (LAS char*)smem_;
    asm volatile("s_load_dword s98, %0, 0x0\n\ts_load_dword s99, %0, 0x40" ::"s"(__builtin_amdgcn_kernarg_segment_ptr()) : "s98", "s99");
    const int tid = threadIdx.x, lane = tid & 63;
    const int wave = __builtin_amdgcn_readfirstlane(tid >> 6);
    const int bid = blockIdx.x, xcd = bid & 7, idx = bid >> 3;
    const int q = xcd & 3, th = xcd >> 2;
    const int ttl = idx >> 2, et = idx & 3, tt = th * 8 + ttl;
    const int t0 = tt * 128, e0 = et * 128;
    const bool typeA = (ttl & 1) == 0;
    const int pg = q;
    const int s1 = typeA ? ((q & 1) * 8 + th * 4 + (ttl >> 1)) * 128 : ((xcd >> 1) * 4 + (ttl >> 1)) * 128;
    const int hg = (xcd & 1) * 4 + et;
    const int s4 = (xcd * 4 + (idx >> 3)) * 64, n4 = (idx & 7) * 64;
    GAS unsigned* flags = (GAS unsigned*)(ws + WS_FLAGS);
    const int slot3 = typeA ? 3 : 0;
#define ROT1 (4 * (((ttl >> 1) + et) & 1))
#define ROT3 (4 * et)
    const int rot1 = ROT1, rot3 = ROT3;

    if (wave == 1 && ttl == 1) {
        unsigned long long pc; int off, len;
        asm volatile("s_getpc_b64 %0\n.Lpf_here:\n\ts_mov_b32 %1, .Lpf_here-_Z5fusedPKDF16bS0_S0_S0_S0_S0_S0_S0_PcPDF16b\n\ts_mov_b32 %2, _Z8code_endPj-_Z5fusedPKDF16bS0_S0_S0_S0_S0_S0_S0_PcPDF16b" : "=s"(pc), "=s"(off), "=s"(len));
        const int o = (et * 64 + lane) * 128;
        if (o < len) { unsigned v = *(const unsigned*)(pc - (unsigned)off + o); asm volatile("" ::"v"(v)); }
    }
    if (__builtin_expect(wave >= 6, 1)) {
        const int lw = wave - 6;
        const int row = 8 * lw + (lane >> 3);
        const int ch = ((lane & 7) ^ (4 * lw + (lane >> 4))) * 16;
        const int cht = ((lane & 7) ^ (2 * (((lane >> 4) & 1) | (lw << 1)))) * 16;
        const bool master = (bid < 8 && lw == 0);
        GAS unsigned* myhb = flags + bid * FW;
        if (master) {
#pragma unroll
            for (int i = 0; i < NCNT; i += 64)
                if (i + lane < NCNT) __hip_atomic_store(flags + (8 + bid * NCNT + i + lane) * FW, 0u, __ATOMIC_RELAXED, __HIP_MEMORY_SCOPE_AGENT);
        }
        rsrc_t rc = mk_rsrc((q >> 1) ? cim : cre, SEQ * SEQ * 2);
        const int vb = row * 4096 + ch;
        const int sb = t0 * 4096 + (q & 1) * 2048;
        auto issueC = [&](int st, int slot) {
            const int so = sb + ((st + rot3) & 15) * 128;
#pragma unroll
            for (int u = 0; u < 8; ++u) glds<NT>(rc, smem + slot * SLOT + 16384 + (lw + 2 * u) * 1024, vb, so + 16 * ((u & 4) | ((u + q) & 3)) * 4096);
        };
        rsrc_t rw = mk_rsrc(wo, DM * DM * 2);
        int slot = 0;
        if (typeA) {
            rsrc_t rh = mk_rsrc((pg >> 1) ? him : hre, DM * DM * 2);
            const int vh = row * 1024 + ch;
#pragma unroll 1
            for (int st = 0; st < 8; ++st) {
                if (st >= 2) bar_vm<8>();
                if (master && st >= 2 && st < 5) add_flag_async(myhb, lane);
                const int so = e0 * 1024 + ((st + rot1) & 7) * 128;
#pragma unroll
                for (int u = 0; u < 8; ++u) glds<NT>(rh, smem + slot * SLOT + 16384 + (lw + 2 * u) * 1024, vh, so + 16 * u * 1024);
                slot = (slot + 1 == 5) ? 0 : slot + 1;
            }
        } else {
            rsrc_t rq = mk_rsrc(wqkv, DM * 3 * DM * 2);
            const int vq = row * 3072 + cht;
            int pos = 1;
#pragma unroll 1
            for (int st = 0; st < 8; ++st) {
                if (st >= 2) bar_vm<12>();
                LAS char* sl = smem + T2_BASE + pos * T2_SLOT;
                const int so = ((st + rot1) & 7) * 64 * 3072 + hg * 128;
#pragma unroll
                for (int v = 0; v < 4; ++v)
#pragma unroll
                    for (int cg = 0; cg < 3; ++cg)
                        glds<NT>(rq, sl + 18432 + ((lw + 2 * v) * 3 + cg) * 1024, vq, so + 16 * v * 3072 + cg * 1024);
                pos = (pos + 1 == 3) ? 0 : pos + 1;
            }
        }
#pragma unroll 1
        for (int st = 0; st < 16; ++st) {
            if (typeA) {
                if (st == 2) { bar_only(); bar_only(); }
                bar_vm<8>();
            } else if (st < 5) {
                if (st == 1) bar_vm<20>();
                if (st == 2) bar_vm<16>();
                if (st == 3) bar_only();
            } else {
                if (st == 5) { bar_vm<32>(); bar_vm<24>(); bar_vm<16>(); }
                bar_vm<8>();
            }
            issueC(st, slot);
            slot = (slot + 1 == 5) ? 0 : slot + 1;
            if (st == 7) pf(rw, lane * 128, idx * 16384 + lw * 8192);
        }
        const int vw = row * 1024 + cht;
#pragma unroll 1
        for (int st = 0; st < 4; ++st) {
            if (st < 2) bar_vm<8>();
            if (st == 2) bar_vm<0>();
#pragma unroll
            for (int h = 0; h < 2; ++h)
#pragma unroll
                for (int u = 0; u < 4; ++u)
                    glds<NT>(rw, smem + slot * SLOT + h * 16384 + 8192 + (lw + 2 * u) * 1024, vw, ((st * 2 + h) * 64 + 16 * u) * 1024 + n4 * 2);
            slot = (slot + 1 == 5) ? 0 : slot + 1;
        }
        bar_vm<0>();
    } else if (__builtin_expect(wave >= 4, 1)) {
        const int lw = wave - 4;
        const int ch = ((lane & 7) ^ (4 * lw + (lane >> 4))) * 16;
        const int prow = 8 * (lane >> 5) + ((lane >> 3) & 3) + 16 * lw;
        const int row = 8 * lw + (lane >> 3);
        GAS unsigned* hbp = flags + (lane & 7) * FW;
        if (!typeA) {
            rsrc_t rx = mk_rsrc(xb, SEQ * DM * 2);
            const int vx = row * 1024 + ch;
            const int sxm = s1 * 1024, sxh = (s1 >= 16 ? s1 - 16 : 0) * 1024;
            int pos = 1;
#pragma unroll 1
            for (int st = 0; st < 8; ++st) {
                if (st >= 2) bar_vm<9>();
                if (st == 1 && lw == 0) LD_FLAG_ASYNC("v254", hbp);
                if (st == 7 && lw == 0) LD_FLAG_ASYNC("v253", hbp);
                LAS char* sl = smem + T2_BASE + pos * T2_SLOT;
                const int ko = ((st + rot1) & 7) * 128;
                glds<NT>(rx, sl + lw * 1024, vx, sxh + ko);
#pragma unroll
                for (int u = 0; u < 8; ++u) glds<NT>(rx, sl + 2048 + (lw + 2 * u) * 1024, vx, sxm + 16 * u * 1024 + ko);
                pos = (pos + 1 == 3) ? 0 : pos + 1;
            }
            bar_vm<9>();
        }
        rsrc_t ro = mk_rsrc(ws + WS_O, SEQ * DM * 2);
        const int vo = row * 32 + (ch & 16) + (ch >> 5) * 65536;
        GAS unsigned* f2 = flags + (F2_IDX + (s4 >> 7)) * FW;
        int slot = 0;
#pragma unroll 1
        for (int pass = typeA ? 0 : 1; pass < 2; ++pass) {
            if (pass) {
                bar_vm<0>();
                if (lw == 0) {
                    unsigned a, b, spins = 0;
                    RD_VEC("v254", a); RD_VEC("v253", b);
                    bool moved = (a != b) || ((lane & 7) == bid);
                    while (!__all(moved)) {
                        if (bid < 8) add_flag(flags + bid * FW, lane);
                        __builtin_amdgcn_s_sleep(1);
                        b = __hip_atomic_load(hbp, __ATOMIC_RELAXED, __HIP_MEMORY_SCOPE_AGENT);
                        moved = moved || (a != b);
                        if (++spins > SPIN_CAP) break;
                    }
                }
                bar_only();
                if (typeA) {
                    bar_only();
                    if (lw == 0 && lane < R1) (void)__hip_atomic_fetch_add(flags + (F1_IDX + (pg * 4 + et) * R1 + lane) * FW, 1u, __ATOMIC_RELAXED, __HIP_MEMORY_SCOPE_AGENT);
                }
                wait_ge<PSL>(flags + (F1_IDX + (q * 4 + et) * R1 + ((lw + 2 * th + 4 * (ttl >> 1) + 16 * (ttl & 1)) & (R1 - 1))) * FW, 8u, flags, lane);
                STAMP(6);
            }
            rsrc_t ra = mk_rsrc(pass ? (const void*)(ws + WS_ABT) : (const void*)xa, pass ? 1024 * SEQ * 2 : SEQ * DM * 2);
            const int va = pass ? prow * 64 + (ch & 48) + (ch >> 6) * 8192 : prow * 1024 + ch;
            const int sa = pass ? (((q >> 1) * 4 + et) * 64 + (q & 1) * 32) * 8192 : s1 * 1024;
            const int rot = pass ? rot3 : rot1, nst = pass ? 16 : 8, ksh = pass ? 14 : 7, rb = pass ? 64 : 1024;
#pragma unroll 1
            for (int st = 0; st < nst; ++st) {
                if (st >= 2) bar_vm<8>();
                if (!pass) {
                    if (st == 1 && lw == 0) LD_FLAG_ASYNC("v254", hbp);
                    if (st == 7 && lw == 0) LD_FLAG_ASYNC("v253", hbp);
                } else if (st == 3 && lw == 1 && !typeA) add_flag_async(flags + (F2_IDX + (s1 >> 7)) * FW, lane);
                const int so = sa + (((st + rot) & (nst - 1)) << ksh);
#pragma unroll
                for (int u = 0; u < 8; ++u) glds<16>(ra, smem + slot * SLOT + (lw + 2 * u) * 1024, va, so + (32 * (u >> 1) + 4 * (u & 1)) * rb);
                slot = (slot + 1 == 5) ? 0 : slot + 1;
                if (st == 13) LD_FLAG_ASYNC("v252", f2);
            }
            if (!pass) bar_vm<8>();
        }
#pragma unroll 1
        for (int st = 0; st < 4; ++st) {
            if (st < 2) bar_vm<8>();
            if (st == 2) bar_vm<0>();
            if (st == 0) {
                unsigned f2v;
                RD_FLAG("v252", f2v);
                if (f2v < 8u) wait_ge(f2, 8u, flags, lane);
            }
#pragma unroll
            for (int h = 0; h < 2; ++h)
#pragma unroll
                for (int u = 0; u < 4; ++u)
                    glds<16>(ro, smem + slot * SLOT + h * 16384 + (lw + 2 * u) * 1024, vo, (s4 + 16 * u) * 32 + (st * 2 + h) * 4 * 65536);
            slot = (slot + 1 == 5) ? 0 : slot + 1;
        }
        bar_vm<0>();
    } else {
        const int r = lane & 15, g = lane >> 4;
        STAMP(8);
        const int tile = tt * 4 + et;
        rsrc_t rp = mk_rsrc(ws + WS_PART, 64 * 16 * 4 * 2048);
        const int inb = (tile * 4 + q) * 32768 + wave * 2048 + lane * 16;
        if (typeA) {
            f32x4 acc[4][4] = {};
            nt_pin(acc);
            bar_lgkm();
            STAMP(1);
            nt_consume<true>(acc, smem, 0, 8, wave, lane);
            STAMP(2);
            const unsigned sg = (pg >> 1) ? 0x80008000u : 0u;
            rsrc_t rs = mk_rsrc(ws + WS_ABT, 1024 * SEQ * 2);
            const int vs = r * 64 + g * 16;
            const int ss = (((pg >> 1) * 4 + et) * 64 + (s1 >> 5) + (wave & 1) * 2) * 8192 + (wave >> 1) * 4096;
#pragma unroll
            for (int n = 0; n < 4; ++n)
#pragma unroll
                for (int u = 0; u < 2; ++u)
                    __builtin_amdgcn_raw_buffer_store_b128(pack8(acc[2 * u][n], acc[2 * u + 1][n]) ^ sg, rs, vs + ss + n * 1024 + u * 8192, 0, 16);
            STAMP(7);
            bar_vm<0>();
            STAMP(3);
        } else {
            const int c0 = ((g ^ (r >> 1)) << 4);
            const int ox = r * 128;
            const int qq = (lane & 15) >> 2, p = lane & 3;
            const int ow = 18432 + g * 3072 + qq * 128 + (((2 * wave + (p >> 1)) ^ (2 * ((qq >> 1) | ((g & 1) << 1)))) << 4) + 8 * (p & 1);
            f32x4 aq[8] = {}, ak[9] = {}, av[9] = {};
            Frag2 f0, f1;
            int so = T2_BASE + T2_SLOT;
            t2_pin(aq, ak, av);
            bar_lgkm();
            STAMP(1);
            t2_read(f0, smem + so + ox + c0, smem + so + ow);
            int px = so + ox + (c0 ^ 64), pw = so + ow;
#pragma unroll 1
            for (int it = 0; it < 7; ++it) {
                __builtin_amdgcn_sched_barrier(0);
                t2_read(f1, smem + px, smem + pw + 12288);
                t2_mma(aq, ak, av, f0);
                so = (so == T2_BASE + 2 * T2_SLOT) ? T2_BASE : so + T2_SLOT;
                int qx = so + ox + c0, qw = so + ow;
                SGB2();
                __builtin_amdgcn_sched_barrier(0);
                asm volatile("" : "+v"(qx), "+v"(qw));
                t2_pin(aq, ak, av);
                bar_cons();
                __builtin_amdgcn_sched_barrier(0);
                t2_read(f0, smem + qx, smem + qw);
                t2_mma(aq, ak, av, f1);
                px = qx ^ 64; pw = qw;
                SGB2();
                __builtin_amdgcn_sched_barrier(0);
                asm volatile("" : "+v"(px));
            }
            t2_read(f1, smem + px, smem + pw + 12288);
            t2_mma(aq, ak, av, f0);
            SGB2();
            __builtin_amdgcn_sched_barrier(0);
            t2_pin(aq, ak, av);
            bar_cons();
            __builtin_amdgcn_sched_barrier(0);
            t2_mma(aq, ak, av, f1);
            STAMP(4);
            const int j = r, par = j & 1;
            const bool uc0 = (4 * g + par) <= j, uc1 = (4 * g + par + 2) <= j;
            const unsigned s0v = par ? 0x01000c0cu : 0x0c0c0100u, s1v = par ? 0x03020c0cu : 0x0c0c0302u, zs = 0x0c0c0c0cu;
            const unsigned selc0 = uc0 ? s0v : zs, selp0 = uc0 ? zs : s0v, selc1 = uc1 ? s1v : zs, selp1 = uc1 ? zs : s1v;
            rsrc_t rs = mk_rsrc(ws + WS_O, SEQ * DM * 2);
            const int vs = j * 32 + g * 8;
            const int ss = (hg * 4 + wave) * 65536 + s1 * 32;
            const f32x4 z = {0.f, 0.f, 0.f, 0.f};
            const float C = 0.25f * 1.44269504088896f;
            u32x2 kb[9], vb[9];
            float l0[8], l1[8], m[8];
#pragma unroll
            for (int i = 0; i < 9; ++i) kb[i] = cvt4u(ak[i]);
#pragma unroll
            for (int b = 0; b < 8; ++b) {
                u32x2 qb = cvt4u(aq[b]);
                f32x4 sc = mfma16(kb[b + 1], qb, z), sp = mfma16(kb[b], qb, z);
                float c0v = par ? sc[1] : sc[0], c1v = par ? sc[3] : sc[2];
                float p0v = par ? sp[1] : sp[0], p1v = par ? sp[3] : sp[2];
                if (b == 0 && s1 == 0) { p0v = -4e9f; p1v = -4e9f; }
                unsigned pk = cvtpk(uc0 ? c0v : p0v, uc1 ? c1v : p1v);
                l0[b] = __uint_as_float(pk << 16); l1[b] = __uint_as_float(pk & 0xffff0000u);
                m[b] = fmx(l0[b], l1[b]);
            }
            allred4<true>(m); allred4<true>(m + 4);
#pragma unroll
            for (int i = 0; i < 9; ++i) vb[i] = cvt4u(av[i]);
#pragma unroll
            for (int b = 0; b < 8; ++b) {
                l0[b] = __builtin_amdgcn_exp2f((l0[b] - m[b]) * C); l1[b] = __builtin_amdgcn_exp2f((l1[b] - m[b]) * C);
                m[b] = l0[b] + l1[b];
            }
            allred4<false>(m); allred4<false>(m + 4);
            f32x4 ov[8];
#pragma unroll
            for (int b = 0; b < 8; ++b) {
                unsigned P = cvtpk(l0[b] * m[b], l1[b] * m[b]);
                u32x2 pc = {__builtin_amdgcn_perm(0u, P, selc0), __builtin_amdgcn_perm(0u, P, selc1)};
                u32x2 pp = {__builtin_amdgcn_perm(0u, P, selp0), __builtin_amdgcn_perm(0u, P, selp1)};
                ov[b] = mfma16(vb[b], pp, mfma16(vb[b + 1], pc, z));
            }
#pragma unroll
            for (int b = 0; b < 8; ++b) __builtin_amdgcn_raw_buffer_store_b64(cvt4u(ov[b]), rs, vs, ss + b * 512, 16);
            STAMP(5);
        }
        f32x4 acc[4][4] = {};
        nt_pin(acc);
        bar_lgkm();
        STAMP(9);
        int slot = nt_consume<false>(acc, smem, slot3, 16, wave, lane);
        STAMP(10);
        u32x4 sv[3][2];
#pragma unroll
        for (int n = 1; n < 4; ++n) { sv[n - 1][0] = pack8(acc[0][n], acc[1][n]); sv[n - 1][1] = pack8(acc[2][n], acc[3][n]); }
        auto send = [&](int n) {
            const int base = (((tile * 4 + ((q + n) & 3)) * 4 + q) * 4 + wave) * 2048 + lane * 16;
            __builtin_amdgcn_raw_buffer_store_b128(sv[n - 1][0], rp, base, 0, 16);
            __builtin_amdgcn_raw_buffer_store_b128(sv[n - 1][1], rp, base + 1024, 0, 16);
        };
        send(1); send(2); send(3);
        f32x4 own[4];
#pragma unroll
        for (int m = 0; m < 4; ++m) own[m] = acc[m][0];
        f32x4 a4[2][2] = {};
        u32x4 pv[3][2];
        {
            const int c0 = ((g ^ (r >> 1)) << 4);
            const int oo = ((wave >> 1) * 32 + r) * 128;
            const int qq = (lane & 15) >> 2, p = lane & 3;
            const int swz = 2 * ((qq >> 1) | ((g & 1) << 1));
            const int ow0 = 8192 + g * 1024 + qq * 128 + ((((wave & 1) * 4 + (p >> 1)) ^ swz) << 4) + 8 * (p & 1);
            const int ow1 = ow0 ^ 32;
            Frag4 h0, h1;
            auto rd = [&](Frag4& f) {
                LAS char* sl = smem + slot * SLOT;
#pragma unroll
                for (int i = 0; i < 4; ++i) {
                    const int ho = (i >> 1) * 16384, kk = i & 1;
                    f.w[i][0] = lds_tr2(sl + ho + ow0 + kk * 4096);
                    f.o[i][0] = lds_b128(sl + ho + oo + (c0 ^ (kk * 64)));
                    f.o[i][1] = lds_b128(sl + ho + oo + (c0 ^ (kk * 64)) + 2048);
                    f.w[i][1] = lds_tr2(sl + ho + ow1 + kk * 4096);
                }
                slot = (slot + 1 == 5) ? 0 : slot + 1;
            };
            auto mm = [&](const Frag4& f) {
#pragma unroll
                for (int i = 0; i < 4; ++i) {
                    a4[0][0] = mfma32(f.w[i][0], f.o[i][0], a4[0][0]);
                    a4[0][1] = mfma32(f.w[i][0], f.o[i][1], a4[0][1]);
                    a4[1][0] = mfma32(f.w[i][1], f.o[i][0], a4[1][0]);
                    a4[1][1] = mfma32(f.w[i][1], f.o[i][1], a4[1][1]);
                }
                asm volatile("" : "+v"(a4[0][0]), "+v"(a4[0][1]), "+v"(a4[1][0]), "+v"(a4[1][1]));
            };
            asm volatile("" : "+v"(own[0]), "+v"(own[1]), "+v"(own[2]), "+v"(own[3]));
            bar_only();
            __builtin_amdgcn_sched_barrier(0);
            rd(h0);
            __builtin_amdgcn_sched_barrier(0);
            rd(h1); mm(h0); SGB4();
            __builtin_amdgcn_sched_barrier(0);
            drain_vm();
            if (lane < 4 && lane != q)
                (void)__hip_atomic_fetch_add(flags + (F3_IDX + tile * 4 + lane) * FW, 1u, __ATOMIC_RELAXED, __HIP_MEMORY_SCOPE_AGENT);
            mm(h1);
            STAMP(15);
            wait_ge(flags + (F3_IDX + tile * 4 + q) * FW, 12u, flags, lane);
            STAMP(12);
#pragma unroll
            for (int k = 0; k < 3; ++k) {
                const int o = inb + ((q + 1 + k) & 3) * 8192;
                pv[k][0] = __builtin_amdgcn_raw_buffer_load_b128(rp, o, 0, 16);
                pv[k][1] = __builtin_amdgcn_raw_buffer_load_b128(rp, o + 1024, 0, 16);
            }
            bar_only();
            __builtin_amdgcn_sched_barrier(0);
            rd(h0);
            __builtin_amdgcn_sched_barrier(0);
            rd(h1); mm(h0); SGB4();
            __builtin_amdgcn_sched_barrier(0);
            mm(h1);
#pragma unroll
            for (int n = 0; n < 2; ++n) {
                u32x2 lo = cvt4u(a4[0][n]), hi = cvt4u(a4[1][n]);
                auto r0 = __builtin_amdgcn_permlane16_swap(lo[0], hi[0], false, false);
                auto r1 = __builtin_amdgcn_permlane16_swap(lo[1], hi[1], false, false);
                u32x4 v = {r0[0], r1[0], r0[1], r1[1]};
                *(u32x4*)(out + (size_t)(s4 + (wave >> 1) * 32 + n * 16 + r) * 1024 + 512 + n4 + (wave & 1) * 32 + (g & 1) * 16 + (g >> 1) * 8) = v;
            }
        }
        STAMP(11);
#pragma unroll
        for (int k = 0; k < 3; ++k) {
            unpack8_add(pv[k][0], own[0], own[1]);
            unpack8_add(pv[k][1], own[2], own[3]);
        }
        const int trow = t0 + (wave >> 1) * 64 + q * 16 + r;
#pragma unroll
        for (int u = 0; u < 2; ++u)
            *(u32x4*)(out + (size_t)trow * 1024 + e0 + (wave & 1) * 64 + 32 * u + 8 * g) = pack8(own[2 * u], own[2 * u + 1]);
    }
}

__global__ void code_end(unsigned* p) { p[0] = 0; }

extern "C" void kernel_launch(void* const* d_in, const int* in_sizes, int n_in,
                              void* d_out, int out_size, void* d_ws, size_t ws_size,
                              hipStream_t stream) {
    fused<<<256, 512, 0, stream>>>((const __bf16*)d_in[0], (const __bf16*)d_in[1], (const __bf16*)d_in[2], (const __bf16*)d_in[3],
                                   (const __bf16*)d_in[4], (const __bf16*)d_in[5], (const __bf16*)d_in[6], (const __bf16*)d_in[7],
                                   (char*)d_ws, (__bf16*)d_out);
}
```

```cpp
#include <hip/hip_runtime.h>
#include <hip/hip_bf16.h>
#include <stdint.h>

#define LAS __attribute__((address_space(3)))
#define GAS __attribute__((address_space(1)))
typedef __attribute__((ext_vector_type(8))) __bf16 bf16x8;
typedef __attribute__((ext_vector_type(4))) __bf16 bf16x4;
typedef __attribute__((ext_vector_type(2))) __bf16 bf16x2;
typedef __attribute__((ext_vector_type(2))) float f32x2;
typedef __attribute__((ext_vector_type(4))) short s16x4;
typedef __attribute__((ext_vector_type(4))) float f32x4;
typedef __attribute__((ext_vector_type(4))) unsigned u32x4;
typedef __attribute__((ext_vector_type(2))) unsigned u32x2;
typedef __amdgpu_buffer_rsrc_t rsrc_t;

constexpr int SEQ = 2048, DM = 512;
constexpr int SLOT = 32768;
constexpr int T2_SLOT = 43008;
constexpr int T2_BASE = 32768;

constexpr unsigned WS_FLAGS = 0;
constexpr unsigned WS_ABT   = 1u << 20;
constexpr unsigned WS_O     = WS_ABT + (4u << 20);
constexpr unsigned WS_PART  = WS_O + (2u << 20);
constexpr int FW = 32;
constexpr int R1 = 16;
constexpr int F1_IDX = 8, F2_IDX = F1_IDX + 16 * R1, F3_IDX = F2_IDX + 16, NCNT = (16 * R1 + 16 + 256) / 8;
constexpr int SC1 = 16;
constexpr unsigned SPIN_CAP = 1u << 17;

__device__ __forceinline__ rsrc_t mk_rsrc(const void* p, unsigned bytes) {
    return __builtin_amdgcn_make_buffer_rsrc((void*)p, 0, bytes, 0x00020000);
}
template <int AUX>
__device__ __forceinline__ void glds(rsrc_t r, LAS char* dst, int voff, int soff) {
    __builtin_amdgcn_raw_ptr_buffer_load_lds(r, (LAS void*)dst, 16, voff, soff, 0, AUX);
}
template <int N> __device__ __forceinline__ void bar_vm() {
    asm volatile("s_waitcnt vmcnt(%0)\n\ts_barrier" ::"n"(N) : "memory");
}
__device__ __forceinline__ void bar_only() { asm volatile("s_barrier" ::: "memory"); }
__device__ __forceinline__ void bar_lgkm() {
    asm volatile("s_waitcnt lgkmcnt(0)\n\ts_barrier" ::: "memory");
}
__device__ __forceinline__ void drain_vm() { asm volatile("s_waitcnt vmcnt(0)" ::: "memory"); }
__device__ __forceinline__ f32x4 mfma32(bf16x8 a, bf16x8 b, f32x4 c) {
    return __builtin_amdgcn_mfma_f32_16x16x32_bf16(a, b, c, 0, 0, 0);
}
__device__ __forceinline__ f32x4 mfma16(u32x2 a, u32x2 b, f32x4 c) {
    return __builtin_amdgcn_mfma_f32_16x16x16bf16_1k(__builtin_bit_cast(s16x4, a), __builtin_bit_cast(s16x4, b), c, 0, 0, 0);
}
__device__ __forceinline__ bf16x8 lds_b128(LAS char* p) { return *(LAS bf16x8*)p; }
__device__ __forceinline__ bf16x8 lds_tr2(LAS char* p) {
    bf16x4 t0 = __builtin_amdgcn_ds_read_tr16_b64_v4bf16((LAS bf16x4*)p);
    bf16x4 t1 = __builtin_amdgcn_ds_read_tr16_b64_v4bf16((LAS bf16x4*)(p + 512));
    return __builtin_shufflevector(t0, t1, 0, 1, 2, 3, 4, 5, 6, 7);
}
__device__ __forceinline__ unsigned cvtpk(float a, float b) {
    f32x2 v = {a, b};
    unsigned u = __builtin_bit_cast(unsigned, __builtin_convertvector(v, bf16x2));
    asm("" : "+v"(u));
    return u;
}
__device__ __forceinline__ u32x2 cvt4u(f32x4 v) { u32x2 r = {cvtpk(v[0], v[1]), cvtpk(v[2], v[3])}; return r; }
__device__ __forceinline__ u32x4 pack8(f32x4 lo, f32x4 hi) {
    u32x4 r = {cvtpk(lo[0], lo[1]), cvtpk(lo[2], lo[3]), cvtpk(hi[0], hi[1]), cvtpk(hi[2], hi[3])};
    return r;
}
__device__ __forceinline__ void unpack8_add(u32x4 v, f32x4& lo, f32x4& hi) {
    unsigned a = v[0], b = v[1], c = v[2], d = v[3];
    lo[0] += __uint_as_float(a << 16); lo[1] += __uint_as_float(a & 0xffff0000u);
    lo[2] += __uint_as_float(b << 16); lo[3] += __uint_as_float(b & 0xffff0000u);
    hi[0] += __uint_as_float(c << 16); hi[1] += __uint_as_float(c & 0xffff0000u);
    hi[2] += __uint_as_float(d << 16); hi[3] += __uint_as_float(d & 0xffff0000u);
}
__device__ __forceinline__ float fmx(float a, float b) { return __builtin_elementwise_maximum(a, b); }
__device__ __forceinline__ void swap16(float& a, float& b) {
    auto r = __builtin_amdgcn_permlane16_swap(__float_as_uint(a), __float_as_uint(b), false, false);
    unsigned x = r[0], y = r[1]; a = __uint_as_float(x); b = __uint_as_float(y);
}
__device__ __forceinline__ void swap32(float& a, float& b) {
    auto r = __builtin_amdgcn_permlane32_swap(__float_as_uint(a), __float_as_uint(b), false, false);
    unsigned x = r[0], y = r[1]; a = __uint_as_float(x); b = __uint_as_float(y);
}
template <bool MAX> __device__ __forceinline__ float op2(float a, float b) { return MAX ? fmx(a, b) : a + b; }
template <bool MAX> __device__ __forceinline__ void allred4(float* v) {
    swap16(v[0], v[1]); float c = op2<MAX>(v[0], v[1]);
    swap16(v[2], v[3]); float d = op2<MAX>(v[2], v[3]);
    swap32(c, d); float e = op2<MAX>(c, d);
    if (!MAX) e = __builtin_amdgcn_rcpf(e);
    float e2 = e; swap32(e, e2);
    float f = e; swap16(e, f);
    float h = e2; swap16(e2, h);
    v[0] = e; v[1] = f; v[2] = e2; v[3] = h;
}
__device__ __forceinline__ unsigned ld_flag(GAS unsigned* f) {
    return __builtin_amdgcn_readfirstlane(__hip_atomic_load(f, __ATOMIC_RELAXED, __HIP_MEMORY_SCOPE_AGENT));
}
__device__ __forceinline__ void add_flag(GAS unsigned* f, int lane) {
    if (lane == 0) (void)__hip_atomic_fetch_add(f, 1u, __ATOMIC_RELAXED, __HIP_MEMORY_SCOPE_AGENT);
}
#define LD_FLAG_ASYNC(REG, f) asm volatile("global_load_dword " REG ", %0, off sc1" ::"v"(f) : "memory", REG)
#define RD_FLAG(REG, out) asm volatile("v_readfirstlane_b32 %0, " REG : "=s"(out)::"memory")
#define RD_VEC(REG, out) asm volatile("v_mov_b32 %0, " REG : "=v"(out)::"memory")
__device__ __forceinline__ void add_flag_async(GAS unsigned* f, int lane) {
    unsigned one = 1u;
    if (lane == 0) asm volatile("global_atomic_add %0, %1, off" ::"v"(f), "v"(one) : "memory");
}
__device__ __forceinline__ void wait_ge(GAS unsigned* f, unsigned target, GAS unsigned* hb, int lane) {
    unsigned spins = 0;
    while (ld_flag(f) < target) {
        ++spins;
        if ((spins & 127u) == 0u) {
            if (lane < 8) (void)__hip_atomic_fetch_add(hb + lane * FW, 1u, __ATOMIC_RELAXED, __HIP_MEMORY_SCOPE_AGENT);
            if (spins > SPIN_CAP) break;
        }
        __builtin_amdgcn_s_sleep(1);
    }
    asm volatile("" ::: "memory");
}
struct Frag { bf16x8 a[4], b[4]; };
#define SGB() do { _Pragma("unroll") for (int _i = 0; _i < 8; ++_i) { __builtin_amdgcn_sched_group_barrier(0x8, 1, 0); __builtin_amdgcn_sched_group_barrier(0x100, 1, 0); } __builtin_amdgcn_sched_group_barrier(0x8, 8, 0); } while (0)
__device__ __forceinline__ void nt_read(Frag& f, LAS char* pa, LAS char* pb) {
    f.a[0] = lds_b128(pa);
#pragma unroll
    for (int n = 0; n < 4; ++n) f.b[n] = lds_b128(pb + n * 2048);
#pragma unroll
    for (int m = 1; m < 4; ++m) f.a[m] = lds_b128(pa + m * 2048);
}
__device__ __forceinline__ void nt_mma(f32x4 (&acc)[4][4], const Frag& f) {
#pragma unroll
    for (int m = 0; m < 4; ++m)
#pragma unroll
        for (int n = 0; n < 4; ++n) acc[m][n] = mfma32(f.a[m], f.b[n], acc[m][n]);
}
__device__ __forceinline__ void bar_cons() {
    asm volatile("" ::: "memory");
    __builtin_amdgcn_s_waitcnt(0x0070);
    __builtin_amdgcn_s_barrier();
    asm volatile("" ::: "memory");
}
#define SGBV() do { _Pragma("unroll") for (int _i = 0; _i < 8; ++_i) { __builtin_amdgcn_sched_group_barrier(0x8, 1, 0); __builtin_amdgcn_sched_group_barrier(0x100, 1, 0); } \
    _Pragma("unroll") for (int _i = 0; _i < 2; ++_i) { __builtin_amdgcn_sched_group_barrier(0x8, 1, 0); __builtin_amdgcn_sched_group_barrier(0x4, 2, 0); } \
    _Pragma("unroll") for (int _i = 0; _i < 2; ++_i) { __builtin_amdgcn_sched_group_barrier(0x8, 1, 0); __builtin_amdgcn_sched_group_barrier(0x2, 1, 0); } \
    __builtin_amdgcn_sched_group_barrier(0x8, 4, 0); } while (0)
__device__ __forceinline__ void nt_pin(f32x4 (&acc)[4][4]) {
    asm volatile("" : "+v"(acc[0][0]), "+v"(acc[0][1]), "+v"(acc[0][2]), "+v"(acc[0][3]), "+v"(acc[1][0]), "+v"(acc[1][1]), "+v"(acc[1][2]), "+v"(acc[1][3]));
    asm volatile("" : "+v"(acc[2][0]), "+v"(acc[2][1]), "+v"(acc[2][2]), "+v"(acc[2][3]), "+v"(acc[3][0]), "+v"(acc[3][1]), "+v"(acc[3][2]), "+v"(acc[3][3]));
}
template <bool LASTBAR>
__device__ __forceinline__ int nt_consume(f32x4 (&acc)[4][4], LAS char* smem, int slot, int nst, int w, int lane) {
    const int r = lane & 15, g = lane >> 4;
    const int c0 = ((g ^ (r >> 1)) << 4), c1 = c0 ^ 64;
    const int oa = ((w & 1) * 64 + r) * 128, ob = 16384 + ((w >> 1) * 64 + r) * 128;
    const int oa0 = oa + c0, ob0 = ob + c0, oa1 = oa + c1, ob1 = ob + c1;
    Frag f0, f1;
    int so = slot * SLOT;
    nt_read(f0, smem + so + oa0, smem + so + ob0);
    int pa = so + oa1, pb = so + ob1;
#pragma unroll 1
    for (int it = 0; it < nst - 1; ++it) {
        __builtin_amdgcn_sched_barrier(0);
        nt_read(f1, smem + pa, smem + pb);
        nt_mma(acc, f0);
        so = (so == 4 * SLOT) ? 0 : so + SLOT;
        int qa = so + oa0, qb = so + ob0;
        SGBV();
        __builtin_amdgcn_sched_barrier(0);
        asm volatile("" : "+v"(qa), "+v"(qb));
        bar_cons();
        __builtin_amdgcn_sched_barrier(0);
        nt_read(f0, smem + qa, smem + qb);
        nt_mma(acc, f1);
        pa = so + oa1; pb = so + ob1;
        SGBV();
        __builtin_amdgcn_sched_barrier(0);
        asm volatile("" : "+v"(pa), "+v"(pb));
    }
    nt_read(f1, smem + pa, smem + pb);
    nt_mma(acc, f0);
    SGB();
    __builtin_amdgcn_sched_barrier(0);
    so = (so == 4 * SLOT) ? 0 : so + SLOT;
    if (LASTBAR) bar_cons();
    __builtin_amdgcn_sched_barrier(0);
    nt_mma(acc, f1);
    return so / SLOT;
}

__device__ __forceinline__ void t2_pin(f32x4 (&aq)[8], f32x4 (&ak)[9], f32x4 (&av)[9]) {
    asm volatile("" : "+v"(aq[0]), "+v"(aq[1]), "+v"(aq[2]), "+v"(aq[3]), "+v"(aq[4]), "+v"(aq[5]), "+v"(aq[6]), "+v"(aq[7]),
                      "+v"(ak[0]), "+v"(ak[1]), "+v"(ak[2]), "+v"(ak[3]), "+v"(ak[4]));
    asm volatile("" : "+v"(ak[5]), "+v"(ak[6]), "+v"(ak[7]), "+v"(ak[8]),
                      "+v"(av[0]), "+v"(av[1]), "+v"(av[2]), "+v"(av[3]), "+v"(av[4]), "+v"(av[5]), "+v"(av[6]), "+v"(av[7]), "+v"(av[8]));
}
struct Frag2 { bf16x8 x[9], w[3]; };
__device__ __forceinline__ void t2_read(Frag2& f, LAS char* px, LAS char* pw) {
    f.w[1] = lds_tr2(pw + 1024);
    f.x[0] = lds_b128(px);
    f.w[2] = lds_tr2(pw + 2048);
    f.w[0] = lds_tr2(pw);
#pragma unroll
    for (int rb = 1; rb < 9; ++rb) f.x[rb] = lds_b128(px + rb * 2048);
}
__device__ __forceinline__ void t2_mma(f32x4 (&aq)[8], f32x4 (&ak)[9], f32x4 (&av)[9], const Frag2& f) {
#pragma unroll
    for (int rb = 0; rb < 9; ++rb) {
        ak[rb] = mfma32(f.w[1], f.x[rb], ak[rb]);
        av[rb] = mfma32(f.x[rb], f.w[2], av[rb]);
        if (rb > 0) aq[rb - 1] = mfma32(f.w[0], f.x[rb], aq[rb - 1]);
    }
}
#define SGB2() do { _Pragma("unroll") for (int _i = 0; _i < 15; ++_i) { __builtin_amdgcn_sched_group_barrier(0x8, 1, 0); __builtin_amdgcn_sched_group_barrier(0x100, 1, 0); } \
    _Pragma("unroll") for (int _i = 0; _i < 2; ++_i) { __builtin_amdgcn_sched_group_barrier(0x8, 1, 0); __builtin_amdgcn_sched_group_barrier(0x4, 2, 0); } \
    _Pragma("unroll") for (int _i = 0; _i < 2; ++_i) { __builtin_amdgcn_sched_group_barrier(0x8, 1, 0); __builtin_amdgcn_sched_group_barrier(0x2, 1, 0); } \
    __builtin_amdgcn_sched_group_barrier(0x8, 7, 0); } while (0)

struct Frag4 { bf16x8 w[4][2], o[4][2]; };
#define SGB4() do { _Pragma("unroll") for (int _i = 0; _i < 12; ++_i) { __builtin_amdgcn_sched_group_barrier(0x100, 2, 0); __builtin_amdgcn_sched_group_barrier(0x8, 1, 0); } __builtin_amdgcn_sched_group_barrier(0x8, 4, 0); } while (0)
__device__ __forceinline__ void pf(rsrc_t r, int voff, int soff) {
    asm volatile("s_nop 4\n\tbuffer_load_dword v255, %0, %1, %2 offen" ::"v"(voff), "s"(r), "s"(__builtin_amdgcn_readfirstlane(soff)) : "memory", "v255");
}

__global__ __launch_bounds__(512, 2) void fused(const __bf16* __restrict__ xa, const __bf16* __restrict__ cre, const __bf16* __restrict__ cim,
                                                const __bf16* __restrict__ hre, const __bf16* __restrict__ him, const __bf16* __restrict__ xb,
                                                const __bf16* __restrict__ wqkv, const __bf16* __restrict__ wo,
                                                char* __restrict__ ws, __bf16* __restrict__ out) {
    __shared__ __attribute__((aligned(1024))) char smem_[163840];
    LAS char* smem = (LAS char*)smem_;
    asm volatile("s_load_dword s98, %0, 0x0\n\ts_load_dword s99, %0, 0x40" ::"s"(__builtin_amdgcn_kernarg_segment_ptr()) : "s98", "s99");
    const int tid = threadIdx.x, lane = tid & 63;
    const int wave = __builtin_amdgcn_readfirstlane(tid >> 6);
    const int bid = blockIdx.x, xcd = bid & 7, idx = bid >> 3;
    const int q = xcd & 3, th = xcd >> 2;
    const int ttl = idx >> 2, et = idx & 3, tt = th * 8 + ttl;
    const int t0 = tt * 128, e0 = et * 128;
    const bool typeA = (ttl & 1) == 0;
    const int s1 = typeA ? ((q & 1) * 8 + th * 4 + (ttl >> 1)) * 128 : ((xcd >> 1) * 4 + (ttl >> 1)) * 128;
    const int hg = (xcd & 1) * 4 + et;
    const int s4 = (xcd * 4 + (idx >> 3)) * 64, n4 = (idx & 7) * 64;
    GAS unsigned* flags = (GAS unsigned*)(ws + WS_FLAGS);
    const int slot3 = typeA ? 3 : 0;
    const int rot1 = 4 * (((ttl >> 1) + et) & 1), rot3 = 4 * et;

    if (wave == 1 && ttl == 1) {
        unsigned long long pc; int len;
        asm volatile("s_getpc_b64 %0\n.Lfnet_pc%=:\n\ts_mov_b32 %1, fnet_code_end-.Lfnet_pc%=" : "=s"(pc), "=s"(len));
        const int o = (et * 64 + lane) * 128;
        if (o + 4 <= len) { unsigned v = *(const unsigned*)(pc + o); asm volatile("" ::"v"(v)); }
    }
    if (__builtin_expect(wave >= 6, 1)) {
        const int lw = wave - 6;
        const int row = 8 * lw + (lane >> 3);
        const int ch = ((lane & 7) ^ (4 * lw + (lane >> 4))) * 16;
        const int cht = ((lane & 7) ^ (2 * (((lane >> 4) & 1) | (lw << 1)))) * 16;
        const bool master = (bid < 8 && lw == 0);
        GAS unsigned* myhb = flags + bid * FW;
        if (master) {
#pragma unroll
            for (int i = 0; i < NCNT; i += 64)
                if (i + lane < NCNT) __hip_atomic_store(flags + (8 + bid * NCNT + i + lane) * FW, 0u, __ATOMIC_RELAXED, __HIP_MEMORY_SCOPE_AGENT);
        }
        rsrc_t rc = mk_rsrc((q >> 1) ? cim : cre, SEQ * SEQ * 2);
        const int vb = row * 4096 + ch;
        const int sb = t0 * 4096 + (q & 1) * 2048;
        auto issueC = [&](int st, int slot) {
            const int so = sb + ((st + rot3) & 15) * 128;
#pragma unroll
            for (int u = 0; u < 8; ++u) glds<0>(rc, smem + slot * SLOT + 16384 + (lw + 2 * u) * 1024, vb, so + 16 * ((u & 4) | ((u + q) & 3)) * 4096);
        };
        rsrc_t rw = mk_rsrc(wo, DM * DM * 2);
        int slot = 0;
        if (typeA) {
            rsrc_t rh = mk_rsrc((q >> 1) ? him : hre, DM * DM * 2);
            const int vh = row * 1024 + ch;
#pragma unroll 1
            for (int st = 0; st < 8; ++st) {
                if (st >= 2) bar_vm<8>();
                if (master && st >= 2 && st < 5) add_flag_async(myhb, lane);
                const int so = e0 * 1024 + ((st + rot1) & 7) * 128;
#pragma unroll
                for (int u = 0; u < 8; ++u) glds<0>(rh, smem + slot * SLOT + 16384 + (lw + 2 * u) * 1024, vh, so + 16 * u * 1024);
                slot = (slot + 1 == 5) ? 0 : slot + 1;
            }
        } else {
            rsrc_t rq = mk_rsrc(wqkv, DM * 3 * DM * 2);
            const int vq = row * 3072 + cht;
            int pos = 1;
#pragma unroll 1
            for (int st = 0; st < 8; ++st) {
                if (st >= 2) bar_vm<12>();
                LAS char* sl = smem + T2_BASE + pos * T2_SLOT;
                const int so = ((st + rot1) & 7) * 64 * 3072 + hg * 128;
#pragma unroll
                for (int v = 0; v < 4; ++v)
#pragma unroll
                    for (int cg = 0; cg < 3; ++cg)
                        glds<0>(rq, sl + 18432 + ((lw + 2 * v) * 3 + cg) * 1024, vq, so + 16 * v * 3072 + cg * 1024);
                pos = (pos + 1 == 3) ? 0 : pos + 1;
            }
        }
#pragma unroll 1
        for (int st = 0; st < 16; ++st) {
            if (typeA) {
                if (st == 2) { bar_only(); bar_only(); }
                bar_vm<8>();
            } else if (st < 5) {
                if (st == 1) bar_vm<20>();
                if (st == 2) bar_vm<16>();
                if (st == 3) bar_only();
            } else {
                if (st == 5) { bar_vm<32>(); bar_vm<24>(); bar_vm<16>(); }
                bar_vm<8>();
            }
            issueC(st, slot);
            slot = (slot + 1 == 5) ? 0 : slot + 1;
            if (st == 7) pf(rw, lane * 128, idx * 16384 + lw * 8192);
        }
        const int vw = row * 1024 + cht;
#pragma unroll 1
        for (int st = 0; st < 4; ++st) {
            if (st < 2) bar_vm<8>();
            if (st == 2) bar_vm<0>();
#pragma unroll
            for (int h = 0; h < 2; ++h)
#pragma unroll
                for (int u = 0; u < 4; ++u)
                    glds<0>(rw, smem + slot * SLOT + h * 16384 + 8192 + (lw + 2 * u) * 1024, vw, ((st * 2 + h) * 64 + 16 * u) * 1024 + n4 * 2);
            slot = (slot + 1 == 5) ? 0 : slot + 1;
        }
        bar_vm<0>();
    } else if (__builtin_expect(wave >= 4, 1)) {
        const int lw = wave - 4;
        const int ch = ((lane & 7) ^ (4 * lw + (lane >> 4))) * 16;
        const int prow = 8 * (lane >> 5) + ((lane >> 3) & 3) + 16 * lw;
        const int row = 8 * lw + (lane >> 3);
        GAS unsigned* hbp = flags + (lane & 7) * FW;
        if (!typeA) {
            rsrc_t rx = mk_rsrc(xb, SEQ * DM * 2);
            const int vx = row * 1024 + ch;
            const int sxm = s1 * 1024, sxh = (s1 >= 16 ? s1 - 16 : 0) * 1024;
            int pos = 1;
#pragma unroll 1
            for (int st = 0; st < 8; ++st) {
                if (st >= 2) bar_vm<9>();
                if (st == 1 && lw == 0) LD_FLAG_ASYNC("v254", hbp);
                if (st == 7 && lw == 0) LD_FLAG_ASYNC("v253", hbp);
                LAS char* sl = smem + T2_BASE + pos * T2_SLOT;
                const int ko = ((st + rot1) & 7) * 128;
                glds<0>(rx, sl + lw * 1024, vx, sxh + ko);
#pragma unroll
                for (int u = 0; u < 8; ++u) glds<0>(rx, sl + 2048 + (lw + 2 * u) * 1024, vx, sxm + 16 * u * 1024 + ko);
                pos = (pos + 1 == 3) ? 0 : pos + 1;
            }
            bar_vm<9>();
        }
        rsrc_t ro = mk_rsrc(ws + WS_O, SEQ * DM * 2);
        const int vo = row * 32 + (ch & 16) + (ch >> 5) * 65536;
        GAS unsigned* f2 = flags + (F2_IDX + (s4 >> 7)) * FW;
        int slot = 0;
#pragma unroll 1
        for (int pass = typeA ? 0 : 1; pass < 2; ++pass) {
            if (pass) {
                bar_vm<0>();
                if (lw == 0) {
                    unsigned a, b, spins = 0;
                    RD_VEC("v254", a); RD_VEC("v253", b);
                    bool moved = (a != b) || ((lane & 7) == bid);
                    while (!__all(moved)) {
                        if (bid < 8) add_flag(flags + bid * FW, lane);
                        __builtin_amdgcn_s_sleep(1);
                        b = __hip_atomic_load(hbp, __ATOMIC_RELAXED, __HIP_MEMORY_SCOPE_AGENT);
                        moved = moved || (a != b);
                        if (++spins > SPIN_CAP) break;
                    }
                }
                bar_only();
                if (typeA) {
                    bar_only();
                    if (lw == 0 && lane < R1) (void)__hip_atomic_fetch_add(flags + (F1_IDX + (q * 4 + et) * R1 + lane) * FW, 1u, __ATOMIC_RELAXED, __HIP_MEMORY_SCOPE_AGENT);
                }
                wait_ge(flags + (F1_IDX + (q * 4 + et) * R1 + ((lw + 2 * th + 4 * (ttl >> 1) + 16 * (ttl & 1)) & (R1 - 1))) * FW, 8u, flags, lane);
            }
            rsrc_t ra = mk_rsrc(pass ? (const void*)(ws + WS_ABT) : (const void*)xa, pass ? 1024 * SEQ * 2 : SEQ * DM * 2);
            const int va = pass ? prow * 64 + (ch & 48) + (ch >> 6) * 8192 : prow * 1024 + ch;
            const int sa = pass ? (((q >> 1) * 4 + et) * 64 + (q & 1) * 32) * 8192 : s1 * 1024;
            const int rot = pass ? rot3 : rot1, nst = pass ? 16 : 8, ksh = pass ? 14 : 7, rb = pass ? 64 : 1024;
#pragma unroll 1
            for (int st = 0; st < nst; ++st) {
                if (st >= 2) bar_vm<8>();
                if (!pass) {
                    if (st == 1 && lw == 0) LD_FLAG_ASYNC("v254", hbp);
                    if (st == 7 && lw == 0) LD_FLAG_ASYNC("v253", hbp);
                } else if (st == 3 && lw == 1 && !typeA) add_flag_async(flags + (F2_IDX + (s1 >> 7)) * FW, lane);
                const int so = sa + (((st + rot) & (nst - 1)) << ksh);
#pragma unroll
                for (int u = 0; u < 8; ++u) glds<SC1>(ra, smem + slot * SLOT + (lw + 2 * u) * 1024, va, so + (32 * (u >> 1) + 4 * (u & 1)) * rb);
                slot = (slot + 1 == 5) ? 0 : slot + 1;
                if (st == 13) LD_FLAG_ASYNC("v252", f2);
            }
            if (!pass) bar_vm<8>();
        }
#pragma unroll 1
        for (int st = 0; st < 4; ++st) {
            if (st < 2) bar_vm<8>();
            if (st == 2) bar_vm<0>();
            if (st == 0) {
                unsigned f2v;
                RD_FLAG("v252", f2v);
                if (f2v < 8u) wait_ge(f2, 8u, flags, lane);
            }
#pragma unroll
            for (int h = 0; h < 2; ++h)
#pragma unroll
                for (int u = 0; u < 4; ++u)
                    glds<SC1>(ro, smem + slot * SLOT + h * 16384 + (lw + 2 * u) * 1024, vo, (s4 + 16 * u) * 32 + (st * 2 + h) * 4 * 65536);
            slot = (slot + 1 == 5) ? 0 : slot + 1;
        }
        bar_vm<0>();
    } else {
        const int r = lane & 15, g = lane >> 4;
        const int tile = tt * 4 + et;
        rsrc_t rp = mk_rsrc(ws + WS_PART, 64 * 16 * 4 * 2048);
        const int inb = (tile * 4 + q) * 32768 + wave * 2048 + lane * 16;
        if (typeA) {
            f32x4 acc[4][4] = {};
            nt_pin(acc);
            bar_lgkm();
            nt_consume<true>(acc, smem, 0, 8, wave, lane);
            const unsigned sg = (q >> 1) ? 0x80008000u : 0u;
            rsrc_t rs = mk_rsrc(ws + WS_ABT, 1024 * SEQ * 2);
            const int vs = r * 64 + g * 16;
            const int ss = (((q >> 1) * 4 + et) * 64 + (s1 >> 5) + (wave & 1) * 2) * 8192 + (wave >> 1) * 4096;
#pragma unroll
            for (int n = 0; n < 4; ++n)
#pragma unroll
                for (int u = 0; u < 2; ++u)
                    __builtin_amdgcn_raw_buffer_store_b128(pack8(acc[2 * u][n], acc[2 * u + 1][n]) ^ sg, rs, vs + ss + n * 1024 + u * 8192, 0, SC1);
            bar_vm<0>();
        } else {
            const int c0 = ((g ^ (r >> 1)) << 4);
            const int ox = r * 128;
            const int qq = (lane & 15) >> 2, p = lane & 3;
            const int ow = 18432 + g * 3072 + qq * 128 + (((2 * wave + (p >> 1)) ^ (2 * ((qq >> 1) | ((g & 1) << 1)))) << 4) + 8 * (p & 1);
            f32x4 aq[8] = {}, ak[9] = {}, av[9] = {};
            Frag2 f0, f1;
            int so = T2_BASE + T2_SLOT;
            t2_pin(aq, ak, av);
            bar_lgkm();
            t2_read(f0, smem + so + ox + c0, smem + so + ow);
            int px = so + ox + (c0 ^ 64), pw = so + ow;
#pragma unroll 1
            for (int it = 0; it < 7; ++it) {
                __builtin_amdgcn_sched_barrier(0);
                t2_read(f1, smem + px, smem + pw + 12288);
                t2_mma(aq, ak, av, f0);
                so = (so == T2_BASE + 2 * T2_SLOT) ? T2_BASE : so + T2_SLOT;
                int qx = so + ox + c0, qw = so + ow;
                SGB2();
                __builtin_amdgcn_sched_barrier(0);
                asm volatile("" : "+v"(qx), "+v"(qw));
                t2_pin(aq, ak, av);
                bar_cons();
                __builtin_amdgcn_sched_barrier(0);
                t2_read(f0, smem + qx, smem + qw);
                t2_mma(aq, ak, av, f1);
                px = qx ^ 64; pw = qw;
                SGB2();
                __builtin_amdgcn_sched_barrier(0);
                asm volatile("" : "+v"(px));
            }
            t2_read(f1, smem + px, smem + pw + 12288);
            t2_mma(aq, ak, av, f0);
            SGB2();
            __builtin_amdgcn_sched_barrier(0);
            t2_pin(aq, ak, av);
            bar_cons();
            __builtin_amdgcn_sched_barrier(0);
            t2_mma(aq, ak, av, f1);
            const int j = r, par = j & 1;
            const bool uc0 = (4 * g + par) <= j, uc1 = (4 * g + par + 2) <= j;
            const unsigned s0v = par ? 0x01000c0cu : 0x0c0c0100u, s1v = par ? 0x03020c0cu : 0x0c0c0302u, zs = 0x0c0c0c0cu;
            const unsigned selc0 = uc0 ? s0v : zs, selp0 = uc0 ? zs : s0v, selc1 = uc1 ? s1v : zs, selp1 = uc1 ? zs : s1v;
            rsrc_t rs = mk_rsrc(ws + WS_O, SEQ * DM * 2);
            const int vs = j * 32 + g * 8;
            const int ss = (hg * 4 + wave) * 65536 + s1 * 32;
            const f32x4 z = {0.f, 0.f, 0.f, 0.f};
            const float C = 0.25f * 1.44269504088896f;
            u32x2 kb[9], vb[9];
            float l0[8], l1[8], m[8];
#pragma unroll
            for (int i = 0; i < 9; ++i) kb[i] = cvt4u(ak[i]);
#pragma unroll
            for (int b = 0; b < 8; ++b) {
                u32x2 qb = cvt4u(aq[b]);
                f32x4 sc = mfma16(kb[b + 1], qb, z), sp = mfma16(kb[b], qb, z);
                float c0v = par ? sc[1] : sc[0], c1v = par ? sc[3] : sc[2];
                float p0v = par ? sp[1] : sp[0], p1v = par ? sp[3] : sp[2];
                if (b == 0 && s1 == 0) { p0v = -4e9f; p1v = -4e9f; }
                unsigned pk = cvtpk(uc0 ? c0v : p0v, uc1 ? c1v : p1v);
                l0[b] = __uint_as_float(pk << 16); l1[b] = __uint_as_float(pk & 0xffff0000u);
                m[b] = fmx(l0[b], l1[b]);
            }
            allred4<true>(m); allred4<true>(m + 4);
#pragma unroll
            for (int i = 0; i < 9; ++i) vb[i] = cvt4u(av[i]);
#pragma unroll
            for (int b = 0; b < 8; ++b) {
                l0[b] = __builtin_amdgcn_exp2f((l0[b] - m[b]) * C); l1[b] = __builtin_amdgcn_exp2f((l1[b] - m[b]) * C);
                m[b] = l0[b] + l1[b];
            }
            allred4<false>(m); allred4<false>(m + 4);
            f32x4 ov[8];
#pragma unroll
            for (int b = 0; b < 8; ++b) {
                unsigned P = cvtpk(l0[b] * m[b], l1[b] * m[b]);
                u32x2 pc = {__builtin_amdgcn_perm(0u, P, selc0), __builtin_amdgcn_perm(0u, P, selc1)};
                u32x2 pp = {__builtin_amdgcn_perm(0u, P, selp0), __builtin_amdgcn_perm(0u, P, selp1)};
                ov[b] = mfma16(vb[b], pp, mfma16(vb[b + 1], pc, z));
            }
#pragma unroll
            for (int b = 0; b < 8; ++b) __builtin_amdgcn_raw_buffer_store_b64(cvt4u(ov[b]), rs, vs, ss + b * 512, SC1);
        }
        f32x4 acc[4][4] = {};
        nt_pin(acc);
        bar_lgkm();
        int slot = nt_consume<false>(acc, smem, slot3, 16, wave, lane);
        u32x4 sv[3][2];
#pragma unroll
        for (int n = 1; n < 4; ++n) { sv[n - 1][0] = pack8(acc[0][n], acc[1][n]); sv[n - 1][1] = pack8(acc[2][n], acc[3][n]); }
        auto send = [&](int n) {
            const int base = (((tile * 4 + ((q + n) & 3)) * 4 + q) * 4 + wave) * 2048 + lane * 16;
            __builtin_amdgcn_raw_buffer_store_b128(sv[n - 1][0], rp, base, 0, SC1);
            __builtin_amdgcn_raw_buffer_store_b128(sv[n - 1][1], rp, base + 1024, 0, SC1);
        };
        send(1); send(2); send(3);
        f32x4 own[4];
#pragma unroll
        for (int m = 0; m < 4; ++m) own[m] = acc[m][0];
        f32x4 a4[2][2] = {};
        rsrc_t rout = mk_rsrc(out, SEQ * 1024 * 2);
        u32x4 pv[3][2];
        {
            const int c0 = ((g ^ (r >> 1)) << 4);
            const int oo = ((wave >> 1) * 32 + r) * 128;
            const int qq = (lane & 15) >> 2, p = lane & 3;
            const int swz = 2 * ((qq >> 1) | ((g & 1) << 1));
            const int ow0 = 8192 + g * 1024 + qq * 128 + ((((wave & 1) * 4 + (p >> 1)) ^ swz) << 4) + 8 * (p & 1);
            const int ow1 = ow0 ^ 32;
            Frag4 h0, h1;
            auto rd = [&](Frag4& f) {
                LAS char* sl = smem + slot * SLOT;
#pragma unroll
                for (int i = 0; i < 4; ++i) {
                    const int ho = (i >> 1) * 16384, kk = i & 1;
                    f.w[i][0] = lds_tr2(sl + ho + ow0 + kk * 4096);
                    f.o[i][0] = lds_b128(sl + ho + oo + (c0 ^ (kk * 64)));
                    f.o[i][1] = lds_b128(sl + ho + oo + (c0 ^ (kk * 64)) + 2048);
                    f.w[i][1] = lds_tr2(sl + ho + ow1 + kk * 4096);
                }
                slot = (slot + 1 == 5) ? 0 : slot + 1;
            };
            auto mm = [&](const Frag4& f) {
#pragma unroll
                for (int i = 0; i < 4; ++i) {
                    a4[0][0] = mfma32(f.w[i][0], f.o[i][0], a4[0][0]);
                    a4[0][1] = mfma32(f.w[i][0], f.o[i][1], a4[0][1]);
                    a4[1][0] = mfma32(f.w[i][1], f.o[i][0], a4[1][0]);
                    a4[1][1] = mfma32(f.w[i][1], f.o[i][1], a4[1][1]);
                }
                asm volatile("" : "+v"(a4[0][0]), "+v"(a4[0][1]), "+v"(a4[1][0]), "+v"(a4[1][1]));
            };
            asm volatile("" : "+v"(own[0]), "+v"(own[1]), "+v"(own[2]), "+v"(own[3]));
            bar_only();
            __builtin_amdgcn_sched_barrier(0);
            rd(h0);
            __builtin_amdgcn_sched_barrier(0);
            rd(h1); mm(h0); SGB4();
            __builtin_amdgcn_sched_barrier(0);
            drain_vm();
            if (lane < 4 && lane != q)
                (void)__hip_atomic_fetch_add(flags + (F3_IDX + tile * 4 + lane) * FW, 1u, __ATOMIC_RELAXED, __HIP_MEMORY_SCOPE_AGENT);
            mm(h1);
            wait_ge(flags + (F3_IDX + tile * 4 + q) * FW, 12u, flags, lane);
#pragma unroll
            for (int k = 0; k < 3; ++k) {
                const int o = inb + ((q + 1 + k) & 3) * 8192;
                pv[k][0] = __builtin_amdgcn_raw_buffer_load_b128(rp, o, 0, SC1);
                pv[k][1] = __builtin_amdgcn_raw_buffer_load_b128(rp, o + 1024, 0, SC1);
            }
            bar_only();
            __builtin_amdgcn_sched_barrier(0);
            rd(h0);
            __builtin_amdgcn_sched_barrier(0);
            rd(h1); mm(h0); SGB4();
            __builtin_amdgcn_sched_barrier(0);
            mm(h1);
#pragma unroll
            for (int n = 0; n < 2; ++n) {
                u32x2 lo = cvt4u(a4[0][n]), hi = cvt4u(a4[1][n]);
                auto r0 = __builtin_amdgcn_permlane16_swap(lo[0], hi[0], false, false);
                auto r1 = __builtin_amdgcn_permlane16_swap(lo[1], hi[1], false, false);
                u32x4 v = {r0[0], r1[0], r0[1], r1[1]};
                __builtin_amdgcn_raw_buffer_store_b128(v, rout, ((s4 + (wave >> 1) * 32 + n * 16 + r) * 1024 + 512 + n4 + (wave & 1) * 32 + (g & 1) * 16 + (g >> 1) * 8) * 2, 0, SC1);
            }
        }
#pragma unroll
        for (int k = 0; k < 3; ++k) {
            unpack8_add(pv[k][0], own[0], own[1]);
            unpack8_add(pv[k][1], own[2], own[3]);
        }
        const int trow = t0 + (wave >> 1) * 64 + q * 16 + r;
#pragma unroll
        for (int u = 0; u < 2; ++u)
            __builtin_amdgcn_raw_buffer_store_b128(pack8(own[2 * u], own[2 * u + 1]), rout, (trow * 1024 + e0 + (wave & 1) * 64 + 32 * u + 8 * g) * 2, 0, SC1);
    }
}

extern "C" __global__ void fnet_code_end(unsigned* p) { p[0] = 0; }

extern "C" void kernel_launch(void* const* d_in, const int* in_sizes, int n_in,
                              void* d_out, int out_size, void* d_ws, size_t ws_size,
                              hipStream_t stream) {
    fused<<<256, 512, 0, stream>>>((const __bf16*)d_in[0], (const __bf16*)d_in[1], (const __bf16*)d_in[2], (const __bf16*)d_in[3],
                                   (const __bf16*)d_in[4], (const __bf16*)d_in[5], (const __bf16*)d_in[6], (const __bf16*)d_in[7],
                                   (char*)d_ws, (__bf16*)d_out);
}
```

```cpp
#define OUTSC 16
#define SA 0
#define SB 8
#define MULT 4
#define DBID 9
#include <hip/hip_runtime.h>
#include <hip/hip_bf16.h>
#include <stdint.h>

#define LAS __attribute__((address_space(3)))
#define GAS __attribute__((address_space(1)))
typedef __attribute__((ext_vector_type(8))) __bf16 bf16x8;
typedef __attribute__((ext_vector_type(4))) __bf16 bf16x4;
typedef __attribute__((ext_vector_type(2))) __bf16 bf16x2;
typedef __attribute__((ext_vector_type(2))) float f32x2;
typedef __attribute__((ext_vector_type(4))) short s16x4;
typedef __attribute__((ext_vector_type(4))) float f32x4;
typedef __attribute__((ext_vector_type(4))) unsigned u32x4;
typedef __attribute__((ext_vector_type(2))) unsigned u32x2;
typedef __amdgpu_buffer_rsrc_t rsrc_t;

#define STAMPS 1
#define STAMP_N 16
#define STAMP_OFF (64u<<20)
#define STAMP(k) do{ ts[k]=(unsigned)__builtin_amdgcn_s_memrealtime(); }while(0)

constexpr int SEQ = 2048, DM = 512;
constexpr int SLOT = 32768;
constexpr int T2_SLOT = 43008;
constexpr int T2_BASE = 32768;

constexpr unsigned WS_FLAGS = 0;
constexpr unsigned WS_ABT   = 1u << 20;
constexpr unsigned WS_O     = WS_ABT + (4u << 20);
constexpr unsigned WS_PART  = WS_O + (2u << 20);
constexpr int FW = 32;
#define R1 16
#define PSL 1
constexpr int F1_IDX = 8, F2_IDX = F1_IDX + 16 * R1, F3_IDX = F2_IDX + 16, NCNT = (16 * R1 + 16 + 256) / 8;
constexpr int NT = 0;
#define NE 3
constexpr unsigned SPIN_CAP = 1u << 17;

__device__ __forceinline__ rsrc_t mk_rsrc(const void* p, unsigned bytes) {
    return __builtin_amdgcn_make_buffer_rsrc((void*)p, 0, bytes, 0x00020000);
}
template <int AUX>
__device__ __forceinline__ void glds(rsrc_t r, LAS char* dst, int voff, int soff) {
    __builtin_amdgcn_raw_ptr_buffer_load_lds(r, (LAS void*)dst, 16, voff, soff, 0, AUX);
}
template <int N> __device__ __forceinline__ void bar_vm() {
    asm volatile("s_waitcnt vmcnt(%0)\n\ts_barrier" ::"n"(N) : "memory");
}
__device__ __forceinline__ void bar_only() { asm volatile("s_barrier" ::: "memory"); }
__device__ __forceinline__ void bar_lgkm() {
    asm volatile("s_waitcnt lgkmcnt(0)\n\ts_barrier" ::: "memory");
}
__device__ __forceinline__ void drain_vm() { asm volatile("s_waitcnt vmcnt(0)" ::: "memory"); }
__device__ __forceinline__ f32x4 mfma32(bf16x8 a, bf16x8 b, f32x4 c) {
    return __builtin_amdgcn_mfma_f32_16x16x32_bf16(a, b, c, 0, 0, 0);
}
__device__ __forceinline__ f32x4 mfma16(u32x2 a, u32x2 b, f32x4 c) {
    return __builtin_amdgcn_mfma_f32_16x16x16bf16_1k(__builtin_bit_cast(s16x4, a), __builtin_bit_cast(s16x4, b), c, 0, 0, 0);
}
__device__ __forceinline__ bf16x8 lds_b128(LAS char* p) { return *(LAS bf16x8*)p; }
__device__ __forceinline__ bf16x8 lds_tr2(LAS char* p) {
    bf16x4 t0 = __builtin_amdgcn_ds_read_tr16_b64_v4bf16((LAS bf16x4*)p);
    bf16x4 t1 = __builtin_amdgcn_ds_read_tr16_b64_v4bf16((LAS bf16x4*)(p + 512));
    return __builtin_shufflevector(t0, t1, 0, 1, 2, 3, 4, 5, 6, 7);
}
__device__ __forceinline__ unsigned cvtpk(float a, float b) {
    f32x2 v = {a, b};
    unsigned u = __builtin_bit_cast(unsigned, __builtin_convertvector(v, bf16x2));
    asm("" : "+v"(u));
    return u;
}
__device__ __forceinline__ u32x2 cvt4u(f32x4 v) { u32x2 r = {cvtpk(v[0], v[1]), cvtpk(v[2], v[3])}; return r; }
__device__ __forceinline__ u32x4 pack8(f32x4 lo, f32x4 hi) {
    u32x4 r = {cvtpk(lo[0], lo[1]), cvtpk(lo[2], lo[3]), cvtpk(hi[0], hi[1]), cvtpk(hi[2], hi[3])};
    return r;
}
__device__ __forceinline__ void unpack8_add(u32x4 v, f32x4& lo, f32x4& hi) {
    unsigned a = v[0], b = v[1], c = v[2], d = v[3];
    lo[0] += __uint_as_float(a << 16); lo[1] += __uint_as_float(a & 0xffff0000u);
    lo[2] += __uint_as_float(b << 16); lo[3] += __uint_as_float(b & 0xffff0000u);
    hi[0] += __uint_as_float(c << 16); hi[1] += __uint_as_float(c & 0xffff0000u);
    hi[2] += __uint_as_float(d << 16); hi[3] += __uint_as_float(d & 0xffff0000u);
}
__device__ __forceinline__ float fmx(float a, float b) { return __builtin_elementwise_maximum(a, b); }
__device__ __forceinline__ void swap16(float& a, float& b) {
    auto r = __builtin_amdgcn_permlane16_swap(__float_as_uint(a), __float_as_uint(b), false, false);
    unsigned x = r[0], y = r[1]; a = __uint_as_float(x); b = __uint_as_float(y);
}
__device__ __forceinline__ void swap32(float& a, float& b) {
    auto r = __builtin_amdgcn_permlane32_swap(__float_as_uint(a), __float_as_uint(b), false, false);
    unsigned x = r[0], y = r[1]; a = __uint_as_float(x); b = __uint_as_float(y);
}
template <bool MAX> __device__ __forceinline__ float op2(float a, float b) { return MAX ? fmx(a, b) : a + b; }
template <bool MAX> __device__ __forceinline__ void allred4(float* v) {
    swap16(v[0], v[1]); float c = op2<MAX>(v[0], v[1]);
    swap16(v[2], v[3]); float d = op2<MAX>(v[2], v[3]);
    swap32(c, d); float e = op2<MAX>(c, d);
    if (!MAX) e = __builtin_amdgcn_rcpf(e);
    float e2 = e; swap32(e, e2);
    float f = e; swap16(e, f);
    float h = e2; swap16(e2, h);
    v[0] = e; v[1] = f; v[2] = e2; v[3] = h;
}
__device__ __forceinline__ unsigned ld_flag(GAS unsigned* f) {
    return __builtin_amdgcn_readfirstlane(__hip_atomic_load(f, __ATOMIC_RELAXED, __HIP_MEMORY_SCOPE_AGENT));
}
__device__ __forceinline__ void add_flag(GAS unsigned* f, int lane) {
    if (lane == 0) (void)__hip_atomic_fetch_add(f, 1u, __ATOMIC_RELAXED, __HIP_MEMORY_SCOPE_AGENT);
}
#define LD_FLAG_ASYNC(REG, f) asm volatile("global_load_dword " REG ", %0, off sc1" ::"v"(f) : "memory", REG)
#define RD_FLAG(REG, out) asm volatile("v_readfirstlane_b32 %0, " REG : "=s"(out)::"memory")
#define RD_VEC(REG, out) asm volatile("v_mov_b32 %0, " REG : "=v"(out)::"memory")
__device__ __forceinline__ void add_flag_async(GAS unsigned* f, int lane) {
    unsigned one = 1u;
    if (lane == 0) asm volatile("global_atomic_add %0, %1, off" ::"v"(f), "v"(one) : "memory");
}
template <int SLP = 1>
__device__ __forceinline__ void wait_ge(GAS unsigned* f, unsigned target, GAS unsigned* hb, int lane) {
    unsigned spins = 0;
    while (ld_flag(f) < target) {
        ++spins;
        if ((spins & 127u) == 0u) {
            if (lane < 8) (void)__hip_atomic_fetch_add(hb + lane * FW, 1u, __ATOMIC_RELAXED, __HIP_MEMORY_SCOPE_AGENT);
            if (spins > SPIN_CAP) break;
        }
        __builtin_amdgcn_s_sleep(SLP);
    }
    asm volatile("" ::: "memory");
}
struct Frag { bf16x8 a[4], b[4]; };
#define SGB() do { _Pragma("unroll") for (int _i = 0; _i < 8; ++_i) { __builtin_amdgcn_sched_group_barrier(0x8, 1, 0); __builtin_amdgcn_sched_group_barrier(0x100, 1, 0); } __builtin_amdgcn_sched_group_barrier(0x8, 8, 0); } while (0)
__device__ __forceinline__ void nt_read(Frag& f, LAS char* pa, LAS char* pb) {
    f.a[0] = lds_b128(pa);
#pragma unroll
    for (int n = 0; n < 4; ++n) f.b[n] = lds_b128(pb + n * 2048);
#pragma unroll
    for (int m = 1; m < 4; ++m) f.a[m] = lds_b128(pa + m * 2048);
}
__device__ __forceinline__ void nt_mma(f32x4 (&acc)[4][4], const Frag& f) {
#pragma unroll
    for (int m = 0; m < 4; ++m)
#pragma unroll
        for (int n = 0; n < 4; ++n) acc[m][n] = mfma32(f.a[m], f.b[n], acc[m][n]);
}
__device__ __forceinline__ void bar_cons() {
    asm volatile("" ::: "memory");
    __builtin_amdgcn_s_waitcnt(0x0070);
    __builtin_amdgcn_s_barrier();
    asm volatile("" ::: "memory");
}
#define SGBV() do { _Pragma("unroll") for (int _i = 0; _i < 8; ++_i) { __builtin_amdgcn_sched_group_barrier(0x8, 1, 0); __builtin_amdgcn_sched_group_barrier(0x100, 1, 0); } \
    _Pragma("unroll") for (int _i = 0; _i < 2; ++_i) { __builtin_amdgcn_sched_group_barrier(0x8, 1, 0); __builtin_amdgcn_sched_group_barrier(0x4, 2, 0); } \
    _Pragma("unroll") for (int _i = 0; _i < 2; ++_i) { __builtin_amdgcn_sched_group_barrier(0x8, 1, 0); __builtin_amdgcn_sched_group_barrier(0x2, 1, 0); } \
    __builtin_amdgcn_sched_group_barrier(0x8, 4, 0); } while (0)
__device__ __forceinline__ void nt_pin(f32x4 (&acc)[4][4]) {
    asm volatile("" : "+v"(acc[0][0]), "+v"(acc[0][1]), "+v"(acc[0][2]), "+v"(acc[0][3]), "+v"(acc[1][0]), "+v"(acc[1][1]), "+v"(acc[1][2]), "+v"(acc[1][3]));
    asm volatile("" : "+v"(acc[2][0]), "+v"(acc[2][1]), "+v"(acc[2][2]), "+v"(acc[2][3]), "+v"(acc[3][0]), "+v"(acc[3][1]), "+v"(acc[3][2]), "+v"(acc[3][3]));
}
template <bool LASTBAR>
__device__ __forceinline__ int nt_consume(f32x4 (&acc)[4][4], LAS char* smem, int slot, int nst, int w, int lane) {
    const int r = lane & 15, g = lane >> 4;
    const int c0 = ((g ^ (r >> 1)) << 4), c1 = c0 ^ 64;
    const int oa = ((w & 1) * 64 + r) * 128, ob = 16384 + ((w >> 1) * 64 + r) * 128;
    const int oa0 = oa + c0, ob0 = ob + c0, oa1 = oa + c1, ob1 = ob + c1;
    Frag f0, f1;
    int so = slot * SLOT;
    nt_read(f0, smem + so + oa0, smem + so + ob0);
    int pa = so + oa1, pb = so + ob1;
#pragma unroll 1
    for (int it = 0; it < nst - 1; ++it) {
        __builtin_amdgcn_sched_barrier(0);
        nt_read(f1, smem + pa, smem + pb);
        nt_mma(acc, f0);
        so = (so == 4 * SLOT) ? 0 : so + SLOT;
        int qa = so + oa0, qb = so + ob0;
        SGBV();
        __builtin_amdgcn_sched_barrier(0);
        asm volatile("" : "+v"(qa), "+v"(qb));
        bar_cons();
        __builtin_amdgcn_sched_barrier(0);
        nt_read(f0, smem + qa, smem + qb);
        nt_mma(acc, f1);
        pa = so + oa1; pb = so + ob1;
        SGBV();
        __builtin_amdgcn_sched_barrier(0);
        asm volatile("" : "+v"(pa), "+v"(pb));
    }
    nt_read(f1, smem + pa, smem + pb);
    nt_mma(acc, f0);
    SGB();
    __builtin_amdgcn_sched_barrier(0);
    so = (so == 4 * SLOT) ? 0 : so + SLOT;
    if (LASTBAR) bar_cons();
    __builtin_amdgcn_sched_barrier(0);
    nt_mma(acc, f1);
    return so / SLOT;
}

#define PIN4(a, b, c, d) asm volatile("" : "+v"(a), "+v"(b), "+v"(c), "+v"(d))
__device__ __forceinline__ void t2_pin(f32x4 (&aq)[8], f32x4 (&ak)[9], f32x4 (&av)[9]) {
    asm volatile("" : "+v"(aq[0]), "+v"(aq[1]), "+v"(aq[2]), "+v"(aq[3]), "+v"(aq[4]), "+v"(aq[5]), "+v"(aq[6]), "+v"(aq[7]),
                      "+v"(ak[0]), "+v"(ak[1]), "+v"(ak[2]), "+v"(ak[3]), "+v"(ak[4]));
    asm volatile("" : "+v"(ak[5]), "+v"(ak[6]), "+v"(ak[7]), "+v"(ak[8]),
                      "+v"(av[0]), "+v"(av[1]), "+v"(av[2]), "+v"(av[3]), "+v"(av[4]), "+v"(av[5]), "+v"(av[6]), "+v"(av[7]), "+v"(av[8]));
}
struct Frag2 { bf16x8 x[9], w[3]; };
__device__ __forceinline__ void t2_read(Frag2& f, LAS char* px, LAS char* pw) {
    f.w[1] = lds_tr2(pw + 1024);
    f.x[0] = lds_b128(px);
    f.w[2] = lds_tr2(pw + 2048);
    f.w[0] = lds_tr2(pw);
#pragma unroll
    for (int rb = 1; rb < 9; ++rb) f.x[rb] = lds_b128(px + rb * 2048);
}
__device__ __forceinline__ void t2_mma(f32x4 (&aq)[8], f32x4 (&ak)[9], f32x4 (&av)[9], const Frag2& f) {
#pragma unroll
    for (int rb = 0; rb < 9; ++rb) {
        ak[rb] = mfma32(f.w[1], f.x[rb], ak[rb]);
        av[rb] = mfma32(f.x[rb], f.w[2], av[rb]);
        if (rb > 0) aq[rb - 1] = mfma32(f.w[0], f.x[rb], aq[rb - 1]);
    }
}
#define SGB2() do { _Pragma("unroll") for (int _i = 0; _i < 15; ++_i) { __builtin_amdgcn_sched_group_barrier(0x8, 1, 0); __builtin_amdgcn_sched_group_barrier(0x100, 1, 0); } \
    _Pragma("unroll") for (int _i = 0; _i < 2; ++_i) { __builtin_amdgcn_sched_group_barrier(0x8, 1, 0); __builtin_amdgcn_sched_group_barrier(0x4, 2, 0); } \
    _Pragma("unroll") for (int _i = 0; _i < 2; ++_i) { __builtin_amdgcn_sched_group_barrier(0x8, 1, 0); __builtin_amdgcn_sched_group_barrier(0x2, 1, 0); } \
    __builtin_amdgcn_sched_group_barrier(0x8, 7, 0); } while (0)

struct Frag4 { bf16x8 w[4][2], o[4][2]; };
#define SGB4() do { _Pragma("unroll") for (int _i = 0; _i < 12; ++_i) { __builtin_amdgcn_sched_group_barrier(0x100, 2, 0); __builtin_amdgcn_sched_group_barrier(0x8, 1, 0); } __builtin_amdgcn_sched_group_barrier(0x8, 4, 0); } while (0)
__device__ __forceinline__ void pf(rsrc_t r, int voff, int soff) {
    asm volatile("s_nop 4\n\tbuffer_load_dword v255, %0, %1, %2 offen" ::"v"(voff), "s"(r), "s"(__builtin_amdgcn_readfirstlane(soff)) : "memory", "v255");
}

__global__ __launch_bounds__(512, 2) void fused(const __bf16* __restrict__ xa, const __bf16* __restrict__ cre, const __bf16* __restrict__ cim,
                                                const __bf16* __restrict__ hre, const __bf16* __restrict__ him, const __bf16* __restrict__ xb,
                                                const __bf16* __restrict__ wqkv, const __bf16* __restrict__ wo,
                                                char* __restrict__ ws, __bf16* __restrict__ out) {
    __shared__ __attribute__((aligned(1024))) char smem_[163840];
    LAS char* smem = (LAS char*)smem_;
    unsigned long long tent = __builtin_amdgcn_s_memrealtime();
    unsigned ts[16] = {};
    asm volatile("s_load_dword s98, %0, 0x0\n\ts_load_dword s99, %0, 0x40" ::"s"(__builtin_amdgcn_kernarg_segment_ptr()) : "s98", "s99");
    const int tid = threadIdx.x, lane = tid & 63;
    const int wave = __builtin_amdgcn_readfirstlane(tid >> 6);
    const int bid = blockIdx.x, xcd = bid & 7, idx = bid >> 3;
    const int q = xcd & 3, th = xcd >> 2;
    const int ttl = idx >> 2, et = idx & 3, tt = th * 8 + ttl;
    const int t0 = tt * 128, e0 = et * 128;
    const bool typeA = (ttl & 1) == 0;
    const int pg = q;
    const int s1 = typeA ? ((q & 1) * 8 + th * 4 + (ttl >> 1)) * 128 : ((xcd >> 1) * 4 + (ttl >> 1)) * 128;
    const int hg = (xcd & 1) * 4 + et;
    const int s4 = (xcd * 4 + (idx >> 3)) * 64, n4 = (idx & 7) * 64;
    GAS unsigned* flags = (GAS unsigned*)(ws + WS_FLAGS);
    const int slot3 = typeA ? 3 : 0;
#define ROT1 (4 * (((ttl >> 1) + et) & 1))
#define ROT3 (4 * et)
    const int rot1 = ROT1, rot3 = ROT3;

    if (wave == 1 && ttl == 1) {
        unsigned long long pc; int off, len;
        asm volatile("s_getpc_b64 %0\n.Lpf_here:\n\ts_mov_b32 %1, .Lpf_here-_Z5fusedPKDF16bS0_S0_S0_S0_S0_S0_S0_PcPDF16b\n\ts_mov_b32 %2, _Z8code_endPj-_Z5fusedPKDF16bS0_S0_S0_S0_S0_S0_S0_PcPDF16b" : "=s"(pc), "=s"(off), "=s"(len));
        const int o = (et * 64 + lane) * 128;
        if (o < len) { unsigned v = *(const unsigned*)(pc - (unsigned)off + o); asm volatile("" ::"v"(v)); }
    }
    if (__builtin_expect(wave >= 6, 1)) {
        const int lw = wave - 6;
        const int row = 8 * lw + (lane >> 3);
        const int ch = ((lane & 7) ^ (4 * lw + (lane >> 4))) * 16;
        const int cht = ((lane & 7) ^ (2 * (((lane >> 4) & 1) | (lw << 1)))) * 16;
        const bool master = (bid < 8 && lw == 0);
        GAS unsigned* myhb = flags + bid * FW;
        if (master) {
#pragma unroll
            for (int i = 0; i < NCNT; i += 64)
                if (i + lane < NCNT) __hip_atomic_store(flags + (8 + bid * NCNT + i + lane) * FW, 0u, __ATOMIC_RELAXED, __HIP_MEMORY_SCOPE_AGENT);
        }
        rsrc_t rc = mk_rsrc((q >> 1) ? cim : cre, SEQ * SEQ * 2);
        const int vb = row * 4096 + ch;
        const int sb = t0 * 4096 + (q & 1) * 2048;
        auto issueC = [&](int st, int slot) {
            const int so = sb + ((st + rot3) & 15) * 128;
#pragma unroll
            for (int u = 0; u < 8; ++u) glds<NT>(rc, smem + slot * SLOT + 16384 + (lw + 2 * u) * 1024, vb, so + 16 * ((u & 4) | ((u + q) & 3)) * 4096);
        };
        rsrc_t rw = mk_rsrc(wo, DM * DM * 2);
        int slot = 0;
        if (typeA) {
            rsrc_t rh = mk_rsrc((pg >> 1) ? him : hre, DM * DM * 2);
            const int vh = row * 1024 + ch;
#pragma unroll 1
            for (int st = 0; st < 8; ++st) {
                if (st >= 2) bar_vm<8>();
                if (master && st >= 2 && st < 5) add_flag_async(myhb, lane);
                const int so = e0 * 1024 + ((st + rot1) & 7) * 128;
#pragma unroll
                for (int u = 0; u < 8; ++u) glds<NT>(rh, smem + slot * SLOT + 16384 + (lw + 2 * u) * 1024, vh, so + 16 * u * 1024);
                slot = (slot + 1 == 5) ? 0 : slot + 1;
            }
        } else {
            rsrc_t rq = mk_rsrc(wqkv, DM * 3 * DM * 2);
            const int vq = row * 3072 + cht;
            int pos = 1;
#pragma unroll 1
            for (int st = 0; st < 8; ++st) {
                if (st >= 2) bar_vm<12>();
                LAS char* sl = smem + T2_BASE + pos * T2_SLOT;
                const int so = ((st + rot1) & 7) * 64 * 3072 + hg * 128;
#pragma unroll
                for (int v = 0; v < 4; ++v)
#pragma unroll
                    for (int cg = 0; cg < 3; ++cg)
                        glds<NT>(rq, sl + 18432 + ((lw + 2 * v) * 3 + cg) * 1024, vq, so + 16 * v * 3072 + cg * 1024);
                pos = (pos + 1 == 3) ? 0 : pos + 1;
            }
        }
#pragma unroll 1
        for (int st = 0; st < 16; ++st) {
            if (typeA) {
                if (st == 2) { bar_only(); bar_only(); }
                bar_vm<8>();
            } else if (st < 5) {
                if (st == 1) bar_vm<20>();
                if (st == 2) bar_vm<16>();
                if (st == 3) bar_only();
            } else {
                if (st == 5) { bar_vm<32>(); bar_vm<24>(); bar_vm<16>(); }
                bar_vm<8>();
            }
            issueC(st, slot);
            slot = (slot + 1 == 5) ? 0 : slot + 1;
            if (st == 7) pf(rw, lane * 128, idx * 16384 + lw * 8192);
        }
        const int vw = row * 1024 + cht;
#pragma unroll 1
        for (int st = 0; st < 4; ++st) {
            if (st < 2) bar_vm<8>();
            if (st == 2) bar_vm<0>();
#pragma unroll
            for (int h = 0; h < 2; ++h)
#pragma unroll
                for (int u = 0; u < 4; ++u)
                    glds<NT>(rw, smem + slot * SLOT + h * 16384 + 8192 + (lw + 2 * u) * 1024, vw, ((st * 2 + h) * 64 + 16 * u) * 1024 + n4 * 2);
            slot = (slot + 1 == 5) ? 0 : slot + 1;
        }
        bar_vm<0>();
    } else if (__builtin_expect(wave >= 4, 1)) {
        const int lw = wave - 4;
        const int ch = ((lane & 7) ^ (4 * lw + (lane >> 4))) * 16;
        const int prow = 8 * (lane >> 5) + ((lane >> 3) & 3) + 16 * lw;
        const int row = 8 * lw + (lane >> 3);
        GAS unsigned* hbp = flags + (lane & 7) * FW;
        if (!typeA) {
            rsrc_t rx = mk_rsrc(xb, SEQ * DM * 2);
            const int vx = row * 1024 + ch;
            const int sxm = s1 * 1024, sxh = (s1 >= 16 ? s1 - 16 : 0) * 1024;
            int pos = 1;
#pragma unroll 1
            for (int st = 0; st < 8; ++st) {
                if (st >= 2) bar_vm<9>();
                if (st == 1 && lw == 0) LD_FLAG_ASYNC("v254", hbp);
                if (st == 7 && lw == 0) LD_FLAG_ASYNC("v253", hbp);
                LAS char* sl = smem + T2_BASE + pos * T2_SLOT;
                const int ko = ((st + rot1) & 7) * 128;
                glds<NT>(rx, sl + lw * 1024, vx, sxh + ko);
#pragma unroll
                for (int u = 0; u < 8; ++u) glds<NT>(rx, sl + 2048 + (lw + 2 * u) * 1024, vx, sxm + 16 * u * 1024 + ko);
                pos = (pos + 1 == 3) ? 0 : pos + 1;
            }
            bar_vm<9>();
        }
        rsrc_t ro = mk_rsrc(ws + WS_O, SEQ * DM * 2);
        const int vo = row * 32 + (ch & 16) + (ch >> 5) * 65536;
        GAS unsigned* f2 = flags + (F2_IDX + (s4 >> 7)) * FW;
        int slot = 0;
#pragma unroll 1
        for (int pass = typeA ? 0 : 1; pass < 2; ++pass) {
            if (pass) {
                bar_vm<0>();
                if (lw == 0) {
                    unsigned a, b, spins = 0;
                    RD_VEC("v254", a); RD_VEC("v253", b);
                    bool moved = (a != b) || ((lane & 7) == bid);
                    while (!__all(moved)) {
                        if (bid < 8) add_flag(flags + bid * FW, lane);
                        __builtin_amdgcn_s_sleep(1);
                        b = __hip_atomic_load(hbp, __ATOMIC_RELAXED, __HIP_MEMORY_SCOPE_AGENT);
                        moved = moved || (a != b);
                        if (++spins > SPIN_CAP) break;
                    }
                }
                bar_only();
                if (typeA) {
                    bar_only();
                    if (lw == 0 && lane < R1) (void)__hip_atomic_fetch_add(flags + (F1_IDX + (pg * 4 + et) * R1 + lane) * FW, 1u, __ATOMIC_RELAXED, __HIP_MEMORY_SCOPE_AGENT);
                }
                wait_ge<PSL>(flags + (F1_IDX + (q * 4 + et) * R1 + ((lw + 2 * th + 4 * (ttl >> 1) + 16 * (ttl & 1)) & (R1 - 1))) * FW, 8u, flags, lane);
                STAMP(6);
            }
            rsrc_t ra = mk_rsrc(pass ? (const void*)(ws + WS_ABT) : (const void*)xa, pass ? 1024 * SEQ * 2 : SEQ * DM * 2);
            const int va = pass ? prow * 64 + (ch & 48) + (ch >> 6) * 8192 : prow * 1024 + ch;
            const int sa = pass ? (((q >> 1) * 4 + et) * 64 + (q & 1) * 32) * 8192 : s1 * 1024;
            const int rot = pass ? rot3 : rot1, nst = pass ? 16 : 8, ksh = pass ? 14 : 7, rb = pass ? 64 : 1024;
#pragma unroll 1
            for (int st = 0; st < nst; ++st) {
                if (st >= 2) bar_vm<8>();
                if (!pass) {
                    if (st == 1 && lw == 0) LD_FLAG_ASYNC("v254", hbp);
                    if (st == 7 && lw == 0) LD_FLAG_ASYNC("v253", hbp);
                } else if (st == 3 && lw == 1 && !typeA) add_flag_async(flags + (F2_IDX + (s1 >> 7)) * FW, lane);
                const int so = sa + (((st + rot) & (nst - 1)) << ksh);
#pragma unroll
                for (int u = 0; u < 8; ++u) glds<16>(ra, smem + slot * SLOT + (lw + 2 * u) * 1024, va, so + (32 * (u >> 1) + 4 * (u & 1)) * rb);
                slot = (slot + 1 == 5) ? 0 : slot + 1;
                if (st == 13) LD_FLAG_ASYNC("v252", f2);
            }
            if (!pass) bar_vm<8>();
        }
#pragma unroll 1
        for (int st = 0; st < 4; ++st) {
            if (st < 2) bar_vm<8>();
            if (st == 2) bar_vm<0>();
            if (st == 0) {
                unsigned f2v;
                RD_FLAG("v252", f2v);
                if (f2v < 8u) wait_ge(f2, 8u, flags, lane);
            }
#pragma unroll
            for (int h = 0; h < 2; ++h)
#pragma unroll
                for (int u = 0; u < 4; ++u)
                    glds<16>(ro, smem + slot * SLOT + h * 16384 + (lw + 2 * u) * 1024, vo, (s4 + 16 * u) * 32 + (st * 2 + h) * 4 * 65536);
            slot = (slot + 1 == 5) ? 0 : slot + 1;
        }
        bar_vm<0>();
    } else {
        const int r = lane & 15, g = lane >> 4;
        STAMP(8);
        const int tile = tt * 4 + et;
        rsrc_t rp = mk_rsrc(ws + WS_PART, 64 * 16 * 4 * 2048);
        const int inb = (tile * 4 + q) * 32768 + wave * 2048 + lane * 16;
        if (typeA) {
            f32x4 acc[4][4] = {};
            nt_pin(acc);
            bar_lgkm();
            STAMP(1);
            nt_consume<true>(acc, smem, 0, 8, wave, lane);
            STAMP(2);
            const unsigned sg = (pg >> 1) ? 0x80008000u : 0u;
            rsrc_t rs = mk_rsrc(ws + WS_ABT, 1024 * SEQ * 2);
            const int vs = r * 64 + g * 16;
            const int ss = (((pg >> 1) * 4 + et) * 64 + (s1 >> 5) + (wave & 1) * 2) * 8192 + (wave >> 1) * 4096;
#pragma unroll
            for (int n = 0; n < 4; ++n)
#pragma unroll
                for (int u = 0; u < 2; ++u)
                    __builtin_amdgcn_raw_buffer_store_b128(pack8(acc[2 * u][n], acc[2 * u + 1][n]) ^ sg, rs, vs + ss + n * 1024 + u * 8192, 0, 16);
            STAMP(7);
            bar_vm<0>();
            STAMP(3);
        } else {
            const int c0 = ((g ^ (r >> 1)) << 4);
            const int ox = r * 128;
            const int qq = (lane & 15) >> 2, p = lane & 3;
            const int ow = 18432 + g * 3072 + qq * 128 + (((2 * wave + (p >> 1)) ^ (2 * ((qq >> 1) | ((g & 1) << 1)))) << 4) + 8 * (p & 1);
            f32x4 aq[8] = {}, ak[9] = {}, av[9] = {};
            Frag2 f0, f1;
            int so = T2_BASE + T2_SLOT;
            t2_pin(aq, ak, av);
            bar_lgkm();
            STAMP(1);
            t2_read(f0, smem + so + ox + c0, smem + so + ow);
            int px = so + ox + (c0 ^ 64), pw = so + ow;
#pragma unroll 1
            for (int it = 0; it < 7; ++it) {
                __builtin_amdgcn_sched_barrier(0);
                t2_read(f1, smem + px, smem + pw + 12288);
                t2_mma(aq, ak, av, f0);
                so = (so == T2_BASE + 2 * T2_SLOT) ? T2_BASE : so + T2_SLOT;
                int qx = so + ox + c0, qw = so + ow;
                SGB2();
                __builtin_amdgcn_sched_barrier(0);
                asm volatile("" : "+v"(qx), "+v"(qw));
                t2_pin(aq, ak, av);
                bar_cons();
                __builtin_amdgcn_sched_barrier(0);
                t2_read(f0, smem + qx, smem + qw);
                t2_mma(aq, ak, av, f1);
                px = qx ^ 64; pw = qw;
                SGB2();
                __builtin_amdgcn_sched_barrier(0);
                asm volatile("" : "+v"(px));
            }
            t2_read(f1, smem + px, smem + pw + 12288);
            t2_mma(aq, ak, av, f0);
            SGB2();
            __builtin_amdgcn_sched_barrier(0);
            t2_pin(aq, ak, av);
            bar_cons();
            __builtin_amdgcn_sched_barrier(0);
            t2_mma(aq, ak, av, f1);
            STAMP(4);
            const int j = r, par = j & 1;
            const bool uc0 = (4 * g + par) <= j, uc1 = (4 * g + par + 2) <= j;
            const unsigned s0v = par ? 0x01000c0cu : 0x0c0c0100u, s1v = par ? 0x03020c0cu : 0x0c0c0302u, zs = 0x0c0c0c0cu;
            const unsigned selc0 = uc0 ? s0v : zs, selp0 = uc0 ? zs : s0v, selc1 = uc1 ? s1v : zs, selp1 = uc1 ? zs : s1v;
            rsrc_t rs = mk_rsrc(ws + WS_O, SEQ * DM * 2);
            const int vs = j * 32 + g * 8;
            const int ss = (hg * 4 + wave) * 65536 + s1 * 32;
            const f32x4 z = {0.f, 0.f, 0.f, 0.f};
            const float C = 0.25f * 1.44269504088896f;
            u32x2 kb[9], vb[9];
            float l0[8], l1[8], m[8];
#pragma unroll
            for (int i = 0; i < 9; ++i) kb[i] = cvt4u(ak[i]);
#pragma unroll
            for (int b = 0; b < 8; ++b) {
                u32x2 qb = cvt4u(aq[b]);
                f32x4 sc = mfma16(kb[b + 1], qb, z), sp = mfma16(kb[b], qb, z);
                float c0v = par ? sc[1] : sc[0], c1v = par ? sc[3] : sc[2];
                float p0v = par ? sp[1] : sp[0], p1v = par ? sp[3] : sp[2];
                if (b == 0 && s1 == 0) { p0v = -4e9f; p1v = -4e9f; }
                unsigned pk = cvtpk(uc0 ? c0v : p0v, uc1 ? c1v : p1v);
                l0[b] = __uint_as_float(pk << 16); l1[b] = __uint_as_float(pk & 0xffff0000u);
                m[b] = fmx(l0[b], l1[b]);
            }
            allred4<true>(m); allred4<true>(m + 4);
#pragma unroll
            for (int i = 0; i < 9; ++i) vb[i] = cvt4u(av[i]);
#pragma unroll
            for (int b = 0; b < 8; ++b) {
                l0[b] = __builtin_amdgcn_exp2f((l0[b] - m[b]) * C); l1[b] = __builtin_amdgcn_exp2f((l1[b] - m[b]) * C);
                m[b] = l0[b] + l1[b];
            }
            allred4<false>(m); allred4<false>(m + 4);
            f32x4 ov[8];
#pragma unroll
            for (int b = 0; b < 8; ++b) {
                unsigned P = cvtpk(l0[b] * m[b], l1[b] * m[b]);
                u32x2 pc = {__builtin_amdgcn_perm(0u, P, selc0), __builtin_amdgcn_perm(0u, P, selc1)};
                u32x2 pp = {__builtin_amdgcn_perm(0u, P, selp0), __builtin_amdgcn_perm(0u, P, selp1)};
                ov[b] = mfma16(vb[b], pp, mfma16(vb[b + 1], pc, z));
            }
#pragma unroll
            for (int b = 0; b < 8; ++b) __builtin_amdgcn_raw_buffer_store_b64(cvt4u(ov[b]), rs, vs, ss + b * 512, 16);
            STAMP(5);
        }
        f32x4 acc[4][4] = {};
        nt_pin(acc);
        bar_lgkm();
        STAMP(9);
        unsigned long long cy0 = __builtin_amdgcn_s_memtime();
        int slot = nt_consume<false>(acc, smem, slot3, 16, wave, lane);
        STAMP(10);
        unsigned long long cy1 = __builtin_amdgcn_s_memtime();
        u32x4 sv[3][2];
#pragma unroll
        for (int n = 1; n < 4; ++n) { sv[n - 1][0] = pack8(acc[0][n], acc[1][n]); sv[n - 1][1] = pack8(acc[2][n], acc[3][n]); }
        auto send = [&](int n) {
            const int base = (((tile * 4 + ((q + n) & 3)) * 4 + q) * 4 + wave) * 2048 + lane * 16;
            __builtin_amdgcn_raw_buffer_store_b128(sv[n - 1][0], rp, base, 0, 16);
            __builtin_amdgcn_raw_buffer_store_b128(sv[n - 1][1], rp, base + 1024, 0, 16);
        };
        send(1); send(2); send(3);
        f32x4 own[4];
#pragma unroll
        for (int m = 0; m < 4; ++m) own[m] = acc[m][0];
        f32x4 a4[2][2] = {};
        rsrc_t rout = mk_rsrc(out, SEQ * 1024 * 2);
        u32x4 pv[3][2];
        {
            const int c0 = ((g ^ (r >> 1)) << 4);
            const int oo = ((wave >> 1) * 32 + r) * 128;
            const int qq = (lane & 15) >> 2, p = lane & 3;
            const int swz = 2 * ((qq >> 1) | ((g & 1) << 1));
            const int ow0 = 8192 + g * 1024 + qq * 128 + ((((wave & 1) * 4 + (p >> 1)) ^ swz) << 4) + 8 * (p & 1);
            const int ow1 = ow0 ^ 32;
            Frag4 h0, h1;
            auto rd = [&](Frag4& f) {
                LAS char* sl = smem + slot * SLOT;
#pragma unroll
                for (int i = 0; i < 4; ++i) {
                    const int ho = (i >> 1) * 16384, kk = i & 1;
                    f.w[i][0] = lds_tr2(sl + ho + ow0 + kk * 4096);
                    f.o[i][0] = lds_b128(sl + ho + oo + (c0 ^ (kk * 64)));
                    f.o[i][1] = lds_b128(sl + ho + oo + (c0 ^ (kk * 64)) + 2048);
                    f.w[i][1] = lds_tr2(sl + ho + ow1 + kk * 4096);
                }
                slot = (slot + 1 == 5) ? 0 : slot + 1;
            };
            auto mm = [&](const Frag4& f) {
#pragma unroll
                for (int i = 0; i < 4; ++i) {
                    a4[0][0] = mfma32(f.w[i][0], f.o[i][0], a4[0][0]);
                    a4[0][1] = mfma32(f.w[i][0], f.o[i][1], a4[0][1]);
                    a4[1][0] = mfma32(f.w[i][1], f.o[i][0], a4[1][0]);
                    a4[1][1] = mfma32(f.w[i][1], f.o[i][1], a4[1][1]);
                }
                asm volatile("" : "+v"(a4[0][0]), "+v"(a4[0][1]), "+v"(a4[1][0]), "+v"(a4[1][1]));
            };
            asm volatile("" : "+v"(own[0]), "+v"(own[1]), "+v"(own[2]), "+v"(own[3]));
            bar_only();
            __builtin_amdgcn_sched_barrier(0);
            rd(h0);
            __builtin_amdgcn_sched_barrier(0);
            rd(h1); mm(h0); SGB4();
            __builtin_amdgcn_sched_barrier(0);
            drain_vm();
            if (lane < 4 && lane != q)
                (void)__hip_atomic_fetch_add(flags + (F3_IDX + tile * 4 + lane) * FW, 1u, __ATOMIC_RELAXED, __HIP_MEMORY_SCOPE_AGENT);
            mm(h1);
            STAMP(15);
            wait_ge(flags + (F3_IDX + tile * 4 + q) * FW, 12u, flags, lane);
            STAMP(12);
#pragma unroll
            for (int k = 0; k < 3; ++k) {
                const int o = inb + ((q + 1 + k) & 3) * 8192;
                pv[k][0] = __builtin_amdgcn_raw_buffer_load_b128(rp, o, 0, 16);
                pv[k][1] = __builtin_amdgcn_raw_buffer_load_b128(rp, o + 1024, 0, 16);
            }
            bar_only();
            __builtin_amdgcn_sched_barrier(0);
            rd(h0);
            __builtin_amdgcn_sched_barrier(0);
            rd(h1); mm(h0); SGB4();
            __builtin_amdgcn_sched_barrier(0);
            mm(h1);
#pragma unroll
            for (int n = 0; n < 2; ++n) {
                u32x2 lo = cvt4u(a4[0][n]), hi = cvt4u(a4[1][n]);
                auto r0 = __builtin_amdgcn_permlane16_swap(lo[0], hi[0], false, false);
                auto r1 = __builtin_amdgcn_permlane16_swap(lo[1], hi[1], false, false);
                u32x4 v = {r0[0], r1[0], r0[1], r1[1]};
                __builtin_amdgcn_raw_buffer_store_b128(v, rout, ((s4 + (wave >> 1) * 32 + n * 16 + r) * 1024 + 512 + n4 + (wave & 1) * 32 + (g & 1) * 16 + (g >> 1) * 8) * 2, 0, OUTSC);
            }
        }
        STAMP(11);
#pragma unroll
        for (int k = 0; k < 3; ++k) {
            unpack8_add(pv[k][0], own[0], own[1]);
            unpack8_add(pv[k][1], own[2], own[3]);
        }
        const int trow = t0 + (wave >> 1) * 64 + q * 16 + r;
#pragma unroll
        for (int u = 0; u < 2; ++u)
            __builtin_amdgcn_raw_buffer_store_b128(pack8(own[2 * u], own[2 * u + 1]), rout, (trow * 1024 + e0 + (wave & 1) * 64 + 32 * u + 8 * g) * 2, 0, OUTSC);
        drain_vm();
        STAMP(13);
        ts[0] = (unsigned)tent;
        if (wave == 0 && bid == DBID && MULT > 0) {
            unsigned long long d = (unsigned long long)(ts[SB] - ts[SA]) * MULT, t = __builtin_amdgcn_s_memrealtime();
            if (d > 20000) d = 20000;
            while (__builtin_amdgcn_s_memrealtime() - t < d) __builtin_amdgcn_s_sleep(2);
        }
    }
}

__global__ void code_end(unsigned* p) { p[0] = 0; }

extern "C" void kernel_launch(void* const* d_in, const int* in_sizes, int n_in,
                              void* d_out, int out_size, void* d_ws, size_t ws_size,
                              hipStream_t stream) {
    fused<<<256, 512, 0, stream>>>((const __bf16*)d_in[0], (const __bf16*)d_in[1], (const __bf16*)d_in[2], (const __bf16*)d_in[3],
                                   (const __bf16*)d_in[4], (const __bf16*)d_in[5], (const __bf16*)d_in[6], (const __bf16*)d_in[7],
                                   (char*)d_ws, (__bf16*)d_out);
}
```

```cpp
#include <hip/hip_runtime.h>
#include <hip/hip_bf16.h>
#include <stdint.h>

#define LAS __attribute__((address_space(3)))
#define GAS __attribute__((address_space(1)))
typedef __attribute__((ext_vector_type(8))) __bf16 bf16x8;
typedef __attribute__((ext_vector_type(4))) __bf16 bf16x4;
typedef __attribute__((ext_vector_type(2))) __bf16 bf16x2;
typedef __attribute__((ext_vector_type(2))) float f32x2;
typedef __attribute__((ext_vector_type(4))) short s16x4;
typedef __attribute__((ext_vector_type(4))) float f32x4;
typedef __attribute__((ext_vector_type(4))) unsigned u32x4;
typedef __attribute__((ext_vector_type(2))) unsigned u32x2;
typedef __amdgpu_buffer_rsrc_t rsrc_t;

constexpr int SEQ = 2048, DM = 512;
constexpr int SLOT = 32768;
constexpr int T2_SLOT = 43008;
constexpr int T2_BASE = 32768;

constexpr unsigned WS_FLAGS = 0;
constexpr unsigned WS_ABT   = 1u << 20;
constexpr unsigned WS_O     = WS_ABT + (4u << 20);
constexpr unsigned WS_PART  = WS_O + (2u << 20);
constexpr int FW = 32;
constexpr int R1 = 16;
constexpr int F1_IDX = 8, F2_IDX = F1_IDX + 16 * R1, F3_IDX = F2_IDX + 16, NCNT = (16 * R1 + 16 + 256) / 8;
constexpr int SC1 = 16;
constexpr unsigned SPIN_CAP = 1u << 17;

__device__ __forceinline__ rsrc_t mk_rsrc(const void* p, unsigned bytes) {
    return __builtin_amdgcn_make_buffer_rsrc((void*)p, 0, bytes, 0x00020000);
}
template <int AUX>
__device__ __forceinline__ void glds(rsrc_t r, LAS char* dst, int voff, int soff) {
    __builtin_amdgcn_raw_ptr_buffer_load_lds(r, (LAS void*)dst, 16, voff, soff, 0, AUX);
}
template <int N> __device__ __forceinline__ void bar_vm() {
    asm volatile("s_waitcnt vmcnt(%0)\n\ts_barrier" ::"n"(N) : "memory");
}
__device__ __forceinline__ void bar_only() { asm volatile("s_barrier" ::: "memory"); }
__device__ __forceinline__ void bar_lgkm() {
    asm volatile("s_waitcnt lgkmcnt(0)\n\ts_barrier" ::: "memory");
}
__device__ __forceinline__ void drain_vm() { asm volatile("s_waitcnt vmcnt(0)" ::: "memory"); }
__device__ __forceinline__ f32x4 mfma32(bf16x8 a, bf16x8 b, f32x4 c) {
    return __builtin_amdgcn_mfma_f32_16x16x32_bf16(a, b, c, 0, 0, 0);
}
__device__ __forceinline__ f32x4 mfma16(u32x2 a, u32x2 b, f32x4 c) {
    return __builtin_amdgcn_mfma_f32_16x16x16bf16_1k(__builtin_bit_cast(s16x4, a), __builtin_bit_cast(s16x4, b), c, 0, 0, 0);
}
__device__ __forceinline__ bf16x8 lds_b128(LAS char* p) { return *(LAS bf16x8*)p; }
__device__ __forceinline__ bf16x8 lds_tr2(LAS char* p) {
    bf16x4 t0 = __builtin_amdgcn_ds_read_tr16_b64_v4bf16((LAS bf16x4*)p);
    bf16x4 t1 = __builtin_amdgcn_ds_read_tr16_b64_v4bf16((LAS bf16x4*)(p + 512));
    return __builtin_shufflevector(t0, t1, 0, 1, 2, 3, 4, 5, 6, 7);
}
__device__ __forceinline__ unsigned cvtpk(float a, float b) {
    f32x2 v = {a, b};
    unsigned u = __builtin_bit_cast(unsigned, __builtin_convertvector(v, bf16x2));
    asm("" : "+v"(u));
    return u;
}
__device__ __forceinline__ u32x2 cvt4u(f32x4 v) { u32x2 r = {cvtpk(v[0], v[1]), cvtpk(v[2], v[3])}; return r; }
__device__ __forceinline__ u32x4 pack8(f32x4 lo, f32x4 hi) {
    u32x4 r = {cvtpk(lo[0], lo[1]), cvtpk(lo[2], lo[3]), cvtpk(hi[0], hi[1]), cvtpk(hi[2], hi[3])};
    return r;
}
__device__ __forceinline__ void unpack8_add(u32x4 v, f32x4& lo, f32x4& hi) {
    unsigned a = v[0], b = v[1], c = v[2], d = v[3];
    lo[0] += __uint_as_float(a << 16); lo[1] += __uint_as_float(a & 0xffff0000u);
    lo[2] += __uint_as_float(b << 16); lo[3] += __uint_as_float(b & 0xffff0000u);
    hi[0] += __uint_as_float(c << 16); hi[1] += __uint_as_float(c & 0xffff0000u);
    hi[2] += __uint_as_float(d << 16); hi[3] += __uint_as_float(d & 0xffff0000u);
}
__device__ __forceinline__ float fmx(float a, float b) { return __builtin_elementwise_maximum(a, b); }
__device__ __forceinline__ void swap16(float& a, float& b) {
    auto r = __builtin_amdgcn_permlane16_swap(__float_as_uint(a), __float_as_uint(b), false, false);
    unsigned x = r[0], y = r[1]; a = __uint_as_float(x); b = __uint_as_float(y);
}
__device__ __forceinline__ void swap32(float& a, float& b) {
    auto r = __builtin_amdgcn_permlane32_swap(__float_as_uint(a), __float_as_uint(b), false, false);
    unsigned x = r[0], y = r[1]; a = __uint_as_float(x); b = __uint_as_float(y);
}
template <bool MAX> __device__ __forceinline__ float op2(float a, float b) { return MAX ? fmx(a, b) : a + b; }
template <bool MAX> __device__ __forceinline__ void allred4(float* v) {
    swap16(v[0], v[1]); float c = op2<MAX>(v[0], v[1]);
    swap16(v[2], v[3]); float d = op2<MAX>(v[2], v[3]);
    swap32(c, d); float e = op2<MAX>(c, d);
    if (!MAX) e = __builtin_amdgcn_rcpf(e);
    float e2 = e; swap32(e, e2);
    float f = e; swap16(e, f);
    float h = e2; swap16(e2, h);
    v[0] = e; v[1] = f; v[2] = e2; v[3] = h;
}
__device__ __forceinline__ unsigned ld_flag(GAS unsigned* f) {
    return __builtin_amdgcn_readfirstlane(__hip_atomic_load(f, __ATOMIC_RELAXED, __HIP_MEMORY_SCOPE_AGENT));
}
__device__ __forceinline__ void add_flag(GAS unsigned* f, int lane) {
    if (lane == 0) (void)__hip_atomic_fetch_add(f, 1u, __ATOMIC_RELAXED, __HIP_MEMORY_SCOPE_AGENT);
}
#define LD_FLAG_ASYNC(REG, f) asm volatile("global_load_dword " REG ", %0, off sc1" ::"v"(f) : "memory", REG)
#define RD_FLAG(REG, out) asm volatile("v_readfirstlane_b32 %0, " REG : "=s"(out)::"memory")
#define RD_VEC(REG, out) asm volatile("v_mov_b32 %0, " REG : "=v"(out)::"memory")
__device__ __forceinline__ void add_flag_async(GAS unsigned* f, int lane) {
    unsigned one = 1u;
    if (lane == 0) asm volatile("global_atomic_add %0, %1, off" ::"v"(f), "v"(one) : "memory");
}
__device__ __forceinline__ void wait_ge(GAS unsigned* f, unsigned target, GAS unsigned* hb, int lane) {
    unsigned spins = 0;
    while (ld_flag(f) < target) {
        ++spins;
        if ((spins & 127u) == 0u) {
            if (lane < 8) (void)__hip_atomic_fetch_add(hb + lane * FW, 1u, __ATOMIC_RELAXED, __HIP_MEMORY_SCOPE_AGENT);
            if (spins > SPIN_CAP) break;
        }
        __builtin_amdgcn_s_sleep(1);
    }
    asm volatile("" ::: "memory");
}
struct Frag { bf16x8 a[4], b[4]; };
#define SGB() do { _Pragma("unroll") for (int _i = 0; _i < 8; ++_i) { __builtin_amdgcn_sched_group_barrier(0x8, 1, 0); __builtin_amdgcn_sched_group_barrier(0x100, 1, 0); } __builtin_amdgcn_sched_group_barrier(0x8, 8, 0); } while (0)
__device__ __forceinline__ void nt_read(Frag& f, LAS char* pa, LAS char* pb) {
    f.a[0] = lds_b128(pa);
#pragma unroll
    for (int n = 0; n < 4; ++n) f.b[n] = lds_b128(pb + n * 2048);
#pragma unroll
    for (int m = 1; m < 4; ++m) f.a[m] = lds_b128(pa + m * 2048);
}
__device__ __forceinline__ void nt_mma(f32x4 (&acc)[4][4], const Frag& f) {
#pragma unroll
    for (int m = 0; m < 4; ++m)
#pragma unroll
        for (int n = 0; n < 4; ++n) acc[m][n] = mfma32(f.a[m], f.b[n], acc[m][n]);
}
__device__ __forceinline__ void bar_cons() {
    asm volatile("" ::: "memory");
    __builtin_amdgcn_s_waitcnt(0x0070);
    __builtin_amdgcn_s_barrier();
    asm volatile("" ::: "memory");
}
#define SGBV() do { _Pragma("unroll") for (int _i = 0; _i < 8; ++_i) { __builtin_amdgcn_sched_group_barrier(0x8, 1, 0); __builtin_amdgcn_sched_group_barrier(0x100, 1, 0); } \
    _Pragma("unroll") for (int _i = 0; _i < 2; ++_i) { __builtin_amdgcn_sched_group_barrier(0x8, 1, 0); __builtin_amdgcn_sched_group_barrier(0x4, 2, 0); } \
    _Pragma("unroll") for (int _i = 0; _i < 2; ++_i) { __builtin_amdgcn_sched_group_barrier(0x8, 1, 0); __builtin_amdgcn_sched_group_barrier(0x2, 1, 0); } \
    __builtin_amdgcn_sched_group_barrier(0x8, 4, 0); } while (0)
__device__ __forceinline__ void nt_pin(f32x4 (&acc)[4][4]) {
    asm volatile("" : "+v"(acc[0][0]), "+v"(acc[0][1]), "+v"(acc[0][2]), "+v"(acc[0][3]), "+v"(acc[1][0]), "+v"(acc[1][1]), "+v"(acc[1][2]), "+v"(acc[1][3]));
    asm volatile("" : "+v"(acc[2][0]), "+v"(acc[2][1]), "+v"(acc[2][2]), "+v"(acc[2][3]), "+v"(acc[3][0]), "+v"(acc[3][1]), "+v"(acc[3][2]), "+v"(acc[3][3]));
}
template <bool LASTBAR>
__device__ __forceinline__ int nt_consume(f32x4 (&acc)[4][4], LAS char* smem, int slot, int nst, int w, int lane) {
    const int r = lane & 15, g = lane >> 4;
    const int c0 = ((g ^ (r >> 1)) << 4), c1 = c0 ^ 64;
    const int oa = ((w & 1) * 64 + r) * 128, ob = 16384 + ((w >> 1) * 64 + r) * 128;
    const int oa0 = oa + c0, ob0 = ob + c0, oa1 = oa + c1, ob1 = ob + c1;
    Frag f0, f1;
    int so = slot * SLOT;
    nt_read(f0, smem + so + oa0, smem + so + ob0);
    int pa = so + oa1, pb = so + ob1;
#pragma unroll 1
    for (int it = 0; it < nst - 1; ++it) {
        __builtin_amdgcn_sched_barrier(0);
        nt_read(f1, smem + pa, smem + pb);
        nt_mma(acc, f0);
        so = (so == 4 * SLOT) ? 0 : so + SLOT;
        int qa = so + oa0, qb = so + ob0;
        SGBV();
        __builtin_amdgcn_sched_barrier(0);
        asm volatile("" : "+v"(qa), "+v"(qb));
        bar_cons();
        __builtin_amdgcn_sched_barrier(0);
        nt_read(f0, smem + qa, smem + qb);
        nt_mma(acc, f1);
        pa = so + oa1; pb = so + ob1;
        SGBV();
        __builtin_amdgcn_sched_barrier(0);
        asm volatile("" : "+v"(pa), "+v"(pb));
    }
    nt_read(f1, smem + pa, smem + pb);
    nt_mma(acc, f0);
    SGB();
    __builtin_amdgcn_sched_barrier(0);
    so = (so == 4 * SLOT) ? 0 : so + SLOT;
    if (LASTBAR) bar_cons();
    __builtin_amdgcn_sched_barrier(0);
    nt_mma(acc, f1);
    return so / SLOT;
}

__device__ __forceinline__ void t2_pin(f32x4 (&aq)[8], f32x4 (&ak)[9], f32x4 (&av)[9]) {
    asm volatile("" : "+v"(aq[0]), "+v"(aq[1]), "+v"(aq[2]), "+v"(aq[3]), "+v"(aq[4]), "+v"(aq[5]), "+v"(aq[6]), "+v"(aq[7]),
                      "+v"(ak[0]), "+v"(ak[1]), "+v"(ak[2]), "+v"(ak[3]), "+v"(ak[4]));
    asm volatile("" : "+v"(ak[5]), "+v"(ak[6]), "+v"(ak[7]), "+v"(ak[8]),
                      "+v"(av[0]), "+v"(av[1]), "+v"(av[2]), "+v"(av[3]), "+v"(av[4]), "+v"(av[5]), "+v"(av[6]), "+v"(av[7]), "+v"(av[8]));
}
struct Frag2 { bf16x8 x[9], w[3]; };
__device__ __forceinline__ void t2_read(Frag2& f, LAS char* px, LAS char* pw) {
    f.w[1] = lds_tr2(pw + 1024);
    f.x[0] = lds_b128(px);
    f.w[2] = lds_tr2(pw + 2048);
    f.w[0] = lds_tr2(pw);
#pragma unroll
    for (int rb = 1; rb < 9; ++rb) f.x[rb] = lds_b128(px + rb * 2048);
}
__device__ __forceinline__ void t2_mma(f32x4 (&aq)[8], f32x4 (&ak)[9], f32x4 (&av)[9], const Frag2& f) {
#pragma unroll
    for (int rb = 0; rb < 9; ++rb) {
        ak[rb] = mfma32(f.w[1], f.x[rb], ak[rb]);
        av[rb] = mfma32(f.x[rb], f.w[2], av[rb]);
        if (rb > 0) aq[rb - 1] = mfma32(f.w[0], f.x[rb], aq[rb - 1]);
    }
}
#define SGB2() do { _Pragma("unroll") for (int _i = 0; _i < 15; ++_i) { __builtin_amdgcn_sched_group_barrier(0x8, 1, 0); __builtin_amdgcn_sched_group_barrier(0x100, 1, 0); } \
    _Pragma("unroll") for (int _i = 0; _i < 2; ++_i) { __builtin_amdgcn_sched_group_barrier(0x8, 1, 0); __builtin_amdgcn_sched_group_barrier(0x4, 2, 0); } \
    _Pragma("unroll") for (int _i = 0; _i < 2; ++_i) { __builtin_amdgcn_sched_group_barrier(0x8, 1, 0); __builtin_amdgcn_sched_group_barrier(0x2, 1, 0); } \
    __builtin_amdgcn_sched_group_barrier(0x8, 7, 0); } while (0)

struct Frag4 { bf16x8 w[4][2], o[4][2]; };
#define SGB4() do { _Pragma("unroll") for (int _i = 0; _i < 12; ++_i) { __builtin_amdgcn_sched_group_barrier(0x100, 2, 0); __builtin_amdgcn_sched_group_barrier(0x8, 1, 0); } __builtin_amdgcn_sched_group_barrier(0x8, 4, 0); } while (0)
__device__ __forceinline__ void pf(rsrc_t r, int voff, int soff) {
    asm volatile("s_nop 4\n\tbuffer_load_dword v255, %0, %1, %2 offen" ::"v"(voff), "s"(r), "s"(__builtin_amdgcn_readfirstlane(soff)) : "memory", "v255");
}

__global__ __launch_bounds__(512, 2) void fused(const __bf16* __restrict__ xa, const __bf16* __restrict__ cre, const __bf16* __restrict__ cim,
                                                const __bf16* __restrict__ hre, const __bf16* __restrict__ him, const __bf16* __restrict__ xb,
                                                const __bf16* __restrict__ wqkv, const __bf16* __restrict__ wo,
                                                char* __restrict__ ws, __bf16* __restrict__ out) {
    __shared__ __attribute__((aligned(1024))) char smem_[163840];
    LAS char* smem = (LAS char*)smem_;
    asm volatile("s_load_dword s98, %0, 0x0\n\ts_load_dword s99, %0, 0x40" ::"s"(__builtin_amdgcn_kernarg_segment_ptr()) : "s98", "s99");
    const int tid = threadIdx.x, lane = tid & 63;
    const int wave = __builtin_amdgcn_readfirstlane(tid >> 6);
    const int bid = blockIdx.x, xcd = bid & 7, idx = bid >> 3;
    const int q = xcd & 3, th = xcd >> 2;
    const int ttl = idx >> 2, et = idx & 3, tt = th * 8 + ttl;
    const int t0 = tt * 128, e0 = et * 128;
    const bool typeA = (ttl & 1) == 0;
    const int s1 = typeA ? ((q & 1) * 8 + th * 4 + (ttl >> 1)) * 128 : ((xcd >> 1) * 4 + (ttl >> 1)) * 128;
    const int hg = (xcd & 1) * 4 + et;
    const int s4 = (xcd * 4 + (idx >> 3)) * 64, n4 = (idx & 7) * 64;
    GAS unsigned* flags = (GAS unsigned*)(ws + WS_FLAGS);
    const int slot3 = typeA ? 3 : 0;
    const int rot1 = 4 * (((ttl >> 1) + et) & 1), rot3 = 4 * et;

    if (wave == 1 && ttl == 1) {
        unsigned long long pc; int len;
        asm volatile("s_getpc_b64 %0\n.Lfnet_pc%=:\n\ts_mov_b32 %1, fnet_code_end-.Lfnet_pc%=" : "=s"(pc), "=s"(len));
        const int o = (et * 64 + lane) * 128;
        if (o + 4 <= len) { unsigned v = *(const unsigned*)(pc + o); asm volatile("" ::"v"(v)); }
    }
    if (__builtin_expect(wave >= 6, 1)) {
        const int lw = wave - 6;
        const int row = 8 * lw + (lane >> 3);
        const int ch = ((lane & 7) ^ (4 * lw + (lane >> 4))) * 16;
        const int cht = ((lane & 7) ^ (2 * (((lane >> 4) & 1) | (lw << 1)))) * 16;
        const bool master = (bid < 8 && lw == 0);
        GAS unsigned* myhb = flags + bid * FW;
        if (master) {
#pragma unroll
            for (int i = 0; i < NCNT; i += 64)
                if (i + lane < NCNT) __hip_atomic_store(flags + (8 + bid * NCNT + i + lane) * FW, 0u, __ATOMIC_RELAXED, __HIP_MEMORY_SCOPE_AGENT);
        }
        rsrc_t rc = mk_rsrc((q >> 1) ? cim : cre, SEQ * SEQ * 2);
        const int vb = row * 4096 + ch;
        const int sb = t0 * 4096 + (q & 1) * 2048;
        auto issueC = [&](int st, int slot) {
            const int so = sb + ((st + rot3) & 15) * 128;
#pragma unroll
            for (int u = 0; u < 8; ++u) glds<0>(rc, smem + slot * SLOT + 16384 + (lw + 2 * u) * 1024, vb, so + 16 * ((u & 4) | ((u + q) & 3)) * 4096);
        };
        rsrc_t rw = mk_rsrc(wo, DM * DM * 2);
        int slot = 0;
        if (typeA) {
            rsrc_t rh = mk_rsrc((q >> 1) ? him : hre, DM * DM * 2);
            const int vh = row * 1024 + ch;
#pragma unroll 1
            for (int st = 0; st < 8; ++st) {
                if (st >= 2) bar_vm<8>();
                if (master && st >= 2 && st < 5) add_flag_async(myhb, lane);
                const int so = e0 * 1024 + ((st + rot1) & 7) * 128;
#pragma unroll
                for (int u = 0; u < 8; ++u) glds<0>(rh, smem + slot * SLOT + 16384 + (lw + 2 * u) * 1024, vh, so + 16 * u * 1024);
                slot = (slot + 1 == 5) ? 0 : slot + 1;
            }
        } else {
            rsrc_t rq = mk_rsrc(wqkv, DM * 3 * DM * 2);
            const int vq = row * 3072 + cht;
            int pos = 1;
#pragma unroll 1
            for (int st = 0; st < 8; ++st) {
                if (st >= 2) bar_vm<12>();
                LAS char* sl = smem + T2_BASE + pos * T2_SLOT;
                const int so = ((st + rot1) & 7) * 64 * 3072 + hg * 128;
#pragma unroll
                for (int v = 0; v < 4; ++v)
#pragma unroll
                    for (int cg = 0; cg < 3; ++cg)
                        glds<0>(rq, sl + 18432 + ((lw + 2 * v) * 3 + cg) * 1024, vq, so + 16 * v * 3072 + cg * 1024);
                pos = (pos + 1 == 3) ? 0 : pos + 1;
            }
        }
#pragma unroll 1
        for (int st = 0; st < 16; ++st) {
            if (typeA) {
                if (st == 2) { bar_only(); bar_only(); if (master) add_flag_async(myhb, lane); }
                bar_vm<8>();
            } else if (st < 5) {
                if (st == 1) bar_vm<20>();
                if (st == 2) bar_vm<16>();
                if (st == 3) bar_only();
            } else {
                if (st == 5) { bar_vm<32>(); bar_vm<24>(); bar_vm<16>(); }
                bar_vm<8>();
            }
            issueC(st, slot);
            slot = (slot + 1 == 5) ? 0 : slot + 1;
            if (st == 7) pf(rw, lane * 128, idx * 16384 + lw * 8192);
        }
        const int vw = row * 1024 + cht;
#pragma unroll 1
        for (int st = 0; st < 4; ++st) {
            if (st < 2) bar_vm<8>();
            if (st == 2) bar_vm<0>();
#pragma unroll
            for (int h = 0; h < 2; ++h)
#pragma unroll
                for (int u = 0; u < 4; ++u)
                    glds<0>(rw, smem + slot * SLOT + h * 16384 + 8192 + (lw + 2 * u) * 1024, vw, ((st * 2 + h) * 64 + 16 * u) * 1024 + n4 * 2);
            slot = (slot + 1 == 5) ? 0 : slot + 1;
        }
        bar_vm<0>();
    } else if (__builtin_expect(wave >= 4, 1)) {
        const int lw = wave - 4;
        const int ch = ((lane & 7) ^ (4 * lw + (lane >> 4))) * 16;
        const int prow = 8 * (lane >> 5) + ((lane >> 3) & 3) + 16 * lw;
        const int row = 8 * lw + (lane >> 3);
        GAS unsigned* hbp = flags + (lane & 7) * FW;
        if (!typeA) {
            rsrc_t rx = mk_rsrc(xb, SEQ * DM * 2);
            const int vx = row * 1024 + ch;
            const int sxm = s1 * 1024, sxh = (s1 >= 16 ? s1 - 16 : 0) * 1024;
            int pos = 1;
#pragma unroll 1
            for (int st = 0; st < 8; ++st) {
                if (st >= 2) bar_vm<9>();
                if (st == 0 && lw == 0) LD_FLAG_ASYNC("v254", hbp);
                if (st == 7 && lw == 0) LD_FLAG_ASYNC("v253", hbp);
                LAS char* sl = smem + T2_BASE + pos * T2_SLOT;
                const int ko = ((st + rot1) & 7) * 128;
                glds<0>(rx, sl + lw * 1024, vx, sxh + ko);
#pragma unroll
                for (int u = 0; u < 8; ++u) glds<0>(rx, sl + 2048 + (lw + 2 * u) * 1024, vx, sxm + 16 * u * 1024 + ko);
                pos = (pos + 1 == 3) ? 0 : pos + 1;
            }
            bar_vm<9>();
        }
        rsrc_t ro = mk_rsrc(ws + WS_O, SEQ * DM * 2);
        const int vo = row * 32 + (ch & 16) + (ch >> 5) * 65536;
        GAS unsigned* f2 = flags + (F2_IDX + (s4 >> 7)) * FW;
        int slot = 0;
#pragma unroll 1
        for (int pass = typeA ? 0 : 1; pass < 2; ++pass) {
            if (pass) {
                bar_vm<0>();
                if (lw == 0) {
                    unsigned a, b, spins = 0;
                    RD_VEC("v254", a); RD_VEC("v253", b);
                    bool moved = (a != b) || ((lane & 7) == bid);
                    while (!__all(moved)) {
                        if (bid < 8) add_flag(flags + bid * FW, lane);
                        __builtin_amdgcn_s_sleep(1);
                        b = __hip_atomic_load(hbp, __ATOMIC_RELAXED, __HIP_MEMORY_SCOPE_AGENT);
                        moved = moved || (a != b);
                        if (++spins > SPIN_CAP) break;
                    }
                }
                bar_only();
                if (typeA) {
                    bar_only();
                    if (lw == 0 && lane < R1) (void)__hip_atomic_fetch_add(flags + (F1_IDX + (q * 4 + et) * R1 + lane) * FW, 1u, __ATOMIC_RELAXED, __HIP_MEMORY_SCOPE_AGENT);
                }
                wait_ge(flags + (F1_IDX + (q * 4 + et) * R1 + ((lw + 2 * th + 4 * (ttl >> 1) + 16 * (ttl & 1)) & (R1 - 1))) * FW, 8u, flags, lane);
            }
            rsrc_t ra = mk_rsrc(pass ? (const void*)(ws + WS_ABT) : (const void*)xa, pass ? 1024 * SEQ * 2 : SEQ * DM * 2);
            const int va = pass ? prow * 64 + (ch & 48) + (ch >> 6) * 8192 : prow * 1024 + ch;
            const int sa = pass ? (((q >> 1) * 4 + et) * 64 + (q & 1) * 32) * 8192 : s1 * 1024;
            const int rot = pass ? rot3 : rot1, nst = pass ? 16 : 8, ksh = pass ? 14 : 7, rb = pass ? 64 : 1024;
#pragma unroll 1
            for (int st = 0; st < nst; ++st) {
                if (st >= 2) bar_vm<8>();
                if (!pass) {
                    if (st == 0 && lw == 0) LD_FLAG_ASYNC("v254", hbp);
                    if (st == 7 && lw == 0) LD_FLAG_ASYNC("v253", hbp);
                } else if (st == 3 && lw == 1 && !typeA) add_flag_async(flags + (F2_IDX + (s1 >> 7)) * FW, lane);
                const int so = sa + (((st + rot) & (nst - 1)) << ksh);
#pragma unroll
                for (int u = 0; u < 8; ++u) glds<SC1>(ra, smem + slot * SLOT + (lw + 2 * u) * 1024, va, so + (32 * (u >> 1) + 4 * (u & 1)) * rb);
                slot = (slot + 1 == 5) ? 0 : slot + 1;
                if (st == 13) LD_FLAG_ASYNC("v252", f2);
            }
            if (!pass) bar_vm<8>();
        }
#pragma unroll 1
        for (int st = 0; st < 4; ++st) {
            if (st < 2) bar_vm<8>();
            if (st == 2) bar_vm<0>();
            if (st == 0) {
                unsigned f2v;
                RD_FLAG("v252", f2v);
                if (f2v < 8u) wait_ge(f2, 8u, flags, lane);
            }
#pragma unroll
            for (int h = 0; h < 2; ++h)
#pragma unroll
                for (int u = 0; u < 4; ++u)
                    glds<SC1>(ro, smem + slot * SLOT + h * 16384 + (lw + 2 * u) * 1024, vo, (s4 + 16 * u) * 32 + (st * 2 + h) * 4 * 65536);
            slot = (slot + 1 == 5) ? 0 : slot + 1;
        }
        bar_vm<0>();
    } else {
        const int r = lane & 15, g = lane >> 4;
        const int tile = tt * 4 + et;
        rsrc_t rp = mk_rsrc(ws + WS_PART, 64 * 16 * 4 * 2048);
        const int inb = (tile * 4 + q) * 32768 + wave * 2048 + lane * 16;
        if (typeA) {
            f32x4 acc[4][4] = {};
            nt_pin(acc);
            bar_lgkm();
            nt_consume<true>(acc, smem, 0, 8, wave, lane);
            const unsigned sg = (q >> 1) ? 0x80008000u : 0u;
            rsrc_t rs = mk_rsrc(ws + WS_ABT, 1024 * SEQ * 2);
            const int vs = r * 64 + g * 16;
            const int ss = (((q >> 1) * 4 + et) * 64 + (s1 >> 5) + (wave & 1) * 2) * 8192 + (wave >> 1) * 4096;
#pragma unroll
            for (int n = 0; n < 4; ++n)
#pragma unroll
                for (int u = 0; u < 2; ++u)
                    __builtin_amdgcn_raw_buffer_store_b128(pack8(acc[2 * u][n], acc[2 * u + 1][n]) ^ sg, rs, vs + ss + n * 1024 + u * 8192, 0, SC1);
            bar_vm<0>();
        } else {
            const int c0 = ((g ^ (r >> 1)) << 4);
            const int ox = r * 128;
            const int qq = (lane & 15) >> 2, p = lane & 3;
            const int ow = 18432 + g * 3072 + qq * 128 + (((2 * wave + (p >> 1)) ^ (2 * ((qq >> 1) | ((g & 1) << 1)))) << 4) + 8 * (p & 1);
            f32x4 aq[8] = {}, ak[9] = {}, av[9] = {};
            Frag2 f0, f1;
            int so = T2_BASE + T2_SLOT;
            t2_pin(aq, ak, av);
            bar_lgkm();
            t2_read(f0, smem + so + ox + c0, smem + so + ow);
            int px = so + ox + (c0 ^ 64), pw = so + ow;
#pragma unroll 1
            for (int it = 0; it < 7; ++it) {
                __builtin_amdgcn_sched_barrier(0);
                t2_read(f1, smem + px, smem + pw + 12288);
                t2_mma(aq, ak, av, f0);
                so = (so == T2_BASE + 2 * T2_SLOT) ? T2_BASE : so + T2_SLOT;
                int qx = so + ox + c0, qw = so + ow;
                SGB2();
                __builtin_amdgcn_sched_barrier(0);
                asm volatile("" : "+v"(qx), "+v"(qw));
                t2_pin(aq, ak, av);
                bar_cons();
                __builtin_amdgcn_sched_barrier(0);
                t2_read(f0, smem + qx, smem + qw);
                t2_mma(aq, ak, av, f1);
                px = qx ^ 64; pw = qw;
                SGB2();
                __builtin_amdgcn_sched_barrier(0);
                asm volatile("" : "+v"(px));
            }
            t2_read(f1, smem + px, smem + pw + 12288);
            t2_mma(aq, ak, av, f0);
            SGB2();
            __builtin_amdgcn_sched_barrier(0);
            t2_pin(aq, ak, av);
            bar_cons();
            __builtin_amdgcn_sched_barrier(0);
            t2_mma(aq, ak, av, f1);
            const int j = r, par = j & 1;
            const bool uc0 = (4 * g + par) <= j, uc1 = (4 * g + par + 2) <= j;
            const unsigned s0v = par ? 0x01000c0cu : 0x0c0c0100u, s1v = par ? 0x03020c0cu : 0x0c0c0302u, zs = 0x0c0c0c0cu;
            const unsigned selc0 = uc0 ? s0v : zs, selp0 = uc0 ? zs : s0v, selc1 = uc1 ? s1v : zs, selp1 = uc1 ? zs : s1v;
            rsrc_t rs = mk_rsrc(ws + WS_O, SEQ * DM * 2);
            const int vs = j * 32 + g * 8;
            const int ss = (hg * 4 + wave) * 65536 + s1 * 32;
            const f32x4 z = {0.f, 0.f, 0.f, 0.f};
            const float C = 0.25f * 1.44269504088896f;
            u32x2 kb[9], vb[9];
            float l0[8], l1[8], m[8];
#pragma unroll
            for (int i = 0; i < 9; ++i) kb[i] = cvt4u(ak[i]);
#pragma unroll
            for (int b = 0; b < 8; ++b) {
                u32x2 qb = cvt4u(aq[b]);
                f32x4 sc = mfma16(kb[b + 1], qb, z), sp = mfma16(kb[b], qb, z);
                float c0v = par ? sc[1] : sc[0], c1v = par ? sc[3] : sc[2];
                float p0v = par ? sp[1] : sp[0], p1v = par ? sp[3] : sp[2];
                if (b == 0 && s1 == 0) { p0v = -4e9f; p1v = -4e9f; }
                unsigned pk = cvtpk(uc0 ? c0v : p0v, uc1 ? c1v : p1v);
                l0[b] = __uint_as_float(pk << 16); l1[b] = __uint_as_float(pk & 0xffff0000u);
                m[b] = fmx(l0[b], l1[b]);
            }
            allred4<true>(m); allred4<true>(m + 4);
#pragma unroll
            for (int i = 0; i < 9; ++i) vb[i] = cvt4u(av[i]);
#pragma unroll
            for (int b = 0; b < 8; ++b) {
                l0[b] = __builtin_amdgcn_exp2f((l0[b] - m[b]) * C); l1[b] = __builtin_amdgcn_exp2f((l1[b] - m[b]) * C);
                m[b] = l0[b] + l1[b];
            }
            allred4<false>(m); allred4<false>(m + 4);
            f32x4 ov[8];
#pragma unroll
            for (int b = 0; b < 8; ++b) {
                unsigned P = cvtpk(l0[b] * m[b], l1[b] * m[b]);
                u32x2 pc = {__builtin_amdgcn_perm(0u, P, selc0), __builtin_amdgcn_perm(0u, P, selc1)};
                u32x2 pp = {__builtin_amdgcn_perm(0u, P, selp0), __builtin_amdgcn_perm(0u, P, selp1)};
                ov[b] = mfma16(vb[b], pp, mfma16(vb[b + 1], pc, z));
            }
#pragma unroll
            for (int b = 0; b < 8; ++b) __builtin_amdgcn_raw_buffer_store_b64(cvt4u(ov[b]), rs, vs, ss + b * 512, SC1);
        }
        f32x4 acc[4][4] = {};
        nt_pin(acc);
        bar_lgkm();
        int slot = nt_consume<false>(acc, smem, slot3, 16, wave, lane);
        u32x4 sv[3][2];
#pragma unroll
        for (int n = 1; n < 4; ++n) { sv[n - 1][0] = pack8(acc[0][n], acc[1][n]); sv[n - 1][1] = pack8(acc[2][n], acc[3][n]); }
        auto send = [&](int n) {
            const int base = (((tile * 4 + ((q + n) & 3)) * 4 + q) * 4 + wave) * 2048 + lane * 16;
            __builtin_amdgcn_raw_buffer_store_b128(sv[n - 1][0], rp, base, 0, SC1);
            __builtin_amdgcn_raw_buffer_store_b128(sv[n - 1][1], rp, base + 1024, 0, SC1);
        };
        send(1); send(2); send(3);
        f32x4 own[4];
#pragma unroll
        for (int m = 0; m < 4; ++m) own[m] = acc[m][0];
        f32x4 a4[2][2] = {};
        rsrc_t rout = mk_rsrc(out, SEQ * 1024 * 2);
        u32x4 pv[3][2];
        {
            const int c0 = ((g ^ (r >> 1)) << 4);
            const int oo = ((wave >> 1) * 32 + r) * 128;
            const int qq = (lane & 15) >> 2, p = lane & 3;
            const int swz = 2 * ((qq >> 1) | ((g & 1) << 1));
            const int ow0 = 8192 + g * 1024 + qq * 128 + ((((wave & 1) * 4 + (p >> 1)) ^ swz) << 4) + 8 * (p & 1);
            const int ow1 = ow0 ^ 32;
            Frag4 h0, h1;
            auto rd = [&](Frag4& f) {
                LAS char* sl = smem + slot * SLOT;
#pragma unroll
                for (int i = 0; i < 4; ++i) {
                    const int ho = (i >> 1) * 16384, kk = i & 1;
                    f.w[i][0] = lds_tr2(sl + ho + ow0 + kk * 4096);
                    f.o[i][0] = lds_b128(sl + ho + oo + (c0 ^ (kk * 64)));
                    f.o[i][1] = lds_b128(sl + ho + oo + (c0 ^ (kk * 64)) + 2048);
                    f.w[i][1] = lds_tr2(sl + ho + ow1 + kk * 4096);
                }
                slot = (slot + 1 == 5) ? 0 : slot + 1;
            };
            auto mm = [&](const Frag4& f) {
#pragma unroll
                for (int i = 0; i < 4; ++i) {
                    a4[0][0] = mfma32(f.w[i][0], f.o[i][0], a4[0][0]);
                    a4[0][1] = mfma32(f.w[i][0], f.o[i][1], a4[0][1]);
                    a4[1][0] = mfma32(f.w[i][1], f.o[i][0], a4[1][0]);
                    a4[1][1] = mfma32(f.w[i][1], f.o[i][1], a4[1][1]);
                }
                asm volatile("" : "+v"(a4[0][0]), "+v"(a4[0][1]), "+v"(a4[1][0]), "+v"(a4[1][1]));
            };
            asm volatile("" : "+v"(own[0]), "+v"(own[1]), "+v"(own[2]), "+v"(own[3]));
            bar_only();
            __builtin_amdgcn_sched_barrier(0);
            rd(h0);
            __builtin_amdgcn_sched_barrier(0);
            rd(h1); mm(h0); SGB4();
            __builtin_amdgcn_sched_barrier(0);
            drain_vm();
            if (lane < 4 && lane != q)
                (void)__hip_atomic_fetch_add(flags + (F3_IDX + tile * 4 + lane) * FW, 1u, __ATOMIC_RELAXED, __HIP_MEMORY_SCOPE_AGENT);
            mm(h1);
            wait_ge(flags + (F3_IDX + tile * 4 + q) * FW, 12u, flags, lane);
#pragma unroll
            for (int k = 0; k < 3; ++k) {
                const int o = inb + ((q + 1 + k) & 3) * 8192;
                pv[k][0] = __builtin_amdgcn_raw_buffer_load_b128(rp, o, 0, SC1);
                pv[k][1] = __builtin_amdgcn_raw_buffer_load_b128(rp, o + 1024, 0, SC1);
            }
            bar_only();
            __builtin_amdgcn_sched_barrier(0);
            rd(h0);
            __builtin_amdgcn_sched_barrier(0);
            rd(h1); mm(h0); SGB4();
            __builtin_amdgcn_sched_barrier(0);
            mm(h1);
#pragma unroll
            for (int n = 0; n < 2; ++n) {
                u32x2 lo = cvt4u(a4[0][n]), hi = cvt4u(a4[1][n]);
                auto r0 = __builtin_amdgcn_permlane16_swap(lo[0], hi[0], false, false);
                auto r1 = __builtin_amdgcn_permlane16_swap(lo[1], hi[1], false, false);
                u32x4 v = {r0[0], r1[0], r0[1], r1[1]};
                __builtin_amdgcn_raw_buffer_store_b128(v, rout, ((s4 + (wave >> 1) * 32 + n * 16 + r) * 1024 + 512 + n4 + (wave & 1) * 32 + (g & 1) * 16 + (g >> 1) * 8) * 2, 0, SC1);
            }
        }
#pragma unroll
        for (int k = 0; k < 3; ++k) {
            unpack8_add(pv[k][0], own[0], own[1]);
            unpack8_add(pv[k][1], own[2], own[3]);
        }
        const int trow = t0 + (wave >> 1) * 64 + q * 16 + r;
#pragma unroll
        for (int u = 0; u < 2; ++u)
            __builtin_amdgcn_raw_buffer_store_b128(pack8(own[2 * u], own[2 * u + 1]), rout, (trow * 1024 + e0 + (wave & 1) * 64 + 32 * u + 8 * g) * 2, 0, SC1);
    }
}

extern "C" __global__ void fnet_code_end(unsigned* p) { p[0] = 0; }

extern "C" void kernel_launch(void* const* d_in, const int* in_sizes, int n_in,
                              void* d_out, int out_size, void* d_ws, size_t ws_size,
                              hipStream_t stream) {
    fused<<<256, 512, 0, stream>>>((const __bf16*)d_in[0], (const __bf16*)d_in[1], (const __bf16*)d_in[2], (const __bf16*)d_in[3],
                                   (const __bf16*)d_in[4], (const __bf16*)d_in[5], (const __bf16*)d_in[6], (const __bf16*)d_in[7],
                                   (char*)d_ws, (__bf16*)d_out);
}
```

```cpp
#include <hip/hip_runtime.h>
#include <hip/hip_bf16.h>
#include <stdint.h>

#define LAS __attribute__((address_space(3)))
#define GAS __attribute__((address_space(1)))
typedef __attribute__((ext_vector_type(8))) __bf16 bf16x8;
typedef __attribute__((ext_vector_type(4))) __bf16 bf16x4;
typedef __attribute__((ext_vector_type(2))) __bf16 bf16x2;
typedef __attribute__((ext_vector_type(2))) float f32x2;
typedef __attribute__((ext_vector_type(4))) short s16x4;
typedef __attribute__((ext_vector_type(4))) float f32x4;
typedef __attribute__((ext_vector_type(4))) unsigned u32x4;
typedef __attribute__((ext_vector_type(2))) unsigned u32x2;
typedef __amdgpu_buffer_rsrc_t rsrc_t;

constexpr int SEQ = 2048, DM = 512;
constexpr int SLOT = 32768;
constexpr int T2_SLOT = 43008;
constexpr int T2_BASE = 32768;

constexpr unsigned WS_FLAGS = 0;
constexpr unsigned WS_ABT   = 1u << 20;
constexpr unsigned WS_O     = WS_ABT + (4u << 20);
constexpr unsigned WS_PART  = WS_O + (2u << 20);
constexpr int FW = 32;
constexpr int R1 = 16;
constexpr int F1_IDX = 8, F2_IDX = F1_IDX + 16 * R1, F3_IDX = F2_IDX + 16, NCNT = (16 * R1 + 16 + 256) / 8;
constexpr int SC1 = 16;
constexpr unsigned SPIN_CAP = 1u << 17;

__device__ __forceinline__ rsrc_t mk_rsrc(const void* p, unsigned bytes) {
    return __builtin_amdgcn_make_buffer_rsrc((void*)p, 0, bytes, 0x00020000);
}
template <int AUX>
__device__ __forceinline__ void glds(rsrc_t r, LAS char* dst, int voff, int soff) {
    __builtin_amdgcn_raw_ptr_buffer_load_lds(r, (LAS void*)dst, 16, voff, soff, 0, AUX);
}
template <int N> __device__ __forceinline__ void bar_vm() {
    asm volatile("s_waitcnt vmcnt(%0)\n\ts_barrier" ::"n"(N) : "memory");
}
__device__ __forceinline__ void bar_only() { asm volatile("s_barrier" ::: "memory"); }
__device__ __forceinline__ void bar_lgkm() {
    asm volatile("s_waitcnt lgkmcnt(0)\n\ts_barrier" ::: "memory");
}
__device__ __forceinline__ void drain_vm() { asm volatile("s_waitcnt vmcnt(0)" ::: "memory"); }
__device__ __forceinline__ f32x4 mfma32(bf16x8 a, bf16x8 b, f32x4 c) {
    return __builtin_amdgcn_mfma_f32_16x16x32_bf16(a, b, c, 0, 0, 0);
}
__device__ __forceinline__ f32x4 mfma16(u32x2 a, u32x2 b, f32x4 c) {
    return __builtin_amdgcn_mfma_f32_16x16x16bf16_1k(__builtin_bit_cast(s16x4, a), __builtin_bit_cast(s16x4, b), c, 0, 0, 0);
}
__device__ __forceinline__ bf16x8 lds_b128(LAS char* p) { return *(LAS bf16x8*)p; }
__device__ __forceinline__ bf16x8 lds_tr2(LAS char* p) {
    bf16x4 t0 = __builtin_amdgcn_ds_read_tr16_b64_v4bf16((LAS bf16x4*)p);
    bf16x4 t1 = __builtin_amdgcn_ds_read_tr16_b64_v4bf16((LAS bf16x4*)(p + 512));
    return __builtin_shufflevector(t0, t1, 0, 1, 2, 3, 4, 5, 6, 7);
}
__device__ __forceinline__ unsigned cvtpk(float a, float b) {
    f32x2 v = {a, b};
    unsigned u = __builtin_bit_cast(unsigned, __builtin_convertvector(v, bf16x2));
    asm("" : "+v"(u));
    return u;
}
__device__ __forceinline__ u32x2 cvt4u(f32x4 v) { u32x2 r = {cvtpk(v[0], v[1]), cvtpk(v[2], v[3])}; return r; }
__device__ __forceinline__ u32x4 pack8(f32x4 lo, f32x4 hi) {
    u32x4 r = {cvtpk(lo[0], lo[1]), cvtpk(lo[2], lo[3]), cvtpk(hi[0], hi[1]), cvtpk(hi[2], hi[3])};
    return r;
}
__device__ __forceinline__ void unpack8_add(u32x4 v, f32x4& lo, f32x4& hi) {
    unsigned a = v[0], b = v[1], c = v[2], d = v[3];
    lo[0] += __uint_as_float(a << 16); lo[1] += __uint_as_float(a & 0xffff0000u);
    lo[2] += __uint_as_float(b << 16); lo[3] += __uint_as_float(b & 0xffff0000u);
    hi[0] += __uint_as_float(c << 16); hi[1] += __uint_as_float(c & 0xffff0000u);
    hi[2] += __uint_as_float(d << 16); hi[3] += __uint_as_float(d & 0xffff0000u);
}
__device__ __forceinline__ float fmx(float a, float b) { return __builtin_elementwise_maximum(a, b); }
__device__ __forceinline__ void swap16(float& a, float& b) {
    auto r = __builtin_amdgcn_permlane16_swap(__float_as_uint(a), __float_as_uint(b), false, false);
    unsigned x = r[0], y = r[1]; a = __uint_as_float(x); b = __uint_as_float(y);
}
__device__ __forceinline__ void swap32(float& a, float& b) {
    auto r = __builtin_amdgcn_permlane32_swap(__float_as_uint(a), __float_as_uint(b), false, false);
    unsigned x = r[0], y = r[1]; a = __uint_as_float(x); b = __uint_as_float(y);
}
template <bool MAX> __device__ __forceinline__ float op2(float a, float b) { return MAX ? fmx(a, b) : a + b; }
template <bool MAX> __device__ __forceinline__ void allred4(float* v) {
    swap16(v[0], v[1]); float c = op2<MAX>(v[0], v[1]);
    swap16(v[2], v[3]); float d = op2<MAX>(v[2], v[3]);
    swap32(c, d); float e = op2<MAX>(c, d);
    if (!MAX) e = __builtin_amdgcn_rcpf(e);
    float e2 = e; swap32(e, e2);
    float f = e; swap16(e, f);
    float h = e2; swap16(e2, h);
    v[0] = e; v[1] = f; v[2] = e2; v[3] = h;
}
__device__ __forceinline__ unsigned ld_flag(GAS unsigned* f) {
    return __builtin_amdgcn_readfirstlane(__hip_atomic_load(f, __ATOMIC_RELAXED, __HIP_MEMORY_SCOPE_AGENT));
}
__device__ __forceinline__ void add_flag(GAS unsigned* f, int lane) {
    if (lane == 0) (void)__hip_atomic_fetch_add(f, 1u, __ATOMIC_RELAXED, __HIP_MEMORY_SCOPE_AGENT);
}
#define LD_FLAG_ASYNC(REG, f) asm volatile("global_load_dword " REG ", %0, off sc1" ::"v"(f) : "memory", REG)
#define RD_FLAG(REG, out) asm volatile("v_readfirstlane_b32 %0, " REG : "=s"(out)::"memory")
#define RD_VEC(REG, out) asm volatile("v_mov_b32 %0, " REG : "=v"(out)::"memory")
__device__ __forceinline__ void add_flag_async(GAS unsigned* f, int lane) {
    unsigned one = 1u;
    if (lane == 0) asm volatile("global_atomic_add %0, %1, off" ::"v"(f), "v"(one) : "memory");
}
__device__ __forceinline__ void wait_ge(GAS unsigned* f, unsigned target, GAS unsigned* hb, int lane) {
    unsigned spins = 0;
    while (ld_flag(f) < target) {
        ++spins;
        if ((spins & 127u) == 0u) {
            if (lane < 8) (void)__hip_atomic_fetch_add(hb + lane * FW, 1u, __ATOMIC_RELAXED, __HIP_MEMORY_SCOPE_AGENT);
            if (spins > SPIN_CAP) break;
        }
        __builtin_amdgcn_s_sleep(1);
    }
    asm volatile("" ::: "memory");
}
struct Frag { bf16x8 a[4], b[4]; };
#define SGB() do { _Pragma("unroll") for (int _i = 0; _i < 8; ++_i) { __builtin_amdgcn_sched_group_barrier(0x8, 1, 0); __builtin_amdgcn_sched_group_barrier(0x100, 1, 0); } __builtin_amdgcn_sched_group_barrier(0x8, 8, 0); } while (0)
__device__ __forceinline__ void nt_read(Frag& f, LAS char* pa, LAS char* pb) {
    f.a[0] = lds_b128(pa);
#pragma unroll
    for (int n = 0; n < 4; ++n) f.b[n] = lds_b128(pb + n * 2048);
#pragma unroll
    for (int m = 1; m < 4; ++m) f.a[m] = lds_b128(pa + m * 2048);
}
__device__ __forceinline__ void nt_mma(f32x4 (&acc)[4][4], const Frag& f) {
#pragma unroll
    for (int m = 0; m < 4; ++m)
#pragma unroll
        for (int n = 0; n < 4; ++n) acc[m][n] = mfma32(f.a[m], f.b[n], acc[m][n]);
}
__device__ __forceinline__ void bar_cons() {
    asm volatile("" ::: "memory");
    __builtin_amdgcn_s_waitcnt(0x0070);
    __builtin_amdgcn_s_barrier();
    asm volatile("" ::: "memory");
}
#define SGBV() do { _Pragma("unroll") for (int _i = 0; _i < 8; ++_i) { __builtin_amdgcn_sched_group_barrier(0x8, 1, 0); __builtin_amdgcn_sched_group_barrier(0x100, 1, 0); } \
    _Pragma("unroll") for (int _i = 0; _i < 2; ++_i) { __builtin_amdgcn_sched_group_barrier(0x8, 1, 0); __builtin_amdgcn_sched_group_barrier(0x4, 2, 0); } \
    _Pragma("unroll") for (int _i = 0; _i < 2; ++_i) { __builtin_amdgcn_sched_group_barrier(0x8, 1, 0); __builtin_amdgcn_sched_group_barrier(0x2, 1, 0); } \
    __builtin_amdgcn_sched_group_barrier(0x8, 4, 0); } while (0)
__device__ __forceinline__ void nt_pin(f32x4 (&acc)[4][4]) {
    asm volatile("" : "+v"(acc[0][0]), "+v"(acc[0][1]), "+v"(acc[0][2]), "+v"(acc[0][3]), "+v"(acc[1][0]), "+v"(acc[1][1]), "+v"(acc[1][2]), "+v"(acc[1][3]));
    asm volatile("" : "+v"(acc[2][0]), "+v"(acc[2][1]), "+v"(acc[2][2]), "+v"(acc[2][3]), "+v"(acc[3][0]), "+v"(acc[3][1]), "+v"(acc[3][2]), "+v"(acc[3][3]));
}
template <bool LASTBAR>
__device__ __forceinline__ int nt_consume(f32x4 (&acc)[4][4], LAS char* smem, int slot, int nst, int w, int lane) {
    const int r = lane & 15, g = lane >> 4;
    const int c0 = ((g ^ (r >> 1)) << 4), c1 = c0 ^ 64;
    const int oa = ((w & 1) * 64 + r) * 128, ob = 16384 + ((w >> 1) * 64 + r) * 128;
    const int oa0 = oa + c0, ob0 = ob + c0, oa1 = oa + c1, ob1 = ob + c1;
    Frag f0, f1;
    int so = slot * SLOT;
    nt_read(f0, smem + so + oa0, smem + so + ob0);
    int pa = so + oa1, pb = so + ob1;
#pragma unroll 1
    for (int it = 0; it < nst - 1; ++it) {
        __builtin_amdgcn_sched_barrier(0);
        nt_read(f1, smem + pa, smem + pb);
        nt_mma(acc, f0);
        so = (so == 4 * SLOT) ? 0 : so + SLOT;
        int qa = so + oa0, qb = so + ob0;
        SGBV();
        __builtin_amdgcn_sched_barrier(0);
        asm volatile("" : "+v"(qa), "+v"(qb));
        bar_cons();
        __builtin_amdgcn_sched_barrier(0);
        nt_read(f0, smem + qa, smem + qb);
        nt_mma(acc, f1);
        pa = so + oa1; pb = so + ob1;
        SGBV();
        __builtin_amdgcn_sched_barrier(0);
        asm volatile("" : "+v"(pa), "+v"(pb));
    }
    nt_read(f1, smem + pa, smem + pb);
    nt_mma(acc, f0);
    SGB();
    __builtin_amdgcn_sched_barrier(0);
    so = (so == 4 * SLOT) ? 0 : so + SLOT;
    if (LASTBAR) bar_cons();
    __builtin_amdgcn_sched_barrier(0);
    nt_mma(acc, f1);
    return so / SLOT;
}

__device__ __forceinline__ void t2_pin(f32x4 (&aq)[8], f32x4 (&ak)[9], f32x4 (&av)[9]) {
    asm volatile("" : "+v"(aq[0]), "+v"(aq[1]), "+v"(aq[2]), "+v"(aq[3]), "+v"(aq[4]), "+v"(aq[5]), "+v"(aq[6]), "+v"(aq[7]),
                      "+v"(ak[0]), "+v"(ak[1]), "+v"(ak[2]), "+v"(ak[3]), "+v"(ak[4]));
    asm volatile("" : "+v"(ak[5]), "+v"(ak[6]), "+v"(ak[7]), "+v"(ak[8]),
                      "+v"(av[0]), "+v"(av[1]), "+v"(av[2]), "+v"(av[3]), "+v"(av[4]), "+v"(av[5]), "+v"(av[6]), "+v"(av[7]), "+v"(av[8]));
}
struct Frag2 { bf16x8 x[9], w[3]; };
__device__ __forceinline__ void t2_read(Frag2& f, LAS char* px, LAS char* pw) {
    f.w[1] = lds_tr2(pw + 1024);
    f.x[0] = lds_b128(px);
    f.w[2] = lds_tr2(pw + 2048);
    f.w[0] = lds_tr2(pw);
#pragma unroll
    for (int rb = 1; rb < 9; ++rb) f.x[rb] = lds_b128(px + rb * 2048);
}
__device__ __forceinline__ void t2_mma(f32x4 (&aq)[8], f32x4 (&ak)[9], f32x4 (&av)[9], const Frag2& f) {
#pragma unroll
    for (int rb = 0; rb < 9; ++rb) {
        ak[rb] = mfma32(f.w[1], f.x[rb], ak[rb]);
        av[rb] = mfma32(f.x[rb], f.w[2], av[rb]);
        if (rb > 0) aq[rb - 1] = mfma32(f.w[0], f.x[rb], aq[rb - 1]);
    }
}
#define SGB2() do { _Pragma("unroll") for (int _i = 0; _i < 15; ++_i) { __builtin_amdgcn_sched_group_barrier(0x8, 1, 0); __builtin_amdgcn_sched_group_barrier(0x100, 1, 0); } \
    _Pragma("unroll") for (int _i = 0; _i < 2; ++_i) { __builtin_amdgcn_sched_group_barrier(0x8, 1, 0); __builtin_amdgcn_sched_group_barrier(0x4, 2, 0); } \
    _Pragma("unroll") for (int _i = 0; _i < 2; ++_i) { __builtin_amdgcn_sched_group_barrier(0x8, 1, 0); __builtin_amdgcn_sched_group_barrier(0x2, 1, 0); } \
    __builtin_amdgcn_sched_group_barrier(0x8, 7, 0); } while (0)

struct Frag4 { bf16x8 w[4][2], o[4][2]; };
#define SGB4() do { _Pragma("unroll") for (int _i = 0; _i < 12; ++_i) { __builtin_amdgcn_sched_group_barrier(0x100, 2, 0); __builtin_amdgcn_sched_group_barrier(0x8, 1, 0); } __builtin_amdgcn_sched_group_barrier(0x8, 4, 0); } while (0)
__device__ __forceinline__ void pf(rsrc_t r, int voff, int soff) {
    asm volatile("s_nop 4\n\tbuffer_load_dword v255, %0, %1, %2 offen" ::"v"(voff), "s"(r), "s"(__builtin_amdgcn_readfirstlane(soff)) : "memory", "v255");
}

__global__ __launch_bounds__(512, 2) void fused(const __bf16* __restrict__ xa, const __bf16* __restrict__ cre, const __bf16* __restrict__ cim,
                                                const __bf16* __restrict__ hre, const __bf16* __restrict__ him, const __bf16* __restrict__ xb,
                                                const __bf16* __restrict__ wqkv, const __bf16* __restrict__ wo,
                                                char* __restrict__ ws, __bf16* __restrict__ out) {
    __shared__ __attribute__((aligned(1024))) char smem_[163840];
    LAS char* smem = (LAS char*)smem_;
    asm volatile("s_load_dword s98, %0, 0x0\n\ts_load_dword s99, %0, 0x40" ::"s"(__builtin_amdgcn_kernarg_segment_ptr()) : "s98", "s99");
    if ((blockIdx.x >> 3) == 4) {
        unsigned long long pc; int len;
        asm volatile("s_getpc_b64 %0\n.Lfnet_pc%=:\n\ts_mov_b32 %1, fnet_code_end-.Lfnet_pc%=-4" : "=s"(pc), "=s"(len));
        const int o = threadIdx.x * 128;
        if (o <= len) { unsigned v = *(const GAS unsigned*)(pc + (unsigned)o); asm volatile("" ::"v"(v)); }
    }
    const int tid = threadIdx.x, lane = tid & 63;
    const int wave = __builtin_amdgcn_readfirstlane(tid >> 6);
    const int bid = blockIdx.x, xcd = bid & 7, idx = bid >> 3;
    const int q = xcd & 3, th = xcd >> 2;
    const int ttl = idx >> 2, et = idx & 3, tt = th * 8 + ttl;
    const int t0 = tt * 128, e0 = et * 128;
    const bool typeA = (ttl & 1) == 0;
    const int s1 = typeA ? ((q & 1) * 8 + th * 4 + (ttl >> 1)) * 128 : ((xcd >> 1) * 4 + (ttl >> 1)) * 128;
    const int hg = (xcd & 1) * 4 + et;
    const int s4 = (xcd * 4 + (idx >> 3)) * 64, n4 = (idx & 7) * 64;
    GAS unsigned* flags = (GAS unsigned*)(ws + WS_FLAGS);
    const int slot3 = typeA ? 3 : 0;
    const int rot1 = 4 * (((ttl >> 1) + et) & 1), rot3 = 4 * et;

    if (__builtin_expect(wave >= 6, 1)) {
        const int lw = wave - 6;
        const int row = 8 * lw + (lane >> 3);
        const int ch = ((lane & 7) ^ (4 * lw + (lane >> 4))) * 16;
        const int cht = ((lane & 7) ^ (2 * (((lane >> 4) & 1) | (lw << 1)))) * 16;
        const bool master = (bid < 8 && lw == 0);
        GAS unsigned* myhb = flags + bid * FW;
        if (master) {
#pragma unroll
            for (int i = 0; i < NCNT; i += 64)
                if (i + lane < NCNT) __hip_atomic_store(flags + (8 + bid * NCNT + i + lane) * FW, 0u, __ATOMIC_RELAXED, __HIP_MEMORY_SCOPE_AGENT);
        }
        rsrc_t rc = mk_rsrc((q >> 1) ? cim : cre, SEQ * SEQ * 2);
        const int vb = row * 4096 + ch;
        const int sb = t0 * 4096 + (q & 1) * 2048;
        auto issueC = [&](int st, int slot) {
            const int so = sb + ((st + rot3) & 15) * 128;
#pragma unroll
            for (int u = 0; u < 8; ++u) glds<0>(rc, smem + slot * SLOT + 16384 + (lw + 2 * u) * 1024, vb, so + 16 * ((u & 4) | ((u + q) & 3)) * 4096);
        };
        rsrc_t rw = mk_rsrc(wo, DM * DM * 2);
        int slot = 0;
        if (typeA) {
            rsrc_t rh = mk_rsrc((q >> 1) ? him : hre, DM * DM * 2);
            const int vh = row * 1024 + ch;
#pragma unroll 1
            for (int st = 0; st < 8; ++st) {
                if (st >= 2) bar_vm<8>();
                if (master && st >= 2 && st < 5) add_flag_async(myhb, lane);
                const int so = e0 * 1024 + ((st + rot1) & 7) * 128;
#pragma unroll
                for (int u = 0; u < 8; ++u) glds<0>(rh, smem + slot * SLOT + 16384 + (lw + 2 * u) * 1024, vh, so + 16 * u * 1024);
                slot = (slot + 1 == 5) ? 0 : slot + 1;
            }
        } else {
            rsrc_t rq = mk_rsrc(wqkv, DM * 3 * DM * 2);
            const int vq = row * 3072 + cht;
            int pos = 1;
#pragma unroll 1
            for (int st = 0; st < 8; ++st) {
                if (st >= 2) bar_vm<12>();
                LAS char* sl = smem + T2_BASE + pos * T2_SLOT;
                const int so = ((st + rot1) & 7) * 64 * 3072 + hg * 128;
#pragma unroll
                for (int v = 0; v < 4; ++v)
#pragma unroll
                    for (int cg = 0; cg < 3; ++cg)
                        glds<0>(rq, sl + 18432 + ((lw + 2 * v) * 3 + cg) * 1024, vq, so + 16 * v * 3072 + cg * 1024);
                pos = (pos + 1 == 3) ? 0 : pos + 1;
            }
        }
#pragma unroll 1
        for (int st = 0; st < 16; ++st) {
            if (typeA) {
                if (st == 2) { bar_only(); bar_only(); if (master) add_flag_async(myhb, lane); }
                bar_vm<8>();
            } else if (st < 5) {
                if (st == 1) bar_vm<20>();
                if (st == 2) bar_vm<16>();
                if (st == 3) bar_only();
            } else {
                if (st == 5) { bar_vm<32>(); bar_vm<24>(); bar_vm<16>(); }
                bar_vm<8>();
            }
            issueC(st, slot);
            slot = (slot + 1 == 5) ? 0 : slot + 1;
            if (st == 7) pf(rw, lane * 128, idx * 16384 + lw * 8192);
        }
        const int vw = row * 1024 + cht;
#pragma unroll 1
        for (int st = 0; st < 4; ++st) {
            if (st < 2) bar_vm<8>();
            if (st == 2) bar_vm<0>();
#pragma unroll
            for (int h = 0; h < 2; ++h)
#pragma unroll
                for (int u = 0; u < 4; ++u)
                    glds<0>(rw, smem + slot * SLOT + h * 16384 + 8192 + (lw + 2 * u) * 1024, vw, ((st * 2 + h) * 64 + 16 * u) * 1024 + n4 * 2);
            slot = (slot + 1 == 5) ? 0 : slot + 1;
        }
        bar_vm<0>();
    } else if (__builtin_expect(wave >= 4, 1)) {
        const int lw = wave - 4;
        const int ch = ((lane & 7) ^ (4 * lw + (lane >> 4))) * 16;
        const int prow = 8 * (lane >> 5) + ((lane >> 3) & 3) + 16 * lw;
        const int row = 8 * lw + (lane >> 3);
        GAS unsigned* hbp = flags + (lane & 7) * FW;
        if (!typeA) {
            rsrc_t rx = mk_rsrc(xb, SEQ * DM * 2);
            const int vx = row * 1024 + ch;
            const int sxm = s1 * 1024, sxh = (s1 >= 16 ? s1 - 16 : 0) * 1024;
            int pos = 1;
#pragma unroll 1
            for (int st = 0; st < 8; ++st) {
                if (st >= 2) bar_vm<9>();
                if (st == 0 && lw == 0) LD_FLAG_ASYNC("v254", hbp);
                if (st == 7 && lw == 0) LD_FLAG_ASYNC("v253", hbp);
                LAS char* sl = smem + T2_BASE + pos * T2_SLOT;
                const int ko = ((st + rot1) & 7) * 128;
                glds<0>(rx, sl + lw * 1024, vx, sxh + ko);
#pragma unroll
                for (int u = 0; u < 8; ++u) glds<0>(rx, sl + 2048 + (lw + 2 * u) * 1024, vx, sxm + 16 * u * 1024 + ko);
                pos = (pos + 1 == 3) ? 0 : pos + 1;
            }
            bar_vm<9>();
        }
        rsrc_t ro = mk_rsrc(ws + WS_O, SEQ * DM * 2);
        const int vo = row * 32 + (ch & 16) + (ch >> 5) * 65536;
        GAS unsigned* f2 = flags + (F2_IDX + (s4 >> 7)) * FW;
        int slot = 0;
#pragma unroll 1
        for (int pass = typeA ? 0 : 1; pass < 2; ++pass) {
            if (pass) {
                bar_vm<0>();
                if (lw == 0) {
                    unsigned a, b, spins = 0;
                    RD_VEC("v254", a); RD_VEC("v253", b);
                    bool moved = (a != b) || ((lane & 7) == bid);
                    while (!__all(moved)) {
                        if (bid < 8) add_flag(flags + bid * FW, lane);
                        __builtin_amdgcn_s_sleep(1);
                        b = __hip_atomic_load(hbp, __ATOMIC_RELAXED, __HIP_MEMORY_SCOPE_AGENT);
                        moved = moved || (a != b);
                        if (++spins > SPIN_CAP) break;
                    }
                }
                bar_only();
                if (typeA) {
                    bar_only();
                    if (lw == 0 && lane < R1) (void)__hip_atomic_fetch_add(flags + (F1_IDX + (q * 4 + et) * R1 + lane) * FW, 1u, __ATOMIC_RELAXED, __HIP_MEMORY_SCOPE_AGENT);
                }
                wait_ge(flags + (F1_IDX + (q * 4 + et) * R1 + ((lw + 2 * th + 4 * (ttl >> 1) + 16 * (ttl & 1)) & (R1 - 1))) * FW, 8u, flags, lane);
            }
            rsrc_t ra = mk_rsrc(pass ? (const void*)(ws + WS_ABT) : (const void*)xa, pass ? 1024 * SEQ * 2 : SEQ * DM * 2);
            const int va = pass ? prow * 64 + (ch & 48) + (ch >> 6) * 8192 : prow * 1024 + ch;
            const int sa = pass ? (((q >> 1) * 4 + et) * 64 + (q & 1) * 32) * 8192 : s1 * 1024;
            const int rot = pass ? rot3 : rot1, nst = pass ? 16 : 8, ksh = pass ? 14 : 7, rb = pass ? 64 : 1024;
#pragma unroll 1
            for (int st = 0; st < nst; ++st) {
                if (st >= 2) bar_vm<8>();
                if (!pass) {
                    if (st == 0 && lw == 0) LD_FLAG_ASYNC("v254", hbp);
                    if (st == 7 && lw == 0) LD_FLAG_ASYNC("v253", hbp);
                } else if (st == 3 && lw == 1 && !typeA) add_flag_async(flags + (F2_IDX + (s1 >> 7)) * FW, lane);
                const int so = sa + (((st + rot) & (nst - 1)) << ksh);
#pragma unroll
                for (int u = 0; u < 8; ++u) glds<SC1>(ra, smem + slot * SLOT + (lw + 2 * u) * 1024, va, so + (32 * (u >> 1) + 4 * (u & 1)) * rb);
                slot = (slot + 1 == 5) ? 0 : slot + 1;
                if (st == 13) LD_FLAG_ASYNC("v252", f2);
            }
            if (!pass) bar_vm<8>();
        }
#pragma unroll 1
        for (int st = 0; st < 4; ++st) {
            if (st < 2) bar_vm<8>();
            if (st == 2) bar_vm<0>();
            if (st == 0) {
                unsigned f2v;
                RD_FLAG("v252", f2v);
                if (f2v < 8u) wait_ge(f2, 8u, flags, lane);
            }
#pragma unroll
            for (int h = 0; h < 2; ++h)
#pragma unroll
                for (int u = 0; u < 4; ++u)
                    glds<SC1>(ro, smem + slot * SLOT + h * 16384 + (lw + 2 * u) * 1024, vo, (s4 + 16 * u) * 32 + (st * 2 + h) * 4 * 65536);
            slot = (slot + 1 == 5) ? 0 : slot + 1;
        }
        bar_vm<0>();
    } else {
        const int r = lane & 15, g = lane >> 4;
        const int tile = tt * 4 + et;
        rsrc_t rp = mk_rsrc(ws + WS_PART, 64 * 16 * 4 * 2048);
        const int inb = (tile * 4 + q) * 32768 + wave * 2048 + lane * 16;
        if (typeA) {
            f32x4 acc[4][4] = {};
            nt_pin(acc);
            bar_lgkm();
            nt_consume<true>(acc, smem, 0, 8, wave, lane);
            const unsigned sg = (q >> 1) ? 0x80008000u : 0u;
            rsrc_t rs = mk_rsrc(ws + WS_ABT, 1024 * SEQ * 2);
            const int vs = r * 64 + g * 16;
            const int ss = (((q >> 1) * 4 + et) * 64 + (s1 >> 5) + (wave & 1) * 2) * 8192 + (wave >> 1) * 4096;
#pragma unroll
            for (int n = 0; n < 4; ++n)
#pragma unroll
                for (int u = 0; u < 2; ++u)
                    __builtin_amdgcn_raw_buffer_store_b128(pack8(acc[2 * u][n], acc[2 * u + 1][n]) ^ sg, rs, vs + ss + n * 1024 + u * 8192, 0, SC1);
            bar_vm<0>();
        } else {
            const int c0 = ((g ^ (r >> 1)) << 4);
            const int ox = r * 128;
            const int qq = (lane & 15) >> 2, p = lane & 3;
            const int ow = 18432 + g * 3072 + qq * 128 + (((2 * wave + (p >> 1)) ^ (2 * ((qq >> 1) | ((g & 1) << 1)))) << 4) + 8 * (p & 1);
            f32x4 aq[8] = {}, ak[9] = {}, av[9] = {};
            Frag2 f0, f1;
            int so = T2_BASE + T2_SLOT;
            t2_pin(aq, ak, av);
            bar_lgkm();
            t2_read(f0, smem + so + ox + c0, smem + so + ow);
            int px = so + ox + (c0 ^ 64), pw = so + ow;
#pragma unroll 1
            for (int it = 0; it < 7; ++it) {
                __builtin_amdgcn_sched_barrier(0);
                t2_read(f1, smem + px, smem + pw + 12288);
                t2_mma(aq, ak, av, f0);
                so = (so == T2_BASE + 2 * T2_SLOT) ? T2_BASE : so + T2_SLOT;
                int qx = so + ox + c0, qw = so + ow;
                SGB2();
                __builtin_amdgcn_sched_barrier(0);
                asm volatile("" : "+v"(qx), "+v"(qw));
                t2_pin(aq, ak, av);
                bar_cons();
                __builtin_amdgcn_sched_barrier(0);
                t2_read(f0, smem + qx, smem + qw);
                t2_mma(aq, ak, av, f1);
                px = qx ^ 64; pw = qw;
                SGB2();
                __builtin_amdgcn_sched_barrier(0);
                asm volatile("" : "+v"(px));
            }
            t2_read(f1, smem + px, smem + pw + 12288);
            t2_mma(aq, ak, av, f0);
            SGB2();
            __builtin_amdgcn_sched_barrier(0);
            t2_pin(aq, ak, av);
            bar_cons();
            __builtin_amdgcn_sched_barrier(0);
            t2_mma(aq, ak, av, f1);
            const int j = r, par = j & 1;
            const bool uc0 = (4 * g + par) <= j, uc1 = (4 * g + par + 2) <= j;
            const unsigned s0v = par ? 0x01000c0cu : 0x0c0c0100u, s1v = par ? 0x03020c0cu : 0x0c0c0302u, zs = 0x0c0c0c0cu;
            const unsigned selc0 = uc0 ? s0v : zs, selp0 = uc0 ? zs : s0v, selc1 = uc1 ? s1v : zs, selp1 = uc1 ? zs : s1v;
            rsrc_t rs = mk_rsrc(ws + WS_O, SEQ * DM * 2);
            const int vs = j * 32 + g * 8;
            const int ss = (hg * 4 + wave) * 65536 + s1 * 32;
            const f32x4 z = {0.f, 0.f, 0.f, 0.f};
            const float C = 0.25f * 1.44269504088896f;
            u32x2 kb[9], vb[9];
            float l0[8], l1[8], m[8];
#pragma unroll
            for (int i = 0; i < 9; ++i) kb[i] = cvt4u(ak[i]);
#pragma unroll
            for (int b = 0; b < 8; ++b) {
                u32x2 qb = cvt4u(aq[b]);
                f32x4 sc = mfma16(kb[b + 1], qb, z), sp = mfma16(kb[b], qb, z);
                float c0v = par ? sc[1] : sc[0], c1v = par ? sc[3] : sc[2];
                float p0v = par ? sp[1] : sp[0], p1v = par ? sp[3] : sp[2];
                if (b == 0 && s1 == 0) { p0v = -4e9f; p1v = -4e9f; }
                unsigned pk = cvtpk(uc0 ? c0v : p0v, uc1 ? c1v : p1v);
                l0[b] = __uint_as_float(pk << 16); l1[b] = __uint_as_float(pk & 0xffff0000u);
                m[b] = fmx(l0[b], l1[b]);
            }
            allred4<true>(m); allred4<true>(m + 4);
#pragma unroll
            for (int i = 0; i < 9; ++i) vb[i] = cvt4u(av[i]);
#pragma unroll
            for (int b = 0; b < 8; ++b) {
                l0[b] = __builtin_amdgcn_exp2f((l0[b] - m[b]) * C); l1[b] = __builtin_amdgcn_exp2f((l1[b] - m[b]) * C);
                m[b] = l0[b] + l1[b];
            }
            allred4<false>(m); allred4<false>(m + 4);
            f32x4 ov[8];
#pragma unroll
            for (int b = 0; b < 8; ++b) {
                unsigned P = cvtpk(l0[b] * m[b], l1[b] * m[b]);
                u32x2 pc = {__builtin_amdgcn_perm(0u, P, selc0), __builtin_amdgcn_perm(0u, P, selc1)};
                u32x2 pp = {__builtin_amdgcn_perm(0u, P, selp0), __builtin_amdgcn_perm(0u, P, selp1)};
                ov[b] = mfma16(vb[b], pp, mfma16(vb[b + 1], pc, z));
            }
#pragma unroll
            for (int b = 0; b < 8; ++b) __builtin_amdgcn_raw_buffer_store_b64(cvt4u(ov[b]), rs, vs, ss + b * 512, SC1);
        }
        f32x4 acc[4][4] = {};
        nt_pin(acc);
        bar_lgkm();
        int slot = nt_consume<false>(acc, smem, slot3, 16, wave, lane);
        u32x4 sv[3][2];
#pragma unroll
        for (int n = 1; n < 4; ++n) { sv[n - 1][0] = pack8(acc[0][n], acc[1][n]); sv[n - 1][1] = pack8(acc[2][n], acc[3][n]); }
        auto send = [&](int n) {
            const int base = (((tile * 4 + ((q + n) & 3)) * 4 + q) * 4 + wave) * 2048 + lane * 16;
            __builtin_amdgcn_raw_buffer_store_b128(sv[n - 1][0], rp, base, 0, SC1);
            __builtin_amdgcn_raw_buffer_store_b128(sv[n - 1][1], rp, base + 1024, 0, SC1);
        };
        send(1); send(2); send(3);
        f32x4 own[4];
#pragma unroll
        for (int m = 0; m < 4; ++m) own[m] = acc[m][0];
        f32x4 a4[2][2] = {};
        rsrc_t rout = mk_rsrc(out, SEQ * 1024 * 2);
        u32x4 pv[3][2];
        {
            const int c0 = ((g ^ (r >> 1)) << 4);
            const int oo = ((wave >> 1) * 32 + r) * 128;
            const int qq = (lane & 15) >> 2, p = lane & 3;
            const int swz = 2 * ((qq >> 1) | ((g & 1) << 1));
            const int ow0 = 8192 + g * 1024 + qq * 128 + ((((wave & 1) * 4 + (p >> 1)) ^ swz) << 4) + 8 * (p & 1);
            const int ow1 = ow0 ^ 32;
            Frag4 h0, h1;
            auto rd = [&](Frag4& f) {
                LAS char* sl = smem + slot * SLOT;
#pragma unroll
                for (int i = 0; i < 4; ++i) {
                    const int ho = (i >> 1) * 16384, kk = i & 1;
                    f.w[i][0] = lds_tr2(sl + ho + ow0 + kk * 4096);
                    f.o[i][0] = lds_b128(sl + ho + oo + (c0 ^ (kk * 64)));
                    f.o[i][1] = lds_b128(sl + ho + oo + (c0 ^ (kk * 64)) + 2048);
                    f.w[i][1] = lds_tr2(sl + ho + ow1 + kk * 4096);
                }
                slot = (slot + 1 == 5) ? 0 : slot + 1;
            };
            auto mm = [&](const Frag4& f) {
#pragma unroll
                for (int i = 0; i < 4; ++i) {
                    a4[0][0] = mfma32(f.w[i][0], f.o[i][0], a4[0][0]);
                    a4[0][1] = mfma32(f.w[i][0], f.o[i][1], a4[0][1]);
                    a4[1][0] = mfma32(f.w[i][1], f.o[i][0], a4[1][0]);
                    a4[1][1] = mfma32(f.w[i][1], f.o[i][1], a4[1][1]);
                }
                asm volatile("" : "+v"(a4[0][0]), "+v"(a4[0][1]), "+v"(a4[1][0]), "+v"(a4[1][1]));
            };
            asm volatile("" : "+v"(own[0]), "+v"(own[1]), "+v"(own[2]), "+v"(own[3]));
            bar_only();
            __builtin_amdgcn_sched_barrier(0);
            rd(h0);
            __builtin_amdgcn_sched_barrier(0);
            rd(h1); mm(h0); SGB4();
            __builtin_amdgcn_sched_barrier(0);
            drain_vm();
            if (lane < 4 && lane != q)
                (void)__hip_atomic_fetch_add(flags + (F3_IDX + tile * 4 + lane) * FW, 1u, __ATOMIC_RELAXED, __HIP_MEMORY_SCOPE_AGENT);
            mm(h1);
            wait_ge(flags + (F3_IDX + tile * 4 + q) * FW, 12u, flags, lane);
#pragma unroll
            for (int k = 0; k < 3; ++k) {
                const int o = inb + ((q + 1 + k) & 3) * 8192;
                pv[k][0] = __builtin_amdgcn_raw_buffer_load_b128(rp, o, 0, SC1);
                pv[k][1] = __builtin_amdgcn_raw_buffer_load_b128(rp, o + 1024, 0, SC1);
            }
            bar_only();
            __builtin_amdgcn_sched_barrier(0);
            rd(h0);
            __builtin_amdgcn_sched_barrier(0);
            rd(h1); mm(h0); SGB4();
            __builtin_amdgcn_sched_barrier(0);
            mm(h1);
#pragma unroll
            for (int n = 0; n < 2; ++n) {
                u32x2 lo = cvt4u(a4[0][n]), hi = cvt4u(a4[1][n]);
                auto r0 = __builtin_amdgcn_permlane16_swap(lo[0], hi[0], false, false);
                auto r1 = __builtin_amdgcn_permlane16_swap(lo[1], hi[1], false, false);
                u32x4 v = {r0[0], r1[0], r0[1], r1[1]};
                __builtin_amdgcn_raw_buffer_store_b128(v, rout, ((s4 + (wave >> 1) * 32 + n * 16 + r) * 1024 + 512 + n4 + (wave & 1) * 32 + (g & 1) * 16 + (g >> 1) * 8) * 2, 0, SC1);
            }
        }
#pragma unroll
        for (int k = 0; k < 3; ++k) {
            unpack8_add(pv[k][0], own[0], own[1]);
            unpack8_add(pv[k][1], own[2], own[3]);
        }
        const int trow = t0 + (wave >> 1) * 64 + q * 16 + r;
#pragma unroll
        for (int u = 0; u < 2; ++u)
            __builtin_amdgcn_raw_buffer_store_b128(pack8(own[2 * u], own[2 * u + 1]), rout, (trow * 1024 + e0 + (wave & 1) * 64 + 32 * u + 8 * g) * 2, 0, SC1);
    }
}

extern "C" __global__ void fnet_code_end(unsigned* p) { p[0] = 0; }

extern "C" void kernel_launch(void* const* d_in, const int* in_sizes, int n_in,
                              void* d_out, int out_size, void* d_ws, size_t ws_size,
                              hipStream_t stream) {
    fused<<<256, 512, 0, stream>>>((const __bf16*)d_in[0], (const __bf16*)d_in[1], (const __bf16*)d_in[2], (const __bf16*)d_in[3],
                                   (const __bf16*)d_in[4], (const __bf16*)d_in[5], (const __bf16*)d_in[6], (const __bf16*)d_in[7],
                                   (char*)d_ws, (__bf16*)d_out);
}
```

```cpp
#include <hip/hip_runtime.h>
#include <hip/hip_bf16.h>
#include <stdint.h>

#define LAS __attribute__((address_space(3)))
#define GAS __attribute__((address_space(1)))
typedef __attribute__((ext_vector_type(8))) __bf16 bf16x8;
typedef __attribute__((ext_vector_type(4))) __bf16 bf16x4;
typedef __attribute__((ext_vector_type(2))) __bf16 bf16x2;
typedef __attribute__((ext_vector_type(2))) float f32x2;
typedef __attribute__((ext_vector_type(4))) short s16x4;
typedef __attribute__((ext_vector_type(4))) float f32x4;
typedef __attribute__((ext_vector_type(4))) unsigned u32x4;
typedef __attribute__((ext_vector_type(2))) unsigned u32x2;
typedef __amdgpu_buffer_rsrc_t rsrc_t;

constexpr int SEQ = 2048, DM = 512;
constexpr int SLOT = 32768;
constexpr int T2_SLOT = 43008;
constexpr int T2_BASE = 32768;

constexpr unsigned WS_FLAGS = 0;
constexpr unsigned WS_ABT   = 1u << 20;
constexpr unsigned WS_O     = WS_ABT + (4u << 20);
constexpr unsigned WS_PART  = WS_O + (2u << 20);
constexpr int FW = 32;
constexpr int R1 = 16;
constexpr int F1_IDX = 8, F2_IDX = F1_IDX + 16 * R1, F3_IDX = F2_IDX + 16, NCNT = (16 * R1 + 16 + 256) / 8;
constexpr int SC1 = 16;
constexpr int MIDX = 5;
constexpr unsigned SPIN_CAP = 1u << 17;

__device__ __forceinline__ rsrc_t mk_rsrc(const void* p, unsigned bytes) {
    return __builtin_amdgcn_make_buffer_rsrc((void*)p, 0, bytes, 0x00020000);
}
template <int AUX>
__device__ __forceinline__ void glds(rsrc_t r, LAS char* dst, int voff, int soff) {
    __builtin_amdgcn_raw_ptr_buffer_load_lds(r, (LAS void*)dst, 16, voff, soff, 0, AUX);
}
template <int N> __device__ __forceinline__ void bar_vm() {
    asm volatile("s_waitcnt vmcnt(%0)\n\ts_barrier" ::"n"(N) : "memory");
}
__device__ __forceinline__ void bar_only() { asm volatile("s_barrier" ::: "memory"); }
__device__ __forceinline__ void bar_lgkm() {
    asm volatile("s_waitcnt lgkmcnt(0)\n\ts_barrier" ::: "memory");
}
__device__ __forceinline__ void drain_vm() { asm volatile("s_waitcnt vmcnt(0)" ::: "memory"); }
__device__ __forceinline__ f32x4 mfma32(bf16x8 a, bf16x8 b, f32x4 c) {
    return __builtin_amdgcn_mfma_f32_16x16x32_bf16(a, b, c, 0, 0, 0);
}
__device__ __forceinline__ f32x4 mfma16(u32x2 a, u32x2 b, f32x4 c) {
    return __builtin_amdgcn_mfma_f32_16x16x16bf16_1k(__builtin_bit_cast(s16x4, a), __builtin_bit_cast(s16x4, b), c, 0, 0, 0);
}
__device__ __forceinline__ bf16x8 lds_b128(LAS char* p) { return *(LAS bf16x8*)p; }
__device__ __forceinline__ bf16x8 lds_tr2(LAS char* p) {
    bf16x4 t0 = __builtin_amdgcn_ds_read_tr16_b64_v4bf16((LAS bf16x4*)p);
    bf16x4 t1 = __builtin_amdgcn_ds_read_tr16_b64_v4bf16((LAS bf16x4*)(p + 512));
    return __builtin_shufflevector(t0, t1, 0, 1, 2, 3, 4, 5, 6, 7);
}
__device__ __forceinline__ unsigned cvtpk(float a, float b) {
    f32x2 v = {a, b};
    unsigned u = __builtin_bit_cast(unsigned, __builtin_convertvector(v, bf16x2));
    asm("" : "+v"(u));
    return u;
}
__device__ __forceinline__ u32x2 cvt4u(f32x4 v) { u32x2 r = {cvtpk(v[0], v[1]), cvtpk(v[2], v[3])}; return r; }
__device__ __forceinline__ u32x4 pack8(f32x4 lo, f32x4 hi) {
    u32x4 r = {cvtpk(lo[0], lo[1]), cvtpk(lo[2], lo[3]), cvtpk(hi[0], hi[1]), cvtpk(hi[2], hi[3])};
    return r;
}
__device__ __forceinline__ void unpack8_add(u32x4 v, f32x4& lo, f32x4& hi) {
    unsigned a = v[0], b = v[1], c = v[2], d = v[3];
    lo[0] += __uint_as_float(a << 16); lo[1] += __uint_as_float(a & 0xffff0000u);
    lo[2] += __uint_as_float(b << 16); lo[3] += __uint_as_float(b & 0xffff0000u);
    hi[0] += __uint_as_float(c << 16); hi[1] += __uint_as_float(c & 0xffff0000u);
    hi[2] += __uint_as_float(d << 16); hi[3] += __uint_as_float(d & 0xffff0000u);
}
__device__ __forceinline__ float fmx(float a, float b) { return __builtin_elementwise_maximum(a, b); }
__device__ __forceinline__ void swap16(float& a, float& b) {
    auto r = __builtin_amdgcn_permlane16_swap(__float_as_uint(a), __float_as_uint(b), false, false);
    unsigned x = r[0], y = r[1]; a = __uint_as_float(x); b = __uint_as_float(y);
}
__device__ __forceinline__ void swap32(float& a, float& b) {
    auto r = __builtin_amdgcn_permlane32_swap(__float_as_uint(a), __float_as_uint(b), false, false);
    unsigned x = r[0], y = r[1]; a = __uint_as_float(x); b = __uint_as_float(y);
}
template <bool MAX> __device__ __forceinline__ float op2(float a, float b) { return MAX ? fmx(a, b) : a + b; }
template <bool MAX> __device__ __forceinline__ void allred4(float* v) {
    swap16(v[0], v[1]); float c = op2<MAX>(v[0], v[1]);
    swap16(v[2], v[3]); float d = op2<MAX>(v[2], v[3]);
    swap32(c, d); float e = op2<MAX>(c, d);
    if (!MAX) e = __builtin_amdgcn_rcpf(e);
    float e2 = e; swap32(e, e2);
    float f = e; swap16(e, f);
    float h = e2; swap16(e2, h);
    v[0] = e; v[1] = f; v[2] = e2; v[3] = h;
}
__device__ __forceinline__ unsigned ld_flag(GAS unsigned* f) {
    return __builtin_amdgcn_readfirstlane(__hip_atomic_load(f, __ATOMIC_RELAXED, __HIP_MEMORY_SCOPE_AGENT));
}
#define LD_FLAG_ASYNC(REG, f) asm volatile("global_load_dword " REG ", %0, off sc1" ::"v"(f) : "memory", REG)
#define RD_FLAG(REG, out) asm volatile("v_readfirstlane_b32 %0, " REG : "=s"(out)::"memory")
#define RD_VEC(REG, out) asm volatile("v_mov_b32 %0, " REG : "=v"(out)::"memory")
__device__ __forceinline__ void add_flag_async(GAS unsigned* f, int lane) {
    unsigned one = 1u;
    if (lane == 0) asm volatile("global_atomic_add %0, %1, off" ::"v"(f), "v"(one) : "memory");
}
__device__ __forceinline__ void hb_bump(GAS unsigned* flags, int mid, int lane) {
    unsigned one = 1u;
    GAS unsigned* f = flags + (lane & 7) * FW + mid;
    if (lane < 8) asm volatile("global_atomic_add %0, %1, off" ::"v"(f), "v"(one) : "memory");
}
__device__ __forceinline__ void wait_ge(GAS unsigned* f, unsigned target, GAS unsigned* hb, int lane) {
    unsigned spins = 0;
    while (ld_flag(f) < target) {
        ++spins;
        if ((spins & 127u) == 0u) {
            (void)__hip_atomic_fetch_add(hb + (lane >> 3) * FW + (lane & 7), 1u, __ATOMIC_RELAXED, __HIP_MEMORY_SCOPE_AGENT);
            if (spins > SPIN_CAP) break;
        }
        __builtin_amdgcn_s_sleep(1);
    }
    asm volatile("" ::: "memory");
}
struct Frag { bf16x8 a[4], b[4]; };
#define SGB() do { _Pragma("unroll") for (int _i = 0; _i < 8; ++_i) { __builtin_amdgcn_sched_group_barrier(0x8, 1, 0); __builtin_amdgcn_sched_group_barrier(0x100, 1, 0); } __builtin_amdgcn_sched_group_barrier(0x8, 8, 0); } while (0)
__device__ __forceinline__ void nt_read(Frag& f, LAS char* pa, LAS char* pb) {
    f.a[0] = lds_b128(pa);
#pragma unroll
    for (int n = 0; n < 4; ++n) f.b[n] = lds_b128(pb + n * 2048);
#pragma unroll
    for (int m = 1; m < 4; ++m) f.a[m] = lds_b128(pa + m * 2048);
}
__device__ __forceinline__ void nt_mma(f32x4 (&acc)[4][4], const Frag& f) {
#pragma unroll
    for (int m = 0; m < 4; ++m)
#pragma unroll
        for (int n = 0; n < 4; ++n) acc[m][n] = mfma32(f.a[m], f.b[n], acc[m][n]);
}
__device__ __forceinline__ void bar_cons() {
    asm volatile("" ::: "memory");
    __builtin_amdgcn_s_waitcnt(0x0070);
    __builtin_amdgcn_s_barrier();
    asm volatile("" ::: "memory");
}
#define SGBV() do { _Pragma("unroll") for (int _i = 0; _i < 8; ++_i) { __builtin_amdgcn_sched_group_barrier(0x8, 1, 0); __builtin_amdgcn_sched_group_barrier(0x100, 1, 0); } \
    _Pragma("unroll") for (int _i = 0; _i < 2; ++_i) { __builtin_amdgcn_sched_group_barrier(0x8, 1, 0); __builtin_amdgcn_sched_group_barrier(0x4, 2, 0); } \
    _Pragma("unroll") for (int _i = 0; _i < 2; ++_i) { __builtin_amdgcn_sched_group_barrier(0x8, 1, 0); __builtin_amdgcn_sched_group_barrier(0x2, 1, 0); } \
    __builtin_amdgcn_sched_group_barrier(0x8, 4, 0); } while (0)
__device__ __forceinline__ void nt_pin(f32x4 (&acc)[4][4]) {
    asm volatile("" : "+v"(acc[0][0]), "+v"(acc[0][1]), "+v"(acc[0][2]), "+v"(acc[0][3]), "+v"(acc[1][0]), "+v"(acc[1][1]), "+v"(acc[1][2]), "+v"(acc[1][3]));
    asm volatile("" : "+v"(acc[2][0]), "+v"(acc[2][1]), "+v"(acc[2][2]), "+v"(acc[2][3]), "+v"(acc[3][0]), "+v"(acc[3][1]), "+v"(acc[3][2]), "+v"(acc[3][3]));
}
template <bool LASTBAR>
__device__ __forceinline__ int nt_consume(f32x4 (&acc)[4][4], LAS char* smem, int slot, int nst, int w, int lane) {
    const int r = lane & 15, g = lane >> 4;
    const int c0 = ((g ^ (r >> 1)) << 4), c1 = c0 ^ 64;
    const int oa = ((w & 1) * 64 + r) * 128, ob = 16384 + ((w >> 1) * 64 + r) * 128;
    const int oa0 = oa + c0, ob0 = ob + c0, oa1 = oa + c1, ob1 = ob + c1;
    Frag f0, f1;
    int so = slot * SLOT;
    nt_read(f0, smem + so + oa0, smem + so + ob0);
    int pa = so + oa1, pb = so + ob1;
#pragma unroll 1
    for (int it = 0; it < nst - 1; ++it) {
        __builtin_amdgcn_sched_barrier(0);
        nt_read(f1, smem + pa, smem + pb);
        nt_mma(acc, f0);
        so = (so == 4 * SLOT) ? 0 : so + SLOT;
        int qa = so + oa0, qb = so + ob0;
        SGBV();
        __builtin_amdgcn_sched_barrier(0);
        asm volatile("" : "+v"(qa), "+v"(qb));
        bar_cons();
        __builtin_amdgcn_sched_barrier(0);
        nt_read(f0, smem + qa, smem + qb);
        nt_mma(acc, f1);
        pa = so + oa1; pb = so + ob1;
        SGBV();
        __builtin_amdgcn_sched_barrier(0);
        asm volatile("" : "+v"(pa), "+v"(pb));
    }
    nt_read(f1, smem + pa, smem + pb);
    nt_mma(acc, f0);
    SGB();
    __builtin_amdgcn_sched_barrier(0);
    so = (so == 4 * SLOT) ? 0 : so + SLOT;
    if (LASTBAR) bar_cons();
    __builtin_amdgcn_sched_barrier(0);
    nt_mma(acc, f1);
    return so / SLOT;
}

__device__ __forceinline__ void t2_pin(f32x4 (&aq)[8], f32x4 (&ak)[9], f32x4 (&av)[9]) {
    asm volatile("" : "+v"(aq[0]), "+v"(aq[1]), "+v"(aq[2]), "+v"(aq[3]), "+v"(aq[4]), "+v"(aq[5]), "+v"(aq[6]), "+v"(aq[7]),
                      "+v"(ak[0]), "+v"(ak[1]), "+v"(ak[2]), "+v"(ak[3]), "+v"(ak[4]));
    asm volatile("" : "+v"(ak[5]), "+v"(ak[6]), "+v"(ak[7]), "+v"(ak[8]),
                      "+v"(av[0]), "+v"(av[1]), "+v"(av[2]), "+v"(av[3]), "+v"(av[4]), "+v"(av[5]), "+v"(av[6]), "+v"(av[7]), "+v"(av[8]));
}
struct Frag2 { bf16x8 x[9], w[3]; };
__device__ __forceinline__ void t2_read(Frag2& f, LAS char* px, LAS char* pw) {
    f.w[1] = lds_tr2(pw + 1024);
    f.x[0] = lds_b128(px);
    f.w[2] = lds_tr2(pw + 2048);
    f.w[0] = lds_tr2(pw);
#pragma unroll
    for (int rb = 1; rb < 9; ++rb) f.x[rb] = lds_b128(px + rb * 2048);
}
__device__ __forceinline__ void t2_mma(f32x4 (&aq)[8], f32x4 (&ak)[9], f32x4 (&av)[9], const Frag2& f) {
#pragma unroll
    for (int rb = 0; rb < 9; ++rb) {
        ak[rb] = mfma32(f.w[1], f.x[rb], ak[rb]);
        av[rb] = mfma32(f.x[rb], f.w[2], av[rb]);
        if (rb > 0) aq[rb - 1] = mfma32(f.w[0], f.x[rb], aq[rb - 1]);
    }
}
#define SGB2() do { _Pragma("unroll") for (int _i = 0; _i < 15; ++_i) { __builtin_amdgcn_sched_group_barrier(0x8, 1, 0); __builtin_amdgcn_sched_group_barrier(0x100, 1, 0); } \
    _Pragma("unroll") for (int _i = 0; _i < 2; ++_i) { __builtin_amdgcn_sched_group_barrier(0x8, 1, 0); __builtin_amdgcn_sched_group_barrier(0x4, 2, 0); } \
    _Pragma("unroll") for (int _i = 0; _i < 2; ++_i) { __builtin_amdgcn_sched_group_barrier(0x8, 1, 0); __builtin_amdgcn_sched_group_barrier(0x2, 1, 0); } \
    __builtin_amdgcn_sched_group_barrier(0x8, 7, 0); } while (0)

struct Frag4 { bf16x8 w[4][2], o[4][2]; };
#define SGB4() do { _Pragma("unroll") for (int _i = 0; _i < 12; ++_i) { __builtin_amdgcn_sched_group_barrier(0x100, 2, 0); __builtin_amdgcn_sched_group_barrier(0x8, 1, 0); } __builtin_amdgcn_sched_group_barrier(0x8, 4, 0); } while (0)
__device__ __forceinline__ void pf(rsrc_t r, int voff, int soff) {
    asm volatile("s_nop 4\n\tbuffer_load_dword v255, %0, %1, %2 offen" ::"v"(voff), "s"(r), "s"(__builtin_amdgcn_readfirstlane(soff)) : "memory", "v255");
}

__global__ __launch_bounds__(512, 2) void fused(const __bf16* __restrict__ xa, const __bf16* __restrict__ cre, const __bf16* __restrict__ cim,
                                                const __bf16* __restrict__ hre, const __bf16* __restrict__ him, const __bf16* __restrict__ xb,
                                                const __bf16* __restrict__ wqkv, const __bf16* __restrict__ wo,
                                                char* __restrict__ ws, __bf16* __restrict__ out) {
    __shared__ __attribute__((aligned(1024))) char smem_[163840];
    LAS char* smem = (LAS char*)smem_;
    asm volatile("s_load_dword s98, %0, 0x0\n\ts_load_dword s99, %0, 0x40" ::"s"(__builtin_amdgcn_kernarg_segment_ptr()) : "s98", "s99");
    if ((blockIdx.x >> 3) == 4) {
        unsigned long long pc; int len;
        asm volatile("s_getpc_b64 %0\n.Lfnet_pc%=:\n\ts_mov_b32 %1, fnet_code_end-.Lfnet_pc%=-4" : "=s"(pc), "=s"(len));
        const int o = threadIdx.x * 128;
        if (o <= len) { unsigned v = *(const GAS unsigned*)(pc + (unsigned)o); asm volatile("" ::"v"(v)); }
    }
    const int tid = threadIdx.x, lane = tid & 63;
    const int wave = __builtin_amdgcn_readfirstlane(tid >> 6);
    const int bid = blockIdx.x, xcd = bid & 7, idx = bid >> 3;
    const int q = xcd & 3, th = xcd >> 2;
    const int ttl = idx >> 2, et = idx & 3, tt = th * 8 + ttl;
    const int t0 = tt * 128, e0 = et * 128;
    const bool typeA = (ttl & 1) == 0;
    const int s1 = typeA ? ((q & 1) * 8 + th * 4 + (ttl >> 1)) * 128 : ((xcd >> 1) * 4 + (ttl >> 1)) * 128;
    const int hg = (xcd & 1) * 4 + et;
    const int s4 = (xcd * 4 + (idx >> 3)) * 64, n4 = (idx & 7) * 64;
    GAS unsigned* flags = (GAS unsigned*)(ws + WS_FLAGS);
    const int slot3 = typeA ? 3 : 0;
    const int rot1 = 4 * (((ttl >> 1) + et) & 1), rot3 = 4 * et;

    if (__builtin_expect(wave >= 6, 1)) {
        const int lw = wave - 6;
        const int row = 8 * lw + (lane >> 3);
        const int ch = ((lane & 7) ^ (4 * lw + (lane >> 4))) * 16;
        const int cht = ((lane & 7) ^ (2 * (((lane >> 4) & 1) | (lw << 1)))) * 16;
        const bool master = (idx == MIDX && lw == 0);
        if (master) {
#pragma unroll
            for (int i = 0; i < NCNT; i += 64)
                if (i + lane < NCNT) __hip_atomic_store(flags + (8 + xcd * NCNT + i + lane) * FW, 0u, __ATOMIC_RELAXED, __HIP_MEMORY_SCOPE_AGENT);
        }
        rsrc_t rc = mk_rsrc((q >> 1) ? cim : cre, SEQ * SEQ * 2);
        const int vb = row * 4096 + ch;
        const int sb = t0 * 4096 + (q & 1) * 2048;
        auto issueC = [&](int st, int slot) {
            const int so = sb + ((st + rot3) & 15) * 128;
#pragma unroll
            for (int u = 0; u < 8; ++u) glds<0>(rc, smem + slot * SLOT + 16384 + (lw + 2 * u) * 1024, vb, so + 16 * ((u & 4) | ((u + q) & 3)) * 4096);
        };
        rsrc_t rw = mk_rsrc(wo, DM * DM * 2);
        int slot = 0;
        if (typeA) {
            rsrc_t rh = mk_rsrc((q >> 1) ? him : hre, DM * DM * 2);
            const int vh = row * 1024 + ch;
#pragma unroll 1
            for (int st = 0; st < 8; ++st) {
                if (st >= 2) bar_vm<8>();
                const int so = e0 * 1024 + ((st + rot1) & 7) * 128;
#pragma unroll
                for (int u = 0; u < 8; ++u) glds<0>(rh, smem + slot * SLOT + 16384 + (lw + 2 * u) * 1024, vh, so + 16 * u * 1024);
                slot = (slot + 1 == 5) ? 0 : slot + 1;
            }
        } else {
            rsrc_t rq = mk_rsrc(wqkv, DM * 3 * DM * 2);
            const int vq = row * 3072 + cht;
            int pos = 1;
#pragma unroll 1
            for (int st = 0; st < 8; ++st) {
                if (st >= 2) bar_vm<12>();
                if (master && st >= 2 && st < 7) hb_bump(flags, xcd, lane);
                LAS char* sl = smem + T2_BASE + pos * T2_SLOT;
                const int so = ((st + rot1) & 7) * 64 * 3072 + hg * 128;
#pragma unroll
                for (int v = 0; v < 4; ++v)
#pragma unroll
                    for (int cg = 0; cg < 3; ++cg)
                        glds<0>(rq, sl + 18432 + ((lw + 2 * v) * 3 + cg) * 1024, vq, so + 16 * v * 3072 + cg * 1024);
                pos = (pos + 1 == 3) ? 0 : pos + 1;
            }
        }
#pragma unroll 1
        for (int st = 0; st < 16; ++st) {
            if (typeA) {
                if (st == 2) { bar_only(); bar_only(); }
                bar_vm<8>();
            } else if (st < 5) {
                if (st == 1) bar_vm<20>();
                if (st == 2) bar_vm<16>();
                if (st == 3) { bar_only(); if (master) hb_bump(flags, xcd, lane); }
            } else {
                if (st == 5) { bar_vm<32>(); bar_vm<24>(); bar_vm<16>(); }
                bar_vm<8>();
            }
            issueC(st, slot);
            slot = (slot + 1 == 5) ? 0 : slot + 1;
            if (st == 7) pf(rw, lane * 128, idx * 16384 + lw * 8192);
        }
        const int vw = row * 1024 + cht;
#pragma unroll 1
        for (int st = 0; st < 4; ++st) {
            if (st < 2) bar_vm<8>();
            if (st == 2) bar_vm<0>();
#pragma unroll
            for (int h = 0; h < 2; ++h)
#pragma unroll
                for (int u = 0; u < 4; ++u)
                    glds<0>(rw, smem + slot * SLOT + h * 16384 + 8192 + (lw + 2 * u) * 1024, vw, ((st * 2 + h) * 64 + 16 * u) * 1024 + n4 * 2);
            slot = (slot + 1 == 5) ? 0 : slot + 1;
        }
        bar_vm<0>();
    } else if (__builtin_expect(wave >= 4, 1)) {
        const int lw = wave - 4;
        const int ch = ((lane & 7) ^ (4 * lw + (lane >> 4))) * 16;
        const int prow = 8 * (lane >> 5) + ((lane >> 3) & 3) + 16 * lw;
        const int row = 8 * lw + (lane >> 3);
        GAS unsigned* hbp = flags + xcd * FW + (lane & 7);
        if (!typeA) {
            rsrc_t rx = mk_rsrc(xb, SEQ * DM * 2);
            const int vx = row * 1024 + ch;
            const int sxm = s1 * 1024, sxh = (s1 >= 16 ? s1 - 16 : 0) * 1024;
            int pos = 1;
#pragma unroll 1
            for (int st = 0; st < 8; ++st) {
                if (st >= 2) bar_vm<9>();
                if (st == 0 && lw == 0) LD_FLAG_ASYNC("v254", hbp);
                if (st == 7 && lw == 0) LD_FLAG_ASYNC("v253", hbp);
                LAS char* sl = smem + T2_BASE + pos * T2_SLOT;
                const int ko = ((st + rot1) & 7) * 128;
                glds<0>(rx, sl + lw * 1024, vx, sxh + ko);
#pragma unroll
                for (int u = 0; u < 8; ++u) glds<0>(rx, sl + 2048 + (lw + 2 * u) * 1024, vx, sxm + 16 * u * 1024 + ko);
                pos = (pos + 1 == 3) ? 0 : pos + 1;
            }
            bar_vm<9>();
        }
        rsrc_t ro = mk_rsrc(ws + WS_O, SEQ * DM * 2);
        const int vo = row * 32 + (ch & 16) + (ch >> 5) * 65536;
        GAS unsigned* f2 = flags + (F2_IDX + (s4 >> 7)) * FW;
        int slot = 0;
#pragma unroll 1
        for (int pass = typeA ? 0 : 1; pass < 2; ++pass) {
            if (pass) {
                bar_vm<0>();
                if (lw == 0) {
                    unsigned a, b, spins = 0;
                    RD_VEC("v254", a); RD_VEC("v253", b);
                    bool moved = (a != b) || (idx == MIDX && (lane & 7) == xcd);
                    while (!__all(moved)) {
                        if (idx == MIDX) hb_bump(flags, xcd, lane);
                        __builtin_amdgcn_s_sleep(1);
                        b = __hip_atomic_load(hbp, __ATOMIC_RELAXED, __HIP_MEMORY_SCOPE_AGENT);
                        moved = moved || (a != b);
                        if (++spins > SPIN_CAP) break;
                    }
                }
                bar_only();
                if (typeA) {
                    bar_only();
                    if (lw == 0 && lane < R1) (void)__hip_atomic_fetch_add(flags + (F1_IDX + (q * 4 + et) * R1 + lane) * FW, 1u, __ATOMIC_RELAXED, __HIP_MEMORY_SCOPE_AGENT);
                }
                wait_ge(flags + (F1_IDX + (q * 4 + et) * R1 + ((lw + 2 * th + 4 * (ttl >> 1) + 16 * (ttl & 1)) & (R1 - 1))) * FW, 8u, flags, lane);
            }
            rsrc_t ra = mk_rsrc(pass ? (const void*)(ws + WS_ABT) : (const void*)xa, pass ? 1024 * SEQ * 2 : SEQ * DM * 2);
            const int va = pass ? prow * 64 + (ch & 48) + (ch >> 6) * 8192 : prow * 1024 + ch;
            const int sa = pass ? (((q >> 1) * 4 + et) * 64 + (q & 1) * 32) * 8192 : s1 * 1024;
            const int rot = pass ? rot3 : rot1, nst = pass ? 16 : 8, ksh = pass ? 14 : 7, rb = pass ? 64 : 1024;
#pragma unroll 1
            for (int st = 0; st < nst; ++st) {
                if (st >= 2) bar_vm<8>();
                if (!pass) {
                    if (st == 0 && lw == 0) LD_FLAG_ASYNC("v254", hbp);
                    if (st == 7 && lw == 0) LD_FLAG_ASYNC("v253", hbp);
                } else if (st == 3 && lw == 1 && !typeA) add_flag_async(flags + (F2_IDX + (s1 >> 7)) * FW, lane);
                const int so = sa + (((st + rot) & (nst - 1)) << ksh);
#pragma unroll
                for (int u = 0; u < 8; ++u) glds<SC1>(ra, smem + slot * SLOT + (lw + 2 * u) * 1024, va, so + (32 * (u >> 1) + 4 * (u & 1)) * rb);
                slot = (slot + 1 == 5) ? 0 : slot + 1;
                if (st == 13) LD_FLAG_ASYNC("v252", f2);
            }
            if (!pass) bar_vm<8>();
        }
#pragma unroll 1
        for (int st = 0; st < 4; ++st) {
            if (st < 2) bar_vm<8>();
            if (st == 2) bar_vm<0>();
            if (st == 0) {
                unsigned f2v;
                RD_FLAG("v252", f2v);
                if (f2v < 8u) wait_ge(f2, 8u, flags, lane);
            }
#pragma unroll
            for (int h = 0; h < 2; ++h)
#pragma unroll
                for (int u = 0; u < 4; ++u)
                    glds<SC1>(ro, smem + slot * SLOT + h * 16384 + (lw + 2 * u) * 1024, vo, (s4 + 16 * u) * 32 + (st * 2 + h) * 4 * 65536);
            slot = (slot + 1 == 5) ? 0 : slot + 1;
        }
        bar_vm<0>();
    } else {
        const int r = lane & 15, g = lane >> 4;
        const int tile = tt * 4 + et;
        rsrc_t rp = mk_rsrc(ws + WS_PART, 64 * 16 * 4 * 2048);
        const int inb = (tile * 4 + q) * 32768 + wave * 2048 + lane * 16;
        if (typeA) {
            f32x4 acc[4][4] = {};
            nt_pin(acc);
            bar_lgkm();
            nt_consume<true>(acc, smem, 0, 8, wave, lane);
            const unsigned sg = (q >> 1) ? 0x80008000u : 0u;
            rsrc_t rs = mk_rsrc(ws + WS_ABT, 1024 * SEQ * 2);
            const int vs = r * 64 + g * 16;
            const int ss = (((q >> 1) * 4 + et) * 64 + (s1 >> 5) + (wave & 1) * 2) * 8192 + (wave >> 1) * 4096;
#pragma unroll
            for (int n = 0; n < 4; ++n)
#pragma unroll
                for (int u = 0; u < 2; ++u)
                    __builtin_amdgcn_raw_buffer_store_b128(pack8(acc[2 * u][n], acc[2 * u + 1][n]) ^ sg, rs, vs + ss + n * 1024 + u * 8192, 0, SC1);
            bar_vm<0>();
        } else {
            const int c0 = ((g ^ (r >> 1)) << 4);
            const int ox = r * 128;
            const int qq = (lane & 15) >> 2, p = lane & 3;
            const int ow = 18432 + g * 3072 + qq * 128 + (((2 * wave + (p >> 1)) ^ (2 * ((qq >> 1) | ((g & 1) << 1)))) << 4) + 8 * (p & 1);
            f32x4 aq[8] = {}, ak[9] = {}, av[9] = {};
            Frag2 f0, f1;
            int so = T2_BASE + T2_SLOT;
            t2_pin(aq, ak, av);
            bar_lgkm();
            t2_read(f0, smem + so + ox + c0, smem + so + ow);
            int px = so + ox + (c0 ^ 64), pw = so + ow;
#pragma unroll 1
            for (int it = 0; it < 7; ++it) {
                __builtin_amdgcn_sched_barrier(0);
                t2_read(f1, smem + px, smem + pw + 12288);
                t2_mma(aq, ak, av, f0);
                so = (so == T2_BASE + 2 * T2_SLOT) ? T2_BASE : so + T2_SLOT;
                int qx = so + ox + c0, qw = so + ow;
                SGB2();
                __builtin_amdgcn_sched_barrier(0);
                asm volatile("" : "+v"(qx), "+v"(qw));
                t2_pin(aq, ak, av);
                bar_cons();
                __builtin_amdgcn_sched_barrier(0);
                t2_read(f0, smem + qx, smem + qw);
                t2_mma(aq, ak, av, f1);
                px = qx ^ 64; pw = qw;
                SGB2();
                __builtin_amdgcn_sched_barrier(0);
                asm volatile("" : "+v"(px));
            }
            t2_read(f1, smem + px, smem + pw + 12288);
            t2_mma(aq, ak, av, f0);
            SGB2();
            __builtin_amdgcn_sched_barrier(0);
            t2_pin(aq, ak, av);
            bar_cons();
            __builtin_amdgcn_sched_barrier(0);
            t2_mma(aq, ak, av, f1);
            const int j = r, par = j & 1;
            const bool uc0 = (4 * g + par) <= j, uc1 = (4 * g + par + 2) <= j;
            const unsigned s0v = par ? 0x01000c0cu : 0x0c0c0100u, s1v = par ? 0x03020c0cu : 0x0c0c0302u, zs = 0x0c0c0c0cu;
            const unsigned selc0 = uc0 ? s0v : zs, selp0 = uc0 ? zs : s0v, selc1 = uc1 ? s1v : zs, selp1 = uc1 ? zs : s1v;
            rsrc_t rs = mk_rsrc(ws + WS_O, SEQ * DM * 2);
            const int vs = j * 32 + g * 8;
            const int ss = (hg * 4 + wave) * 65536 + s1 * 32;
            const f32x4 z = {0.f, 0.f, 0.f, 0.f};
            const float C = 0.25f * 1.44269504088896f;
            u32x2 kb[9], vb[9];
            float l0[8], l1[8], m[8];
#pragma unroll
            for (int i = 0; i < 9; ++i) kb[i] = cvt4u(ak[i]);
#pragma unroll
            for (int b = 0; b < 8; ++b) {
                u32x2 qb = cvt4u(aq[b]);
                f32x4 sc = mfma16(kb[b + 1], qb, z), sp = mfma16(kb[b], qb, z);
                float c0v = par ? sc[1] : sc[0], c1v = par ? sc[3] : sc[2];
                float p0v = par ? sp[1] : sp[0], p1v = par ? sp[3] : sp[2];
                if (b == 0 && s1 == 0) { p0v = -4e9f; p1v = -4e9f; }
                unsigned pk = cvtpk(uc0 ? c0v : p0v, uc1 ? c1v : p1v);
                l0[b] = __uint_as_float(pk << 16); l1[b] = __uint_as_float(pk & 0xffff0000u);
                m[b] = fmx(l0[b], l1[b]);
            }
            allred4<true>(m); allred4<true>(m + 4);
#pragma unroll
            for (int i = 0; i < 9; ++i) vb[i] = cvt4u(av[i]);
#pragma unroll
            for (int b = 0; b < 8; ++b) {
                l0[b] = __builtin_amdgcn_exp2f((l0[b] - m[b]) * C); l1[b] = __builtin_amdgcn_exp2f((l1[b] - m[b]) * C);
                m[b] = l0[b] + l1[b];
            }
            allred4<false>(m); allred4<false>(m + 4);
            f32x4 ov[8];
#pragma unroll
            for (int b = 0; b < 8; ++b) {
                unsigned P = cvtpk(l0[b] * m[b], l1[b] * m[b]);
                u32x2 pc = {__builtin_amdgcn_perm(0u, P, selc0), __builtin_amdgcn_perm(0u, P, selc1)};
                u32x2 pp = {__builtin_amdgcn_perm(0u, P, selp0), __builtin_amdgcn_perm(0u, P, selp1)};
                ov[b] = mfma16(vb[b], pp, mfma16(vb[b + 1], pc, z));
            }
#pragma unroll
            for (int b = 0; b < 8; ++b) __builtin_amdgcn_raw_buffer_store_b64(cvt4u(ov[b]), rs, vs, ss + b * 512, SC1);
        }
        f32x4 acc[4][4] = {};
        nt_pin(acc);
        bar_lgkm();
        int slot = nt_consume<false>(acc, smem, slot3, 16, wave, lane);
        u32x4 sv[3][2];
#pragma unroll
        for (int n = 1; n < 4; ++n) { sv[n - 1][0] = pack8(acc[0][n], acc[1][n]); sv[n - 1][1] = pack8(acc[2][n], acc[3][n]); }
        auto send = [&](int n) {
            const int base = (((tile * 4 + ((q + n) & 3)) * 4 + q) * 4 + wave) * 2048 + lane * 16;
            __builtin_amdgcn_raw_buffer_store_b128(sv[n - 1][0], rp, base, 0, SC1);
            __builtin_amdgcn_raw_buffer_store_b128(sv[n - 1][1], rp, base + 1024, 0, SC1);
        };
        send(1); send(2); send(3);
        f32x4 own[4];
#pragma unroll
        for (int m = 0; m < 4; ++m) own[m] = acc[m][0];
        f32x4 a4[2][2] = {};
        rsrc_t rout = mk_rsrc(out, SEQ * 1024 * 2);
        u32x4 pv[3][2];
        {
            const int c0 = ((g ^ (r >> 1)) << 4);
            const int oo = ((wave >> 1) * 32 + r) * 128;
            const int qq = (lane & 15) >> 2, p = lane & 3;
            const int swz = 2 * ((qq >> 1) | ((g & 1) << 1));
            const int ow0 = 8192 + g * 1024 + qq * 128 + ((((wave & 1) * 4 + (p >> 1)) ^ swz) << 4) + 8 * (p & 1);
            const int ow1 = ow0 ^ 32;
            Frag4 h0, h1;
            auto rd = [&](Frag4& f) {
                LAS char* sl = smem + slot * SLOT;
#pragma unroll
                for (int i = 0; i < 4; ++i) {
                    const int ho = (i >> 1) * 16384, kk = i & 1;
                    f.w[i][0] = lds_tr2(sl + ho + ow0 + kk * 4096);
                    f.o[i][0] = lds_b128(sl + ho + oo + (c0 ^ (kk * 64)));
                    f.o[i][1] = lds_b128(sl + ho + oo + (c0 ^ (kk * 64)) + 2048);
                    f.w[i][1] = lds_tr2(sl + ho + ow1 + kk * 4096);
                }
                slot = (slot + 1 == 5) ? 0 : slot + 1;
            };
            auto mm = [&](const Frag4& f) {
#pragma unroll
                for (int i = 0; i < 4; ++i) {
                    a4[0][0] = mfma32(f.w[i][0], f.o[i][0], a4[0][0]);
                    a4[0][1] = mfma32(f.w[i][0], f.o[i][1], a4[0][1]);
                    a4[1][0] = mfma32(f.w[i][1], f.o[i][0], a4[1][0]);
                    a4[1][1] = mfma32(f.w[i][1], f.o[i][1], a4[1][1]);
                }
                asm volatile("" : "+v"(a4[0][0]), "+v"(a4[0][1]), "+v"(a4[1][0]), "+v"(a4[1][1]));
            };
            asm volatile("" : "+v"(own[0]), "+v"(own[1]), "+v"(own[2]), "+v"(own[3]));
            bar_only();
            __builtin_amdgcn_sched_barrier(0);
            rd(h0);
            __builtin_amdgcn_sched_barrier(0);
            rd(h1); mm(h0); SGB4();
            __builtin_amdgcn_sched_barrier(0);
            drain_vm();
            if (lane < 4 && lane != q)
                (void)__hip_atomic_fetch_add(flags + (F3_IDX + tile * 4 + lane) * FW, 1u, __ATOMIC_RELAXED, __HIP_MEMORY_SCOPE_AGENT);
            mm(h1);
            wait_ge(flags + (F3_IDX + tile * 4 + q) * FW, 12u, flags, lane);
#pragma unroll
            for (int k = 0; k < 3; ++k) {
                const int o = inb + ((q + 1 + k) & 3) * 8192;
                pv[k][0] = __builtin_amdgcn_raw_buffer_load_b128(rp, o, 0, SC1);
                pv[k][1] = __builtin_amdgcn_raw_buffer_load_b128(rp, o + 1024, 0, SC1);
            }
            bar_only();
            __builtin_amdgcn_sched_barrier(0);
            rd(h0);
            __builtin_amdgcn_sched_barrier(0);
            rd(h1); mm(h0); SGB4();
            __builtin_amdgcn_sched_barrier(0);
            mm(h1);
#pragma unroll
            for (int n = 0; n < 2; ++n) {
                u32x2 lo = cvt4u(a4[0][n]), hi = cvt4u(a4[1][n]);
                auto r0 = __builtin_amdgcn_permlane16_swap(lo[0], hi[0], false, false);
                auto r1 = __builtin_amdgcn_permlane16_swap(lo[1], hi[1], false, false);
                u32x4 v = {r0[0], r1[0], r0[1], r1[1]};
                __builtin_amdgcn_raw_buffer_store_b128(v, rout, ((s4 + (wave >> 1) * 32 + n * 16 + r) * 1024 + 512 + n4 + (wave & 1) * 32 + (g & 1) * 16 + (g >> 1) * 8) * 2, 0, SC1);
            }
        }
#pragma unroll
        for (int k = 0; k < 3; ++k) {
            unpack8_add(pv[k][0], own[0], own[1]);
            unpack8_add(pv[k][1], own[2], own[3]);
        }
        const int trow = t0 + (wave >> 1) * 64 + q * 16 + r;
#pragma unroll
        for (int u = 0; u < 2; ++u)
            __builtin_amdgcn_raw_buffer_store_b128(pack8(own[2 * u], own[2 * u + 1]), rout, (trow * 1024 + e0 + (wave & 1) * 64 + 32 * u + 8 * g) * 2, 0, SC1);
    }
}

extern "C" __global__ void fnet_code_end(unsigned* p) { p[0] = 0; }

extern "C" void kernel_launch(void* const* d_in, const int* in_sizes, int n_in,
                              void* d_out, int out_size, void* d_ws, size_t ws_size,
                              hipStream_t stream) {
    fused<<<256, 512, 0, stream>>>((const __bf16*)d_in[0], (const __bf16*)d_in[1], (const __bf16*)d_in[2], (const __bf16*)d_in[3],
                                   (const __bf16*)d_in[4], (const __bf16*)d_in[5], (const __bf16*)d_in[6], (const __bf16*)d_in[7],
                                   (char*)d_ws, (__bf16*)d_out);
}
```
